```python
import torch

def _softmax(x, axis=-1):
    return torch.softmax(x, dim=axis)
def _gelu(x):
    return torch.nn.functional.gelu(x, approximate="tanh")
def _silu(x):
    return torch.nn.functional.silu(x)
def _sigmoid(x):
    return torch.sigmoid(x)
def _layernorm(x, axis=-1, eps=1e-5):
    m = x.mean(dim=axis, keepdim=True)
    v = x.var(dim=axis, keepdim=True, unbiased=False)
    return (x - m) / torch.sqrt(v + eps)
def _scatter_add(tgt, idx, src, axis=0):
    return tgt.clone().index_add_(axis, idx, src)
def _segment_sum(src, seg, n):
    out = torch.zeros((n,) + src.shape[1:], dtype=src.dtype)
    return out.index_add_(0, seg, src)
def _pad(x, pw):
    flat = tuple(v for pair in reversed(pw) for v in pair)
    return torch.nn.functional.pad(x, flat)

kernel_name = 'sequential_preLN__mha_block__glu_gated_glu__prompt'

def setup_inputs():

    _w = (lambda a: (lambda t: t.to(torch.bfloat16) if t.is_floating_point() else t)(torch.as_tensor(a)))
    def _A_setup():
        B, S, D, H = 1, 2048, 1024, 32
        return {
            "x": _w((lambda *s: torch.randn(*s, dtype=torch.bfloat16) * 0.05)(B, S, D)),
            "wqkv": _w((lambda *s: torch.randn(*s, dtype=torch.bfloat16) * 0.05)(D, 3 * D)),
            "wo": _w((lambda *s: torch.randn(*s, dtype=torch.bfloat16) * 0.05)(D, D)),
        }
    _A_in = _A_setup()

    def _B_setup():
        B, S, D, F = 1, 2048, 1024, 3584
        return {
            "x": _w((lambda *s: torch.randn(*s, dtype=torch.bfloat16) * 0.05)(B, S, D)),
            "w_gate": _w((lambda *s: torch.randn(*s, dtype=torch.bfloat16) * 0.05)(D, F)),
            "w_up": _w((lambda *s: torch.randn(*s, dtype=torch.bfloat16) * 0.05)(D, F)),
            "w_down": _w((lambda *s: torch.randn(*s, dtype=torch.bfloat16) * 0.05)(F, D)),
        }
    _B_in = _B_setup()

    return {
        "A_x": _A_in["x"],
        "A_wqkv": _A_in["wqkv"],
        "A_wo": _A_in["wo"],
        "B_x": _B_in["x"],
        "B_w_gate": _B_in["w_gate"],
        "B_w_up": _B_in["w_up"],
        "B_w_down": _B_in["w_down"],
    }

def reference(A_x, A_wqkv, A_wo, B_x, B_w_gate, B_w_up, B_w_down):
    def _A_ref(x, wqkv, wo):
        B, S, D = x.shape
        H = 32
        Dh = D // H
        qkv = (lambda x, *d: x.permute(*d))((x @ wqkv).reshape(B, S, 3, H, Dh), 2, 0, 3, 1, 4)
        q, k, v = qkv[0], qkv[1], qkv[2]
        scores = torch.einsum("bhqd,bhkd->bhqk", q, k) * (Dh ** -0.5)
        p = _softmax(scores, axis=-1)
        o = torch.einsum("bhqk,bhkd->bhqd", p, v)
        return (lambda x, *d: x.permute(*d))(o, 0, 2, 1, 3).reshape(B, S, D) @ wo

    def _B_ref(x, w_gate, w_up, w_down):
        g = torch.einsum("bsd,df->bsf", x, w_gate)
        u = torch.einsum("bsd,df->bsf", x, w_up)
        h = _sigmoid(g) * u
        return torch.einsum("bsf,fd->bsd", h, w_down)

    x = A_x
    h = x + _A_ref(_layernorm(x, axis=-1), A_wqkv, A_wo)
    h2 = h + _B_ref(_layernorm(h, axis=-1), B_w_gate, B_w_up, B_w_down)
    return h2
```

```cpp
#include <hip/hip_runtime.h>
#include <hip/hip_bf16.h>
#include <stdint.h>

#define LAS __attribute__((address_space(3)))
typedef unsigned short u16;
typedef short bf16x8 __attribute__((ext_vector_type(8)));
typedef short s16x4 __attribute__((ext_vector_type(4)));
typedef float f32x2 __attribute__((ext_vector_type(2)));
typedef float f32x4 __attribute__((ext_vector_type(4)));
typedef float f32x16 __attribute__((ext_vector_type(16)));
typedef unsigned u32x2 __attribute__((ext_vector_type(2)));
typedef unsigned u32x4 __attribute__((ext_vector_type(4)));
typedef __bf16 bf16x2_t __attribute__((ext_vector_type(2)));
typedef __amdgpu_buffer_rsrc_t srd_t;

constexpr int SEQ = 2048, DM = 1024, NH = 32, FF = 3584;
constexpr float LN_EPS = 1e-5f;
constexpr float QSCALE = 0.17677669529663687f * 1.4426950408889634f;
constexpr float LOG2E = 1.4426950408889634f;

constexpr size_t WS_XN = 0;
constexpr size_t WS_Q = WS_XN + (4u << 20);
constexpr size_t WS_K = WS_Q + (4u << 20);
constexpr size_t WS_V = WS_K + (4u << 20);
constexpr size_t WS_O = WS_V + (4u << 20);
constexpr size_t WS_H = WS_O + (4u << 20);
constexpr size_t WS_ACT = WS_H + (4u << 20);
constexpr size_t WS_ST = WS_ACT + (size_t)SEQ * FF * 2;
constexpr size_t WS_CS = WS_ST + (size_t)SEQ * 16 * 8;
constexpr size_t WS_END = WS_CS + 4 * 7168 * 4;

__device__ __forceinline__ unsigned pk_bf16(float lo, float hi) { f32x2 v = {lo, hi}; bf16x2_t b = __builtin_convertvector(v, bf16x2_t); return __builtin_bit_cast(unsigned, b); }
__device__ __forceinline__ float bf_lo(unsigned u) { return __uint_as_float(u << 16); }
__device__ __forceinline__ float bf_hi(unsigned u) { return __uint_as_float(u & 0xffff0000u); }
__device__ __forceinline__ s16x4 trd(LAS const char* p) { return __builtin_bit_cast(s16x4, __builtin_amdgcn_ds_read_tr16_b64_v4i16((LAS s16x4*)p)); }
__device__ __forceinline__ bf16x8 cat8(s16x4 a, s16x4 b) { return (bf16x8){a[0], a[1], a[2], a[3], b[0], b[1], b[2], b[3]}; }
__device__ __forceinline__ bf16x8 ldf(LAS const char* p) { return *(LAS const bf16x8*)p; }
__device__ __forceinline__ srd_t mk_srd(const void* p, unsigned bytes) { return __builtin_amdgcn_make_buffer_rsrc((void*)p, (short)0, (int)bytes, 0x00020000); }
__device__ __forceinline__ void dma16(unsigned ldsa, unsigned voff, srd_t srd, unsigned soff) {
    asm volatile("s_mov_b32 m0, %0\n\ts_nop 0\n\tbuffer_load_dwordx4 %1, %2, %3 offen lds" :: "s"(ldsa), "v"(voff), "s"(srd), "s"(soff));
}
#define MFMA16(a, b, c) __builtin_amdgcn_mfma_f32_16x16x32_bf16(a, b, c, 0, 0, 0)
#define MFMA32(a, b, c) __builtin_amdgcn_mfma_f32_32x32x16_bf16(a, b, c, 0, 0, 0)
#define WAITVL(n) asm volatile("s_waitcnt vmcnt(%0) lgkmcnt(0)" :: "n"(n) : "memory")
#define WAITBAR(n) asm volatile("s_waitcnt vmcnt(%0) lgkmcnt(0)\n\ts_barrier" :: "n"(n) : "memory")
#define SCHED0() __builtin_amdgcn_sched_barrier(0)

__global__ __launch_bounds__(512) void k0_ln(const u16* __restrict__ x, char* __restrict__ ws) {
    __shared__ float red[2][8][16];
    const int tid = threadIdx.x, lane = tid & 63, w = tid >> 6, g = lane >> 4, r = lane & 15;
    const int mt = blockIdx.x;
    const u16* xr = x + (size_t)(mt * 16 + r) * DM + w * 128 + g * 4;
    u32x2 v[8];
#pragma unroll
    for (int i = 0; i < 8; ++i) v[i] = *(const u32x2*)(xr + i * 16);
    float s = 0.f;
#pragma unroll
    for (int i = 0; i < 8; ++i) s += (bf_lo(v[i].x) + bf_hi(v[i].x)) + (bf_lo(v[i].y) + bf_hi(v[i].y));
    s += __shfl_xor(s, 16); s += __shfl_xor(s, 32);
    if (g == 0) red[0][w][r] = s;
    __syncthreads();
    float tot = 0.f;
#pragma unroll
    for (int i = 0; i < 8; ++i) tot += red[0][i][r];
    const float mean = tot * (1.0f / DM);
    float q = 0.f;
#pragma unroll
    for (int i = 0; i < 8; ++i) { float a = bf_lo(v[i].x) - mean, b = bf_hi(v[i].x) - mean, c = bf_lo(v[i].y) - mean, d = bf_hi(v[i].y) - mean; q += (a * a + b * b) + (c * c + d * d); }
    q += __shfl_xor(q, 16); q += __shfl_xor(q, 32);
    if (g == 0) red[1][w][r] = q;
    __syncthreads();
    float qt = 0.f;
#pragma unroll
    for (int i = 0; i < 8; ++i) qt += red[1][i][r];
    const float rstd = 1.0f / sqrtf(qt * (1.0f / DM) + LN_EPS);
    char* xn = ws + WS_XN + ((size_t)mt * 32 + w * 4) * 1024 + lane * 16;
#pragma unroll
    for (int i = 0; i < 4; ++i) {
        const u32x2 a = v[2 * i], b = v[2 * i + 1];
        u32x4 o;
        o.x = pk_bf16((bf_lo(a.x) - mean) * rstd, (bf_hi(a.x) - mean) * rstd); o.y = pk_bf16((bf_lo(a.y) - mean) * rstd, (bf_hi(a.y) - mean) * rstd);
        o.z = pk_bf16((bf_lo(b.x) - mean) * rstd, (bf_hi(b.x) - mean) * rstd); o.w = pk_bf16((bf_lo(b.y) - mean) * rstd, (bf_hi(b.y) - mean) * rstd);
        *(u32x4*)(xn + i * 1024) = o;
    }
}

constexpr int K1_SLOT = 40960, K1_LDS = 3 * K1_SLOT;
__global__ __launch_bounds__(512, 2) void k1_qkv(const u16* __restrict__ wqkv, char* __restrict__ ws) {
    extern __shared__ __attribute__((aligned(16))) char smem_g[];
    LAS char* lds = (LAS char*)smem_g;
    const unsigned lds0 = (unsigned)(uintptr_t)lds;
    const int tid = threadIdx.x, lane = tid & 63, w = __builtin_amdgcn_readfirstlane(tid >> 6), wr = w >> 2, wc = w & 3, g = lane >> 4, r = lane & 15;
    const int xcd = blockIdx.x & 7, loc = blockIdx.x >> 3;
    const int p = xcd * 2 + (loc & 1), rt = loc >> 1;
    const srd_t srx = mk_srd(ws + WS_XN, SEQ * DM * 2), srw = mk_srd(wqkv, DM * 3 * DM * 2);
    const unsigned xv = lane * 16;
    const unsigned xs0 = (unsigned)(rt * 8 + w) * 32768u;
    const int wrow = lane >> 3, wch = (lane & 7) ^ (((wrow >> 1) & 3) << 1);
    const unsigned wv = (unsigned)(w * 8 + wrow) * 6144u + wch * 16;
    const unsigned ws0 = (unsigned)p * 128u;
    const unsigned ld0 = lds0 + w * 2048, ld1 = lds0 + 16384 + w * 1024;
#define K1_DMA(i, t, slot) do { if ((i) < 2) dma16(ld0 + (slot) + (i) * 1024, xv, srx, xs0 + (t) * 2048 + (i) * 1024); \
        else dma16(ld1 + (slot) + ((i) - 2) * 8192, wv, srw, ws0 + (t) * (64 * 6144) + ((i) - 2) * 2048); } while (0)
#define K1_STAGE(t, slot) do { _Pragma("unroll") for (int i_ = 0; i_ < 5; ++i_) K1_DMA(i_, t, slot); } while (0)
    const int xrd = wr * 8192 + lane * 16;
    const int q4 = (lane >> 2) & 3, p4 = lane & 3, trow = 4 * g + q4;
    const int wrd = 16384 + trow * 128 + ((wc ^ ((trow >> 1) & 3)) * 32) + (p4 >> 1) * 16 + (p4 & 1) * 8;
    f32x4 acc[4][3];
#pragma unroll
    for (int m = 0; m < 4; ++m)
#pragma unroll
        for (int n = 0; n < 3; ++n) acc[m][n] = (f32x4){0.f, 0.f, 0.f, 0.f};
    bf16x8 xa[4], xb[4]; s16x4 wa[6], wb[6];
#define K1_RD(i, X, W, px, pw, s) do { if ((i) < 6) W[i] = trd((pw) + ((i) >> 1) * 8192 + (s) * 4096 + ((i) & 1) * 2048); else X[(i) - 6] = ldf((px) + ((i) - 6) * 2048 + (s) * 1024); } while (0)
#define K1_MM(i, X, W) do { const int m_ = (i) / 3, n_ = (i) % 3; const bf16x8 wf_ = cat8(W[2 * n_], W[2 * n_ + 1]); \
        acc[m_][n_] = (n_ < 2) ? MFMA16(wf_, X[m_], acc[m_][n_]) : MFMA16(X[m_], wf_, acc[m_][n_]); } while (0)
#define K1_HALF(XM, WM, XR, WR, px, pw, s, DOMMA, DODMA, t, slot) do { \
        _Pragma("unroll") for (int i_ = 0; i_ < 12; ++i_) { \
            if (DOMMA) K1_MM(i_, XM, WM); \
            if (i_ < 5) { if (DODMA) K1_DMA(i_, t, slot); K1_RD(2 * i_, XR, WR, px, pw, s); K1_RD(2 * i_ + 1, XR, WR, px, pw, s); } \
            SCHED0(); } } while (0)
    constexpr int NT = DM / 64;
    K1_STAGE(0, 0); K1_STAGE(1, K1_SLOT);
    WAITBAR(5);
    { LAS const char* px = lds + xrd; LAS const char* pw = lds + wrd;
      K1_HALF(xb, wb, xa, wa, px, pw, 0, false, true, 2, 2 * K1_SLOT);
      K1_HALF(xa, wa, xb, wb, px, pw, 1, true, false, 0, 0); }
    int cur = K1_SLOT, fr = 0;
    for (int t = 1; t < NT - 2; ++t) {
        WAITBAR(5);
        LAS const char* px = lds + cur + xrd; LAS const char* pw = lds + cur + wrd;
        K1_HALF(xb, wb, xa, wa, px, pw, 0, true, true, t + 2, fr);
        K1_HALF(xa, wa, xb, wb, px, pw, 1, true, false, 0, 0);
        cur = (cur == 2 * K1_SLOT) ? 0 : cur + K1_SLOT; fr = (fr == 2 * K1_SLOT) ? 0 : fr + K1_SLOT;
    }
#pragma unroll
    for (int t = NT - 2; t < NT; ++t) {
        if (t == NT - 2) WAITBAR(5); else WAITBAR(0);
        LAS const char* px = lds + cur + xrd; LAS const char* pw = lds + cur + wrd;
        K1_HALF(xb, wb, xa, wa, px, pw, 0, true, false, 0, 0);
        K1_HALF(xa, wa, xb, wb, px, pw, 1, true, false, 0, 0);
        cur = (cur == 2 * K1_SLOT) ? 0 : cur + K1_SLOT;
    }
#pragma unroll
    for (int i = 0; i < 12; ++i) K1_MM(i, xb, wb);
    const int head = 2 * p + (wc >> 1), half = wc & 1;
#pragma unroll
    for (int m = 0; m < 4; ++m) {
        const int row0 = rt * 128 + wr * 64 + m * 16;
        const size_t blk = ((size_t)head * 64 + (row0 >> 5)) * 2048;
        {
            const size_t o = blk + (g >> 1) * 1024 + (g & 1) * 512 + (((row0 & 16) + r) * 16) + half * 8;
            u32x2 qv; qv.x = pk_bf16(acc[m][0][0] * QSCALE, acc[m][0][1] * QSCALE); qv.y = pk_bf16(acc[m][0][2] * QSCALE, acc[m][0][3] * QSCALE);
            *(u32x2*)(ws + WS_Q + o) = qv;
            u32x2 kv; kv.x = pk_bf16(acc[m][1][0], acc[m][1][1]); kv.y = pk_bf16(acc[m][1][2], acc[m][1][3]);
            *(u32x2*)(ws + WS_K + o) = kv;
        }
        {
            const size_t o = blk + ((row0 >> 4) & 1) * 1024 + (g & 1) * 512 + (16 * half + r) * 16 + (g >> 1) * 8;
            u32x2 vv; vv.x = pk_bf16(acc[m][2][0], acc[m][2][1]); vv.y = pk_bf16(acc[m][2][2], acc[m][2][3]);
            *(u32x2*)(ws + WS_V + o) = vv;
        }
    }
}

constexpr int K2_SLOT = 32768, K2_LDS = 3 * K2_SLOT;
#define K2_STAGE(rd, slot) do { dma16(d0 + (slot), lv, srk, s0 + (rd) * 8192); dma16(d0 + (slot) + 1024, lv, srk, s0 + (rd) * 8192 + 1024); \
        dma16(d0 + (slot) + 8192, lv, srv, s0 + (rd) * 8192); dma16(d0 + (slot) + 9216, lv, srv, s0 + (rd) * 8192 + 1024); } while (0)
__device__ __forceinline__ void attn_fast(LAS char* lds, srd_t srk, srd_t srv, unsigned hbase, const bf16x8 (&qf)[2][2], int w, int lane, f32x16 (&o)[2], f32x4 (&lacc)[2]) {
    const int kh = w >> 2, tl = w & 3;
    const unsigned lds0 = (unsigned)(uintptr_t)lds;
    const unsigned lv = lane * 16;
    const unsigned s0 = hbase + (unsigned)(kh * 32 + tl) * 2048u, d0 = lds0 + kh * 16384 + tl * 2048;
    const int rd0 = kh * 16384 + lane * 16;
    bf16x8 ones;
    { const short v = ((lane & 15) == ((lane >> 4) & 1)) ? (short)0x3f80 : (short)0;
#pragma unroll
      for (int j = 0; j < 8; ++j) ones[j] = v; }
    K2_STAGE(0, 0); K2_STAGE(1, K2_SLOT);
    WAITBAR(0);
    K2_STAGE(2, 2 * K2_SLOT);
    f32x16 sa[2], sb[2];
    const f32x16 zero16 = {};
    { const bf16x8 k0 = ldf(lds + rd0), k1 = ldf(lds + rd0 + 1024);
#pragma unroll
      for (int qt = 0; qt < 2; ++qt) { sa[qt] = MFMA32(k0, qf[qt][0], zero16); sa[qt] = MFMA32(k1, qf[qt][1], sa[qt]); } }
#define K2_TILE(SC, SN, kaddr, vaddr) do { \
        const bf16x8 k0 = ldf(kaddr), k1 = ldf((kaddr) + 1024); \
        const bf16x8 v0 = ldf(vaddr), v1 = ldf((vaddr) + 1024); \
        _Pragma("unroll") for (int qt = 0; qt < 2; ++qt) { SN[qt] = MFMA32(k0, qf[qt][0], zero16); SN[qt] = MFMA32(k1, qf[qt][1], SN[qt]); } \
        _Pragma("unroll") for (int qt = 0; qt < 2; ++qt) { \
            u32x4 p0, p1; \
            _Pragma("unroll") for (int j = 0; j < 4; ++j) p0[j] = pk_bf16(__builtin_amdgcn_exp2f(SC[qt][2 * j]), __builtin_amdgcn_exp2f(SC[qt][2 * j + 1])); \
            const bf16x8 b0 = __builtin_bit_cast(bf16x8, p0); \
            o[qt] = MFMA32(v0, b0, o[qt]); \
            lacc[qt] = MFMA16(ones, b0, lacc[qt]); \
            _Pragma("unroll") for (int j = 0; j < 4; ++j) p1[j] = pk_bf16(__builtin_amdgcn_exp2f(SC[qt][8 + 2 * j]), __builtin_amdgcn_exp2f(SC[qt][9 + 2 * j])); \
            const bf16x8 b1 = __builtin_bit_cast(bf16x8, p1); \
            o[qt] = MFMA32(v1, b1, o[qt]); \
            lacc[qt] = MFMA16(ones, b1, lacc[qt]); \
        } SCHED0(); } while (0)
    int cur = 0;
    for (int rd = 0; rd < 8; ++rd) {
        const int nx = (cur == 2 * K2_SLOT) ? 0 : cur + K2_SLOT;
        if (rd > 0) { WAITBAR(0); if (rd + 2 < 8) { const int fr = (cur == 0) ? 2 * K2_SLOT : cur - K2_SLOT; K2_STAGE(rd + 2, fr); } }
        LAS const char* pc = lds + cur + rd0; LAS const char* pn = lds + nx + rd0;
        K2_TILE(sa, sb, pc + 2048, pc + 8192);
        K2_TILE(sb, sa, pc + 4096, pc + 8192 + 2048);
        K2_TILE(sa, sb, pc + 6144, pc + 8192 + 4096);
        K2_TILE(sb, sa, pn, pc + 8192 + 6144);
        cur = nx;
    }
    WAITBAR(0);
#undef K2_TILE
}
__device__ __forceinline__ void attn_safe(LAS char* lds, srd_t srk, srd_t srv, unsigned hbase, const bf16x8 (&qf)[2][2], int w, int lane, f32x16 (&o)[2], float (&mrun)[2], float (&lrun)[2]) {
    const int kh = w >> 2, tl = w & 3;
    const unsigned lds0 = (unsigned)(uintptr_t)lds;
    const unsigned lv = lane * 16;
    const unsigned s0 = hbase + (unsigned)(kh * 32 + tl) * 2048u, d0 = lds0 + kh * 16384 + tl * 2048;
    const int rd0 = kh * 16384 + lane * 16;
    const f32x16 zero16 = {};
#pragma unroll 1
    for (int rd = 0; rd < 8; ++rd) {
        K2_STAGE(rd, 0);
        WAITBAR(0);
#pragma unroll 1
        for (int i = 0; i < 4; ++i) {
            LAS const char* pk = lds + rd0 + i * 2048;
            const bf16x8 k0 = ldf(pk), k1 = ldf(pk + 1024), v0 = ldf(pk + 8192), v1 = ldf(pk + 9216);
#pragma unroll
            for (int qt = 0; qt < 2; ++qt) {
                f32x16 sc = MFMA32(k0, qf[qt][0], zero16); sc = MFMA32(k1, qf[qt][1], sc);
                float mx = sc[0];
#pragma unroll
                for (int j = 1; j < 16; ++j) mx = fmaxf(mx, sc[j]);
                mx = fmaxf(mx, __shfl_xor(mx, 32));
                const float mn = fmaxf(mrun[qt], mx);
                const float ms = (mn == -INFINITY) ? 0.f : mn;
                const float al = __builtin_amdgcn_exp2f(mrun[qt] - ms);
                mrun[qt] = mn;
                u32x4 p0, p1;
#pragma unroll
                for (int j = 0; j < 4; ++j) { p0[j] = pk_bf16(__builtin_amdgcn_exp2f(sc[2 * j] - ms), __builtin_amdgcn_exp2f(sc[2 * j + 1] - ms));
                                              p1[j] = pk_bf16(__builtin_amdgcn_exp2f(sc[8 + 2 * j] - ms), __builtin_amdgcn_exp2f(sc[9 + 2 * j] - ms)); }
                float s_ = 0.f;
#pragma unroll
                for (int j = 0; j < 4; ++j) { s_ += bf_lo(p0[j]) + bf_hi(p0[j]); s_ += bf_lo(p1[j]) + bf_hi(p1[j]); }
                lrun[qt] = lrun[qt] * al + s_;
#pragma unroll
                for (int j = 0; j < 16; ++j) o[qt][j] *= al;
                o[qt] = MFMA32(v0, __builtin_bit_cast(bf16x8, p0), o[qt]); o[qt] = MFMA32(v1, __builtin_bit_cast(bf16x8, p1), o[qt]);
            }
        }
        WAITBAR(0);
    }
}
#undef K2_STAGE

__global__ __launch_bounds__(512, 2) void k2_attn(char* __restrict__ ws, const u16* __restrict__ wg, const u16* __restrict__ wu) {
    extern __shared__ __attribute__((aligned(16))) char smem_g[];
    LAS char* lds = (LAS char*)smem_g;
    const int tid = threadIdx.x, lane = tid & 63, w = __builtin_amdgcn_readfirstlane(tid >> 6);
    const int xcd = blockIdx.x & 7, loc = blockIdx.x >> 3;
    const int head = xcd * 4 + (loc & 3), qb = loc >> 2;
    u32x4 cw[8];
    const bool do_cs = blockIdx.x < 224;
    const int cgrp = blockIdx.x % 56, rq = blockIdx.x / 56;
    if (do_cs) {
        const u16* src = (cgrp < 28 ? wg + cgrp * 128 : wu + (cgrp - 28) * 128) + (size_t)(rq * 256 + w * 32 + (lane >> 4)) * FF + (lane & 15) * 8;
#pragma unroll
        for (int i = 0; i < 8; ++i) cw[i] = *(const u32x4*)(src + (size_t)i * 4 * FF);
    }
    const char* qsrc = ws + WS_Q + ((size_t)head * 64 + qb * 8 + (w & 3) * 2) * 2048 + lane * 16;
    bf16x8 qf[2][2];
#pragma unroll
    for (int qt = 0; qt < 2; ++qt)
#pragma unroll
        for (int s = 0; s < 2; ++s) qf[qt][s] = *(const bf16x8*)(qsrc + qt * 2048 + s * 1024);
    const srd_t srk = mk_srd(ws + WS_K, SEQ * DM * 2), srv = mk_srd(ws + WS_V, SEQ * DM * 2);
    const unsigned hbase = (unsigned)head * 64u * 2048u;
    f32x16 o[2] = {}; f32x4 lacc[2] = {};
    attn_fast(lds, srk, srv, hbase, qf, w, lane, o, lacc);
    LAS float* ob = (LAS float*)lds;
    LAS float* lb = (LAS float*)(lds + 32768);
    LAS unsigned* flag = (LAS unsigned*)(lds + 32768 + 4096);
    LAS float* cb = (LAS float*)(lds + 40960);
    const int kh = w >> 2, hi = lane >> 5, q32 = lane & 31;
    char* dst = ws + WS_O + ((size_t)(qb * 16 + (w & 3) * 4 + kh * 2 + (q32 >> 4)) * 32 + head) * 1024 + (hi * 16 + (q32 & 15)) * 16;
    bool bad;
    {
        if (tid == 0) flag[0] = 0u;
        if (lane < 16) { lb[w * 64 + lane] = lacc[0][0]; lb[w * 64 + 16 + lane] = lacc[0][1]; lb[w * 64 + 32 + lane] = lacc[1][0]; lb[w * 64 + 48 + lane] = lacc[1][1]; }
#pragma unroll
        for (int j = 0; j < 16; ++j) ob[(w * 16 + j) * 64 + lane] = kh ? o[0][j] : o[1][j];
        if (do_cs) {
            bf16x8 sel;
#pragma unroll
            for (int j = 0; j < 8; ++j) sel[j] = ((lane & 15) == j) ? (short)0x3f80 : (short)0;
            f32x4 cs = {0.f, 0.f, 0.f, 0.f};
#pragma unroll
            for (int i = 0; i < 8; ++i) cs = MFMA16(sel, __builtin_bit_cast(bf16x8, cw[i]), cs);
            if (lane < 32) *(LAS f32x4*)(cb + w * 128 + (lane & 15) * 8 + (lane >> 4) * 4) = cs;
        }
        WAITBAR(0);
        const int pw = w ^ 4;
        f32x16 of;
#pragma unroll
        for (int j = 0; j < 16; ++j) of[j] = (kh ? o[1][j] : o[0][j]) + ob[(pw * 16 + j) * 64 + lane];
        const float l = lb[w * 64 + kh * 32 + q32] + lb[pw * 64 + kh * 32 + q32];
        bool ok = (l >= 0x1p-60f) && (l <= 0x1p+80f);
#pragma unroll
        for (int j = 0; j < 16; ++j) ok = ok && (fabsf(of[j]) <= 0x1p+120f);
        if (!__all(ok)) { if (lane == 0) flag[0] = 1u; }
        if (do_cs && tid < 128) {
            float s = 0.f;
#pragma unroll
            for (int i = 0; i < 8; ++i) s += cb[i * 128 + tid];
            ((float*)(ws + WS_CS))[rq * 7168 + cgrp * 128 + tid] = s;
        }
        WAITBAR(0);
        bad = flag[0] != 0u;
        if (!bad) {
            const float rl = 1.0f / l;
#pragma unroll
            for (int c = 0; c < 2; ++c) {
                u32x4 ov; ov.x = pk_bf16(of[8 * c + 0] * rl, of[8 * c + 1] * rl); ov.y = pk_bf16(of[8 * c + 2] * rl, of[8 * c + 3] * rl);
                ov.z = pk_bf16(of[8 * c + 4] * rl, of[8 * c + 5] * rl); ov.w = pk_bf16(of[8 * c + 6] * rl, of[8 * c + 7] * rl);
                *(u32x4*)(dst + c * 512) = ov;
            }
        }
    }
    if (bad) {
        WAITBAR(0);
        o[0] = (f32x16){}; o[1] = (f32x16){}; float mrun[2] = {-INFINITY, -INFINITY}, lrun[2] = {0.f, 0.f};
        attn_safe(lds, srk, srv, hbase, qf, w, lane, o, mrun, lrun);
        LAS float* mb = lb + 512;
#pragma unroll
        for (int qt = 0; qt < 2; ++qt) { const float lt = lrun[qt] + __shfl_xor(lrun[qt], 32); if (lane < 32) { lb[w * 64 + qt * 32 + lane] = lt; mb[w * 64 + qt * 32 + lane] = mrun[qt]; } }
#pragma unroll
        for (int j = 0; j < 16; ++j) ob[(w * 16 + j) * 64 + lane] = kh ? o[0][j] : o[1][j];
        WAITBAR(0);
        const int pw = w ^ 4;
        const float ma = mb[w * 64 + kh * 32 + q32], mp = mb[pw * 64 + kh * 32 + q32];
        const float mn = fmaxf(ma, mp), ms = (mn == -INFINITY) ? 0.f : mn;
        const float fa = __builtin_amdgcn_exp2f(ma - ms), fp = __builtin_amdgcn_exp2f(mp - ms);
        const float l = lb[w * 64 + kh * 32 + q32] * fa + lb[pw * 64 + kh * 32 + q32] * fp;
        const float rl = 1.0f / l;
        float of[16];
#pragma unroll
        for (int j = 0; j < 16; ++j) of[j] = ((kh ? o[1][j] : o[0][j]) * fa + ob[(pw * 16 + j) * 64 + lane] * fp) * rl;
#pragma unroll
        for (int c = 0; c < 2; ++c) {
            u32x4 ov; ov.x = pk_bf16(of[8 * c + 0], of[8 * c + 1]); ov.y = pk_bf16(of[8 * c + 2], of[8 * c + 3]);
            ov.z = pk_bf16(of[8 * c + 4], of[8 * c + 5]); ov.w = pk_bf16(of[8 * c + 6], of[8 * c + 7]);
            *(u32x4*)(dst + c * 512) = ov;
        }
    }
}

constexpr int K3_SLOT = 24576, K3_NS = 5, K3_LDS = K3_NS * K3_SLOT;
template <int MODE>
__global__ __launch_bounds__(512, 2) void k35_gemm(const u16* __restrict__ wgt, char* __restrict__ ws, const u16* __restrict__ xin, u16* __restrict__ out) {
    constexpr int KD = MODE == 0 ? DM : FF, KT = KD / 32, NT = KD / 64;
    extern __shared__ __attribute__((aligned(16))) char smem_g[];
    LAS char* lds = (LAS char*)smem_g;
    const unsigned lds0 = (unsigned)(uintptr_t)lds;
    const int tid = threadIdx.x, lane = tid & 63, w = __builtin_amdgcn_readfirstlane(tid >> 6), kp = w >> 2, wm = (w >> 1) & 1, wn = w & 1, g = lane >> 4, r = lane & 15;
    const int xcd = blockIdx.x & 7, loc = blockIdx.x >> 3;
    const int rt = (xcd >> 1) * 4 + (loc & 3), ct = (xcd & 1) * 8 + (loc >> 2);
    const srd_t srx = mk_srd(ws + (MODE == 0 ? WS_O : WS_ACT), SEQ * KD * 2), srw = mk_srd(wgt, KD * DM * 2);
    const unsigned xv = lane * 16, xs0 = (unsigned)(rt * 8 + w) * (KT * 1024u);
    const int wrow = lane >> 3;
    const int wch = (lane & 7) ^ (((wrow >> 1) & 3) << 1);
    const unsigned wv = (unsigned)(w * 8 + wrow) * 2048u + wch * 16, ws0 = (unsigned)ct * 128u;
    const unsigned ld0 = lds0 + w * 2048, ld1 = lds0 + 16384 + w * 1024;
#define K3_DMA(i, t, slot) do { if ((i) < 2) dma16(ld0 + (slot) + (i) * 1024, xv, srx, xs0 + (t) * 2048 + (i) * 1024); else dma16(ld1 + (slot), wv, srw, ws0 + (t) * (64 * 2048)); } while (0)
#define K3_STAGE(t, slot) do { K3_DMA(0, t, slot); K3_DMA(1, t, slot); K3_DMA(2, t, slot); } while (0)
    const int xrd = wm * 8192 + kp * 1024 + lane * 16;
    const int q4 = (lane >> 2) & 3, p4 = lane & 3;
    const int trow = (MODE == 0) ? (4 * (g & 1) + 16 * (g >> 1) + q4) : (4 * g + q4);
    constexpr int TR2 = (MODE == 0) ? 8 * 128 : 16 * 128;
    int wrd[2];
#pragma unroll
    for (int n = 0; n < 2; ++n) wrd[n] = 16384 + (kp * 32 + trow) * 128 + (((wn * 2 + n) ^ ((trow >> 1) & 3)) * 32) + (p4 >> 1) * 16 + (p4 & 1) * 8;
    f32x4 acc[4][2];
#pragma unroll
    for (int m = 0; m < 4; ++m) { acc[m][0] = (f32x4){0.f, 0.f, 0.f, 0.f}; acc[m][1] = (f32x4){0.f, 0.f, 0.f, 0.f}; }
    bf16x8 xa[4], xb[4]; s16x4 wa[4], wb[4];
#define K3_RD(i, X, W, sl) do { if ((i) < 4) W[i] = trd(lds + (sl) + wrd[(i) >> 1] + ((i) & 1) * TR2); else X[(i) - 4] = ldf(lds + (sl) + xrd + ((i) - 4) * 2048); } while (0)
#define K3_MM(i, X, W) do { const int m_ = (i) >> 1, n_ = (i) & 1; acc[m_][n_] = MFMA16(cat8(W[2 * n_], W[2 * n_ + 1]), X[m_], acc[m_][n_]); } while (0)
#define K3_BODY(XR, WR, XM, WM, DOMMA, DODMA, t, sl, fsl) do { \
        _Pragma("unroll") for (int i_ = 0; i_ < 8; ++i_) { \
            if (DOMMA) K3_MM(i_, XM, WM); \
            if (i_ < 4) { K3_RD(2 * i_, XR, WR, sl); K3_RD(2 * i_ + 1, XR, WR, sl); } \
            if (i_ >= 1 && i_ < 4 && (DODMA)) K3_DMA(i_ - 1, t, fsl); \
            SCHED0(); } } while (0)
    K3_STAGE(0, 0); K3_STAGE(1, K3_SLOT); K3_STAGE(2, 2 * K3_SLOT); K3_STAGE(3, 3 * K3_SLOT);
    WAITBAR(9);
    K3_BODY(xa, wa, xb, wb, false, true, 4, 0, 4 * K3_SLOT);
    int cur = K3_SLOT, fr = 0;
#define K3_ADV() do { fr = (fr == (K3_NS - 1) * K3_SLOT) ? 0 : fr + K3_SLOT; cur = (cur == (K3_NS - 1) * K3_SLOT) ? 0 : cur + K3_SLOT; } while (0)
    int t = 1;
    for (; t + 5 < NT; t += 2) {
        WAITBAR(9); K3_BODY(xb, wb, xa, wa, true, true, t + 4, cur, fr); K3_ADV();
        WAITBAR(9); K3_BODY(xa, wa, xb, wb, true, true, t + 5, cur, fr); K3_ADV();
    }
    WAITBAR(9); K3_BODY(xb, wb, xa, wa, true, true, t + 4, cur, fr); K3_ADV();
    WAITBAR(9); K3_BODY(xa, wa, xb, wb, true, false, 0, cur, fr); K3_ADV();
    WAITBAR(6); K3_BODY(xb, wb, xa, wa, true, false, 0, cur, fr); K3_ADV();
    WAITBAR(3); K3_BODY(xa, wa, xb, wb, true, false, 0, cur, fr); K3_ADV();
    WAITBAR(0); K3_BODY(xb, wb, xa, wa, true, false, 0, cur, fr);
#pragma unroll
    for (int i = 0; i < 8; ++i) K3_MM(i, xb, wb);
    WAITBAR(0);
    LAS f32x4* rb = (LAS f32x4*)lds;
#pragma unroll
    for (int i = 0; i < 2; ++i)
#pragma unroll
        for (int n = 0; n < 2; ++n) rb[(w * 4 + i * 2 + n) * 64 + lane] = kp ? acc[i][n] : acc[2 + i][n];
    WAITBAR(0);
    f32x4 fin[2][2];
#pragma unroll
    for (int i = 0; i < 2; ++i)
#pragma unroll
        for (int n = 0; n < 2; ++n) fin[i][n] = (kp ? acc[2 + i][n] : acc[i][n]) + rb[((w ^ 4) * 4 + i * 2 + n) * 64 + lane];
    const int col0 = ct * 64 + wn * 32 + 4 * g;
    LAS f32x2* sb = (LAS f32x2*)(lds + 32768);
#pragma unroll
    for (int i = 0; i < 2; ++i) {
        const int mrow = rt * 128 + wm * 64 + (kp * 2 + i) * 16;
        char* hp = ws + WS_H + ((size_t)(mrow >> 4) * 32 + ct * 2 + wn) * 1024 + lane * 16;
        if (MODE == 0) {
            const u32x2 xa_ = *(const u32x2*)(xin + (size_t)(mrow + r) * DM + col0), xb_ = *(const u32x2*)(xin + (size_t)(mrow + r) * DM + col0 + 16);
            u32x4 hv;
            hv.x = pk_bf16(bf_lo(xa_.x) + fin[i][0][0], bf_hi(xa_.x) + fin[i][0][1]); hv.y = pk_bf16(bf_lo(xa_.y) + fin[i][0][2], bf_hi(xa_.y) + fin[i][0][3]);
            hv.z = pk_bf16(bf_lo(xb_.x) + fin[i][1][0], bf_hi(xb_.x) + fin[i][1][1]); hv.w = pk_bf16(bf_lo(xb_.y) + fin[i][1][2], bf_hi(xb_.y) + fin[i][1][3]);
            *(u32x4*)hp = hv;
            float s = 0.f, q = 0.f;
#pragma unroll
            for (int j = 0; j < 4; ++j) { const float a = bf_lo(hv[j]), b = bf_hi(hv[j]); s += a + b; q += a * a + b * b; }
            s += __shfl_xor(s, 16); s += __shfl_xor(s, 32); q += __shfl_xor(q, 16); q += __shfl_xor(q, 32);
            if (g == 0) sb[(wm * 64 + (kp * 2 + i) * 16 + r) * 2 + wn] = (f32x2){s, q};
        } else {
            const u32x4 hv = *(const u32x4*)hp;
            u32x2 o0, o1;
            o0.x = pk_bf16(bf_lo(hv.x) + fin[i][0][0], bf_hi(hv.x) + fin[i][0][1]); o0.y = pk_bf16(bf_lo(hv.y) + fin[i][0][2], bf_hi(hv.y) + fin[i][0][3]);
            o1.x = pk_bf16(bf_lo(hv.z) + fin[i][1][0], bf_hi(hv.z) + fin[i][1][1]); o1.y = pk_bf16(bf_lo(hv.w) + fin[i][1][2], bf_hi(hv.w) + fin[i][1][3]);
            *(u32x2*)(out + (size_t)(mrow + r) * DM + col0) = o0; *(u32x2*)(out + (size_t)(mrow + r) * DM + col0 + 16) = o1;
        }
    }
    if (MODE == 0) {
        WAITBAR(0);
        if (tid < 128) { const f32x2 a = sb[tid * 2], b = sb[tid * 2 + 1]; ((f32x2*)(ws + WS_ST))[(size_t)(rt * 128 + tid) * 16 + ct] = a + b; }
    }
}

constexpr int K4_SLOT = 30720, K4_NS = 4, K4_STAT = K4_NS * K4_SLOT, K4_LDS = K4_STAT + 2048;
struct K4Dma { srd_t srx, srg, sru; unsigned xv, gv, xs0, gs0, ld0, ld1; };
#define K4_DMA(i, t, slot) do { if ((i) < 2) dma16(D.ld0 + (slot) + (i) * 1024, D.xv, D.srx, D.xs0 + (t) * 1024 + (i) * 32768); \
        else dma16(D.ld1 + (slot) + ((i) - 2) * 7168, D.gv, (i) == 2 ? D.srg : D.sru, D.gs0 + (t) * (32 * FF * 2)); } while (0)
template <int NJ, int J0, bool HASW>
__device__ __forceinline__ void k4_body(LAS char* lds, const K4Dma& D, char* ws, int w, int lane, int rt, int ct) {
    const int wm = w & 3, g = lane >> 4, r = lane & 15;
    constexpr int NP = HASW ? 4 : 2;
    const int xrd = wm * 4096 + lane * 16;
    const int q4 = (lane >> 2) & 3, p4 = lane & 3;
    const int wrd = 16384 + (4 * g + q4) * 224 + J0 * 32 + (p4 >> 1) * 16 + (p4 & 1) * 8;
    f32x4 ag[4][NJ], au[4][NJ];
#pragma unroll
    for (int m = 0; m < 4; ++m)
#pragma unroll
        for (int j = 0; j < NJ; ++j) { ag[m][j] = (f32x4){0.f, 0.f, 0.f, 0.f}; au[m][j] = (f32x4){0.f, 0.f, 0.f, 0.f}; }
    bf16x8 xa[4], xb[4]; s16x4 wgf[2 * NJ], wuf[2 * NJ];
#define K4_RW(i, W, sl, o) W[i] = trd(lds + (sl) + wrd + (o) + ((i) >> 1) * 32 + ((i) & 1) * (16 * 224))
#define K4_RX(i, X, sl) X[i] = ldf(lds + (sl) + xrd + (i) * 1024)
#define K4_MM(i, A, X, W) do { const int j_ = (i) >> 2, m_ = (i) & 3; A[m_][j_] = MFMA16(cat8(W[2 * j_], W[2 * j_ + 1]), X[m_], A[m_][j_]); } while (0)
#define K4_H1(XC, XP, DOMMA, DODMA, t, sl, fsl) do { \
        _Pragma("unroll") for (int i_ = 0; i_ < 4 * NJ; ++i_) { \
            if (DOMMA) K4_MM(i_, au, XP, wuf); \
            if (i_ < NJ) { K4_RW(2 * i_, wgf, sl, 0); K4_RW(2 * i_ + 1, wgf, sl, 0); } \
            if (i_ < 4) K4_RX(i_, XC, sl); \
            if (i_ >= 4 && i_ < 4 + NP && (DODMA)) K4_DMA(i_ - 4, t, fsl); \
            SCHED0(); } } while (0)
#define K4_H2(XC, sl) do { \
        _Pragma("unroll") for (int i_ = 0; i_ < 4 * NJ; ++i_) { \
            K4_MM(i_, ag, XC, wgf); \
            if (i_ >= 1 && i_ <= NJ) { } \
            SCHED0(); \
            if (i_ >= 3 && i_ < 3 + NJ) { } \
            } } while (0)
    (void)0;
#define K4_H2R(XC, sl) do { \
        _Pragma("unroll") for (int i_ = 0; i_ < 4 * NJ; ++i_) { \
            K4_MM(i_, ag, XC, wgf); \
            if (i_ < NJ) { K4_RW(2 * i_, wuf, sl, 7168); K4_RW(2 * i_ + 1, wuf, sl, 7168); } \
            SCHED0(); } } while (0)
    constexpr int NT = DM / 32;
    WAITBAR(2 * NP);
    K4_H1(xa, xb, false, true, 3, 0, 3 * K4_SLOT);
    K4_H2R(xa, 0);
    int cur = K4_SLOT, fr = 0;
#define K4_ADV() do { fr = (fr == (K4_NS - 1) * K4_SLOT) ? 0 : fr + K4_SLOT; cur = (cur == (K4_NS - 1) * K4_SLOT) ? 0 : cur + K4_SLOT; } while (0)
    int t = 1;
    for (; t + 4 < NT; t += 2) {
        WAITBAR(2 * NP); K4_H1(xb, xa, true, true, t + 3, cur, fr); K4_H2R(xb, cur); K4_ADV();
        WAITBAR(2 * NP); K4_H1(xa, xb, true, true, t + 4, cur, fr); K4_H2R(xa, cur); K4_ADV();
    }
    WAITBAR(2 * NP); K4_H1(xb, xa, true, false, 0, cur, fr); K4_H2R(xb, cur); K4_ADV();
    WAITBAR(NP);     K4_H1(xa, xb, true, false, 0, cur, fr); K4_H2R(xa, cur); K4_ADV();
    WAITBAR(0);      K4_H1(xb, xa, true, false, 0, cur, fr); K4_H2R(xb, cur);
#pragma unroll
    for (int i = 0; i < 4 * NJ; ++i) K4_MM(i, au, xb, wuf);
    const LAS f32x2* st = (const LAS f32x2*)(lds + K4_STAT);
    const float* cs = (const float*)(ws + WS_CS);
    f32x2 ms[4];
#pragma unroll
    for (int m = 0; m < 4; ++m) ms[m] = st[wm * 64 + m * 16 + r];
    char* ap = ws + WS_ACT + (size_t)(rt * 16 + wm * 4) * (112 * 1024) + lane * 16;
#pragma unroll
    for (int j = 0; j < NJ; ++j) {
        const int f0 = ct * 112 + (J0 + j) * 16 + 4 * g, ft = ct * 7 + J0 + j;
        const f32x4 cg = (*(const f32x4*)(cs + f0) + *(const f32x4*)(cs + 7168 + f0)) + (*(const f32x4*)(cs + 2 * 7168 + f0) + *(const f32x4*)(cs + 3 * 7168 + f0));
        const f32x4 cu = (*(const f32x4*)(cs + 3584 + f0) + *(const f32x4*)(cs + 7168 + 3584 + f0)) + (*(const f32x4*)(cs + 2 * 7168 + 3584 + f0) + *(const f32x4*)(cs + 3 * 7168 + 3584 + f0));
#pragma unroll
        for (int m = 0; m < 4; ++m) {
            const float c1 = -LOG2E * ms[m].y;
            float a[4];
#pragma unroll
            for (int e = 0; e < 4; ++e) {
                const float gz = (ag[m][j][e] - ms[m].x * cg[e]) * c1;
                const float uu = (au[m][j][e] - ms[m].x * cu[e]) * ms[m].y;
                a[e] = uu * __builtin_amdgcn_rcpf(1.0f + __builtin_amdgcn_exp2f(gz));
            }
            u32x2 ov; ov.x = pk_bf16(a[0], a[1]); ov.y = pk_bf16(a[2], a[3]);
            *(u32x2*)(ap + (size_t)m * (112 * 1024) + (size_t)(ft >> 1) * 1024 + (ft & 1) * 8) = ov;
        }
        SCHED0();
    }
}

__global__ __launch_bounds__(512, 2) void k4_gateup(const u16* __restrict__ wg, const u16* __restrict__ wu, char* __restrict__ ws) {
    extern __shared__ __attribute__((aligned(16))) char smem_g[];
    LAS char* lds = (LAS char*)smem_g;
    const unsigned lds0 = (unsigned)(uintptr_t)lds;
    const int tid = threadIdx.x, lane = tid & 63, w = __builtin_amdgcn_readfirstlane(tid >> 6);
    const int xcd = blockIdx.x & 7, loc = blockIdx.x >> 3;
    const int rt = (xcd >> 2) * 4 + (loc & 3), ct = (xcd & 3) * 8 + (loc >> 2);
    K4Dma D;
    D.srx = mk_srd(ws + WS_H, SEQ * DM * 2); D.srg = mk_srd(wg, DM * FF * 2); D.sru = mk_srd(wu, DM * FF * 2);
    D.xv = lane * 16; D.xs0 = (unsigned)(rt * 16 + w * 2) * 32768u;
    { const int ci = (w < 7 ? w : 6) * 64 + lane; const int row = ci / 14, c = ci % 14; D.gv = (unsigned)row * (FF * 2) + c * 16; }
    D.gs0 = (unsigned)ct * 224u; D.ld0 = lds0 + w * 2048; D.ld1 = lds0 + 16384 + w * 1024;
#pragma unroll
    for (int t = 0; t < 3; ++t) {
        K4_DMA(0, t, t * K4_SLOT); K4_DMA(1, t, t * K4_SLOT);
        if (w < 7) { K4_DMA(2, t, t * K4_SLOT); K4_DMA(3, t, t * K4_SLOT); }
    }
    {
        const int row = tid >> 1, hf = tid & 1;
        const f32x4* sp = (const f32x4*)(ws + WS_ST + ((size_t)(rt * 256 + row) * 16 + hf * 8) * 8);
        float s = 0.f, q = 0.f;
#pragma unroll
        for (int i = 0; i < 4; ++i) { const f32x4 v = sp[i]; s += v[0] + v[2]; q += v[1] + v[3]; }
        s += __shfl_xor(s, 1); q += __shfl_xor(q, 1);
        const float mean = s * (1.0f / DM);
        const float var = fmaxf(q * (1.0f / DM) - mean * mean, 0.f);
        if (hf == 0) ((LAS f32x2*)(lds + K4_STAT))[row] = (f32x2){mean, 1.0f / sqrtf(var + LN_EPS)};
    }
    if (w < 4) k4_body<4, 0, true>(lds, D, ws, w, lane, rt, ct);
    else if (w < 7) k4_body<3, 4, true>(lds, D, ws, w, lane, rt, ct);
    else k4_body<3, 4, false>(lds, D, ws, w, lane, rt, ct);
}

extern "C" void kernel_launch(void* const* d_in, const int* in_sizes, int n_in, void* d_out, int out_size, void* d_ws, size_t ws_size, hipStream_t stream) {
    const u16* x = (const u16*)d_in[0];
    const u16* wqkv = (const u16*)d_in[1];
    const u16* wo = (const u16*)d_in[2];
    const u16* wg = (const u16*)d_in[4];
    const u16* wu = (const u16*)d_in[5];
    const u16* wd = (const u16*)d_in[6];
    char* ws = (char*)d_ws;
    (void)hipFuncSetAttribute((const void*)k1_qkv, hipFuncAttributeMaxDynamicSharedMemorySize, K1_LDS);
    (void)hipFuncSetAttribute((const void*)k2_attn, hipFuncAttributeMaxDynamicSharedMemorySize, K2_LDS);
    (void)hipFuncSetAttribute((const void*)k35_gemm<0>, hipFuncAttributeMaxDynamicSharedMemorySize, K3_LDS);
    (void)hipFuncSetAttribute((const void*)k35_gemm<1>, hipFuncAttributeMaxDynamicSharedMemorySize, K3_LDS);
    (void)hipFuncSetAttribute((const void*)k4_gateup, hipFuncAttributeMaxDynamicSharedMemorySize, K4_LDS);
    k0_ln<<<128, 512, 0, stream>>>(x, ws);
    k1_qkv<<<256, 512, K1_LDS, stream>>>(wqkv, ws);
    k2_attn<<<256, 512, K2_LDS, stream>>>(ws, wg, wu);
    k35_gemm<0><<<256, 512, K3_LDS, stream>>>(wo, ws, x, nullptr);
    k4_gateup<<<256, 512, K4_LDS, stream>>>(wg, wu, ws);
    k35_gemm<1><<<256, 512, K3_LDS, stream>>>(wd, ws, nullptr, (u16*)d_out);
}
```

```cpp
#include <hip/hip_runtime.h>
#include <hip/hip_bf16.h>
#include <stdint.h>

#define LAS __attribute__((address_space(3)))
typedef unsigned short u16;
typedef short bf16x8 __attribute__((ext_vector_type(8)));
typedef short s16x4 __attribute__((ext_vector_type(4)));
typedef float f32x2 __attribute__((ext_vector_type(2)));
typedef float f32x4 __attribute__((ext_vector_type(4)));
typedef float f32x16 __attribute__((ext_vector_type(16)));
typedef unsigned u32x2 __attribute__((ext_vector_type(2)));
typedef unsigned u32x4 __attribute__((ext_vector_type(4)));
typedef __bf16 bf16x2_t __attribute__((ext_vector_type(2)));
typedef __amdgpu_buffer_rsrc_t srd_t;

constexpr int SEQ = 2048, DM = 1024, NH = 32, FF = 3584;
constexpr float LN_EPS = 1e-5f;
constexpr float QSCALE = 0.17677669529663687f * 1.4426950408889634f;
constexpr float LOG2E = 1.4426950408889634f;

constexpr size_t WS_Q = 0;
constexpr size_t WS_K = WS_Q + (4u << 20);
constexpr size_t WS_V = WS_K + (4u << 20);
constexpr size_t WS_O = WS_V + (4u << 20);
constexpr size_t WS_H = WS_O + (4u << 20);
constexpr size_t WS_ACT = WS_H + (4u << 20);
constexpr size_t WS_ST = WS_ACT + (size_t)SEQ * FF * 2;
constexpr size_t WS_CS = WS_ST + (size_t)SEQ * 16 * 8;
constexpr size_t WS_S1 = WS_CS + 4 * 7168 * 4;
constexpr size_t WS_C1 = WS_S1 + SEQ * 8;
constexpr size_t WS_FLG = WS_C1 + 16 * 3072 * 4;
constexpr size_t WS_END = WS_FLG + 8192;
constexpr int FL_EPOCH = 0, FL_ERR = 32, FL_0 = 64, FL_1 = FL_0 + 256, FL_2 = FL_1 + 256, FL_3 = FL_2 + 256, FL_4 = FL_3 + 256;

#define ST_AUX 16

__device__ __forceinline__ unsigned pk_bf16(float lo, float hi) { f32x2 v = {lo, hi}; bf16x2_t b = __builtin_convertvector(v, bf16x2_t); return __builtin_bit_cast(unsigned, b); }
__device__ __forceinline__ float bf_lo(unsigned u) { return __uint_as_float(u << 16); }
__device__ __forceinline__ float bf_hi(unsigned u) { return __uint_as_float(u & 0xffff0000u); }
__device__ __forceinline__ s16x4 trd(LAS const char* p) { return __builtin_bit_cast(s16x4, __builtin_amdgcn_ds_read_tr16_b64_v4i16((LAS s16x4*)p)); }
__device__ __forceinline__ bf16x8 cat8(s16x4 a, s16x4 b) { return (bf16x8){a[0], a[1], a[2], a[3], b[0], b[1], b[2], b[3]}; }
__device__ __forceinline__ bf16x8 ldf(LAS const char* p) { return *(LAS const bf16x8*)p; }
__device__ __forceinline__ srd_t mk_srd(const void* p, unsigned bytes) { return __builtin_amdgcn_make_buffer_rsrc((void*)p, (short)0, (int)bytes, 0x00020000); }
__device__ __forceinline__ void dma16(unsigned ldsa, unsigned voff, srd_t srd, unsigned soff) {
    asm volatile("s_mov_b32 m0, %0\n\ts_nop 0\n\tbuffer_load_dwordx4 %1, %2, %3 offen lds" :: "s"(ldsa), "v"(voff), "s"(srd), "s"(soff));
}
__device__ __forceinline__ void dma16c(unsigned ldsa, unsigned voff, srd_t srd, unsigned soff) {
    asm volatile("s_mov_b32 m0, %0\n\ts_nop 0\n\tbuffer_load_dwordx4 %1, %2, %3 offen sc1 lds" :: "s"(ldsa), "v"(voff), "s"(srd), "s"(soff));
}
__device__ __forceinline__ void st8(srd_t srd, unsigned off, u32x2 v) { __builtin_amdgcn_raw_buffer_store_b64(v, srd, off, 0, ST_AUX); }
__device__ __forceinline__ void st16(srd_t srd, unsigned off, u32x4 v) { __builtin_amdgcn_raw_buffer_store_b128(v, srd, off, 0, ST_AUX); }
__device__ __forceinline__ u32x4 pk8(const f32x4 a, const f32x4 b) { u32x4 o; o.x = pk_bf16(a[0], a[1]); o.y = pk_bf16(a[2], a[3]); o.z = pk_bf16(b[0], b[1]); o.w = pk_bf16(b[2], b[3]); return o; }
#define MFMA16(a, b, c) __builtin_amdgcn_mfma_f32_16x16x32_bf16(a, b, c, 0, 0, 0)
#define MFMA32(a, b, c) __builtin_amdgcn_mfma_f32_32x32x16_bf16(a, b, c, 0, 0, 0)
#define WAITVL(n) asm volatile("s_waitcnt vmcnt(%0) lgkmcnt(0)" :: "n"(n) : "memory")
#define WAITBAR(n) asm volatile("s_waitcnt vmcnt(%0) lgkmcnt(0)\n\ts_barrier" :: "n"(n) : "memory")
#define LBAR() asm volatile("s_waitcnt lgkmcnt(0)\n\ts_barrier" ::: "memory")
#define SCHED0() __builtin_amdgcn_sched_barrier(0)
#define WAITV(n) asm volatile("s_waitcnt vmcnt(%0)" :: "n"(n) : "memory")
#define RLX_AGENT __ATOMIC_RELAXED, __HIP_MEMORY_SCOPE_AGENT
typedef __attribute__((address_space(1))) unsigned gu32;
typedef __attribute__((address_space(1))) unsigned long long gu64;
struct Params { const u16 *x, *wqkv, *wo, *wg, *wu, *wd; u16* out; char* ws; };
__device__ __forceinline__ void flag_set(char* ws, int idx, unsigned tag) { __hip_atomic_store((gu32*)(ws + WS_FLG) + idx, tag, RLX_AGENT); }
__device__ __forceinline__ void flags_wait(char* ws, int idx, unsigned tag) {
    gu32* fl = (gu32*)(ws + WS_FLG);
    const unsigned long long t0 = __builtin_amdgcn_s_memrealtime();
    for (unsigned spins = 1;; ++spins) {
        const unsigned v = idx >= 0 ? __hip_atomic_load(fl + idx, RLX_AGENT) : tag;
        if (__all(v == tag)) break;
        if ((spins & 63u) == 0u) {
            if (__builtin_amdgcn_readfirstlane(__hip_atomic_load(fl + FL_ERR, RLX_AGENT)) == tag) break;
            if (__builtin_amdgcn_s_memrealtime() - t0 > 2000000ull) { __hip_atomic_store(fl + FL_ERR, tag, RLX_AGENT); break; }
        }
        __builtin_amdgcn_s_sleep(1);
    }
    asm volatile("" ::: "memory");
}
__device__ __forceinline__ u32x4 ldc16(srd_t srd, unsigned off) { return __builtin_amdgcn_raw_buffer_load_b128(srd, off, 0, 16); }
__device__ __forceinline__ float ldc4(srd_t srd, unsigned off) { return __uint_as_float(__builtin_amdgcn_raw_buffer_load_b32(srd, off, 0, 16)); }

#define STAMP_DECL
#define STAMP_AT(i)
#define STAMP_OUT(kid, n)
#define STAMP_ARGS
#define STAMP_PASS

constexpr int K1_SLOT = 40960, K1_AUX = 3 * K1_SLOT;
template <bool SYNC>
__device__ __forceinline__ void phase1(LAS char* lds, const Params& P, unsigned tag STAMP_ARGS) {
    const unsigned lds0 = (unsigned)(uintptr_t)lds;
    const int tid = threadIdx.x, lane = tid & 63, w = __builtin_amdgcn_readfirstlane(tid >> 6), g = lane >> 4, r = lane & 15;
    const int b = blockIdx.x, xcd = b & 7, loc = b >> 3;
    const int p = xcd * 2 + (loc & 1), rt = loc >> 1;
    char* ws = P.ws;
    constexpr int NT = DM / 64;
    LAS f32x2* stat = (LAS f32x2*)(lds + K1_AUX);
    LAS float* csl = (LAS float*)(lds + K1_AUX + 1024);
    if (w >= 4) {
        const srd_t srx = mk_srd(P.x, SEQ * DM * 2), srw = mk_srd(P.wqkv, DM * 3 * DM * 2);
        const int j = w - 4, rr = lane >> 3, c8 = lane & 7;
        unsigned xv[2], wv[2];
#pragma unroll
        for (int i = 0; i < 2; ++i) {
            xv[i] = rr * 2048 + ((c8 ^ (4 * i + (rr >> 1))) * 16);
            const int f = ((rr >> 1) & 1) | (i << 1);
            wv[i] = rr * 6144 + (((c8 >> 1) ^ f) * 32) + (c8 & 1) * 16;
        }
        const unsigned xs0 = (unsigned)(rt * 128 + 32 * j) * 2048u, ws0 = (unsigned)p * 128u + (unsigned)(16 * j) * 6144u;
        const unsigned xl0 = lds0 + j * 4096, wl0 = lds0 + 16384 + j * 2048;
#define K1_DMA(i, t, slot) do { if ((i) < 4) dma16(xl0 + (slot) + (i) * 1024, xv[(i) & 1], srx, xs0 + (i) * (8 * 2048) + (t) * 128); \
        else dma16(wl0 + (slot) + (((i) - 4) >> 1) * 8192 + (((i) - 4) & 1) * 1024, wv[((i) - 4) & 1], srw, ws0 + (((i) - 4) >> 1) * 2048 + (((i) - 4) & 1) * (8 * 6144) + (t) * (64 * 6144)); } while (0)
#define K1_STAGE(t, slot) do { _Pragma("unroll") for (int i_ = 0; i_ < 10; ++i_) K1_DMA(i_, t, slot); } while (0)
        K1_STAGE(0, 0); K1_STAGE(1, K1_SLOT);
        int fr = 2 * K1_SLOT;
#pragma unroll 1
        for (int t = 0; t + 2 < NT; ++t) {
            WAITBAR(10);
            K1_STAGE(t + 2, fr);
            fr = (fr == 2 * K1_SLOT) ? 0 : fr + K1_SLOT;
        }
        WAITBAR(10);
        if (j == 0) {
            if (SYNC) flags_wait(ws, lane < 16 ? FL_0 + 16 * rt + lane : -1, tag);
            const u32x4 v = ldc16(mk_srd(ws + WS_S1, SEQ * 8), (unsigned)(rt * 128) * 8u + lane * 16);
            *(LAS u32x4*)(lds + K1_AUX + lane * 16) = v;
        } else {
            const int grp = (j - 1) * 8 + (p >> 1);
            if (SYNC) flags_wait(ws, lane < 8 ? FL_0 + grp + 24 * lane : -1, tag);
            const srd_t src = mk_srd(ws + WS_C1, 16 * 3072 * 4);
            float a[16];
#pragma unroll
            for (int i = 0; i < 16; ++i) a[i] = ldc4(src, (unsigned)(i * 3072 + (j - 1) * 1024 + p * 64 + lane) * 4u);
            float s = 0.f;
#pragma unroll
            for (int i = 0; i < 16; ++i) s += a[i];
            csl[(j - 1) * 64 + lane] = s;
        }
        WAITBAR(0);
        LBAR();
    } else {
        const int wr = w >> 1, wc = w & 1;
        {
            const bool docs = b < 192 && w < 2;
            const int grp = b % 24, part = 2 * (b / 24) + w;
            u32x4 xr[2][2], cw[16];
#pragma unroll
            for (int i = 0; i < 2; ++i)
#pragma unroll
                for (int k = 0; k < 2; ++k) xr[i][k] = *(const u32x4*)(P.x + (size_t)(8 * b + 2 * w + i) * DM + k * 512 + lane * 8);
            if (docs) {
                const u16* src = P.wqkv + (size_t)(part * 64 + g) * (3 * DM) + grp * 128 + r * 8;
#pragma unroll
                for (int i = 0; i < 16; ++i) cw[i] = *(const u32x4*)(src + (size_t)i * 4 * (3 * DM));
            }
#pragma unroll
            for (int i = 0; i < 2; ++i) {
                float s = 0.f;
#pragma unroll
                for (int k = 0; k < 2; ++k)
#pragma unroll
                    for (int e = 0; e < 4; ++e) s += bf_lo(xr[i][k][e]) + bf_hi(xr[i][k][e]);
#pragma unroll
                for (int o = 1; o < 64; o <<= 1) s += __shfl_xor(s, o);
                const float mean = s * (1.0f / DM);
                float q = 0.f;
#pragma unroll
                for (int k = 0; k < 2; ++k)
#pragma unroll
                    for (int e = 0; e < 4; ++e) { const float u = bf_lo(xr[i][k][e]) - mean, v = bf_hi(xr[i][k][e]) - mean; q += u * u + v * v; }
#pragma unroll
                for (int o = 1; o < 64; o <<= 1) q += __shfl_xor(q, o);
                const float rstd = 1.0f / sqrtf(q * (1.0f / DM) + LN_EPS);
                if (lane == 0) __hip_atomic_store((gu64*)(ws + WS_S1) + (8 * b + 2 * w + i), ((unsigned long long)__float_as_uint(rstd) << 32) | __float_as_uint(mean), RLX_AGENT);
            }
            if (docs) {
                bf16x8 sel;
#pragma unroll
                for (int e = 0; e < 8; ++e) sel[e] = (r == e) ? (short)0x3f80 : (short)0;
                f32x4 cs = {0.f, 0.f, 0.f, 0.f};
#pragma unroll
                for (int i = 0; i < 16; ++i) cs = MFMA16(sel, __builtin_bit_cast(bf16x8, cw[i]), cs);
                if (lane < 32) __builtin_amdgcn_raw_buffer_store_b128(__builtin_bit_cast(u32x4, cs), mk_srd(ws + WS_C1, 16 * 3072 * 4), (unsigned)(part * 3072 + grp * 128 + r * 8 + g * 4) * 4u, 0, 16);
            }
        }
        int xrd[2], wrd[2];
        { const int q4 = (lane >> 2) & 3, p4 = lane & 3, trow = 8 * g + q4, f = ((trow >> 1) & 1) | (((trow >> 3) & 1) << 1);
#pragma unroll
          for (int s = 0; s < 2; ++s) { xrd[s] = (64 * wr + r) * 128 + (((4 * s + g) ^ ((r >> 1) & 7)) * 16); wrd[s] = 16384 + trow * 128 + (((2 * wc + s) ^ f) * 32) + (p4 >> 1) * 16 + (p4 & 1) * 8; } }
        f32x4 acc[4][6];
#pragma unroll
        for (int m = 0; m < 4; ++m)
#pragma unroll
            for (int n = 0; n < 6; ++n) acc[m][n] = (f32x4){0.f, 0.f, 0.f, 0.f};
        bf16x8 xa[4], xb[4]; s16x4 wa[12], wb[12];
#define K1_RW(i, W, sl, s) W[i] = trd(lds + (sl) + wrd[((i) >> 1) & 1] + ((i) >> 2) * 8192 + (s) * 4096 + ((i) & 1) * 512)
#define K1_RX(i, X, sl, s) X[i] = ldf(lds + (sl) + xrd[s] + (i) * 2048)
#define K1_RD(i, X, W, sl, s) do { if ((i) == 2) K1_RX(0, X, sl, s); else if ((i) == 7) K1_RX(1, X, sl, s); else if ((i) == 12) K1_RX(2, X, sl, s); else if ((i) == 15) K1_RX(3, X, sl, s); \
        else K1_RW((i) < 2 ? (i) : (i) < 7 ? (i) - 1 : (i) < 12 ? (i) - 2 : (i) - 3, W, sl, s); } while (0)
#define K1_MM(i, X, W) do { const int m_ = (i) / 6, n_ = (i) % 6; const bf16x8 wf_ = cat8(W[2 * n_], W[2 * n_ + 1]); \
        acc[m_][n_] = (n_ < 4) ? MFMA16(wf_, X[m_], acc[m_][n_]) : MFMA16(X[m_], wf_, acc[m_][n_]); } while (0)
#define K1_HALF(XM, WM, XR, WR, sl, s, DOMMA) do { \
        _Pragma("unroll") for (int i_ = 0; i_ < 24; ++i_) { \
            if (DOMMA) K1_MM(i_, XM, WM); \
            if (i_ < 16) K1_RD(i_, XR, WR, sl, s); \
            SCHED0(); } } while (0)
        WAITBAR(0);
        if (SYNC && tid == 0) flag_set(ws, FL_0 + b, tag);
        STAMP_AT(1);
        K1_HALF(xb, wb, xa, wa, 0, 0, false);
        K1_HALF(xa, wa, xb, wb, 0, 1, true);
        int cur = K1_SLOT;
#pragma unroll 1
        for (int t = 1; t < NT; ++t) {
            LBAR();
            K1_HALF(xb, wb, xa, wa, cur, 0, true);
            K1_HALF(xa, wa, xb, wb, cur, 1, true);
            cur = (cur == 2 * K1_SLOT) ? 0 : cur + K1_SLOT;
        }
#pragma unroll
        for (int i = 0; i < 24; ++i) K1_MM(i, xb, wb);
        STAMP_AT(2);
        LBAR();
        const int head = 2 * p + wc;
        const srd_t srq = mk_srd(ws + WS_Q, 12u << 20);
        f32x4 cq[4]; float cv[2];
#pragma unroll
        for (int n = 0; n < 4; ++n) cq[n] = *(LAS const f32x4*)(csl + (n >> 1) * 64 + wc * 32 + (n & 1) * 16 + 4 * g);
#pragma unroll
        for (int n = 0; n < 2; ++n) cv[n] = csl[128 + wc * 32 + n * 16 + r];
        const int rho = 16 * (r >> 3) + 4 * ((r >> 2) & 1) + (r & 3);
        f32x4 vv[2][2];
#pragma unroll
        for (int m = 0; m < 4; ++m) {
            const int lrow = wr * 64 + m * 16, row0 = rt * 128 + lrow;
            const unsigned blk = ((unsigned)head * 64 + (row0 >> 5)) * 2048u + (g >> 1) * 1024 + (g & 1) * 512;
            const f32x2 st = stat[lrow + r];
            const float qs = st.y * QSCALE;
            st16(srq, (4u << 20) + blk + (rho + (m & 1) * 8) * 16, pk8((acc[m][2] - st.x * cq[2]) * st.y, (acc[m][3] - st.x * cq[3]) * st.y));
            const f32x4 s01 = *(LAS const f32x4*)(stat + lrow + 4 * g), s23 = *(LAS const f32x4*)(stat + lrow + 4 * g + 2);
            const f32x4 mean4 = {s01[0], s01[2], s23[0], s23[2]}, rstd4 = {s01[1], s01[3], s23[1], s23[3]};
#pragma unroll
            for (int n = 0; n < 2; ++n) vv[m & 1][n] = (acc[m][4 + n] - mean4 * cv[n]) * rstd4;
            if (m & 1) {
#pragma unroll
                for (int n = 0; n < 2; ++n) st16(srq, (8u << 20) + blk + (16 * n + r) * 16, pk8(vv[0][n], vv[1][n]));
            }
            st16(srq, blk + ((m & 1) * 16 + r) * 16, pk8((acc[m][0] - st.x * cq[0]) * qs, (acc[m][1] - st.x * cq[1]) * qs));
        }
    }
    WAITBAR(0);
    if (SYNC && tid == 0) flag_set(ws, FL_1 + p * 16 + rt, tag);
}

constexpr int K2_SLOT = 32768, K2_SIDE = 3 * K2_SLOT;
struct K2Side { srd_t src, srp, sro; unsigned cv, cs0, ps0; };
template <int KH>
__device__ __forceinline__ void attn_fast(LAS char* lds, srd_t srk, srd_t srv, unsigned hbase, const K2Side& S, int w, int lane, const bf16x8 (&qf)[2][2], f32x16 (&o)[2], f32x4 (&lacc)[2], f32x4& cs) {
    const int tl = w & 3;
    const unsigned lds0 = (unsigned)(uintptr_t)lds;
    const unsigned lv = lane * 16;
    const unsigned s0 = hbase + (unsigned)(KH * 32 + tl) * 2048u, d0 = lds0 + KH * 16384 + tl * 2048;
    const int rd0 = KH * 16384 + lane * 16;
    bf16x8 ones, sel;
    { const short v = ((lane & 15) == ((lane >> 4) & 1)) ? (short)0x3f80 : (short)0;
#pragma unroll
      for (int j = 0; j < 8; ++j) { ones[j] = v; sel[j] = ((lane & 15) == j) ? (short)0x3f80 : (short)0; } }
    const unsigned sd = lds0 + K2_SIDE + w * 1024;
#define K2_DMA(k, rd, fr) do { \
        if ((k) < 4) dma16c(d0 + (fr) + ((k) >> 1) * 8192 + ((k) & 1) * 1024, lv, (k) < 2 ? srk : srv, s0 + ((rd) + 2) * 8192 + ((k) & 1) * 1024); \
        else if ((k) == 4) dma16(sd + ((rd) & 1) * 8192, S.cv, S.src, S.cs0 + (rd) * (4 * FF * 2)); \
        else dma16(sd + 16384, lv, (rd) < 4 ? S.srp : S.sro, (rd) < 4 ? S.ps0 + (rd) * 1024 : (rd) == 4 ? (S.ps0 >> 2) : 0x7ff00000u); } while (0)
#define K2_HOOK(gi, rd, fr) do { if (((gi) >> 1) < 6 && ((gi) & 1) == KH) K2_DMA((gi) >> 1, rd, fr); } while (0)
    f32x16 sa[2], sb[2];
    const f32x16 zero16 = {};
    bf16x8 k0 = ldf(lds + rd0), k1 = ldf(lds + rd0 + 1024), v0, v1;
#pragma unroll
    for (int qt = 0; qt < 2; ++qt) { sa[qt] = MFMA32(k0, qf[qt][0], zero16); sa[qt] = MFMA32(k1, qf[qt][1], sa[qt]); }
    k0 = ldf(lds + rd0 + 2048);
    u32x4 pa = {0u, 0u, 0u, 0u}, pb = {0u, 0u, 0u, 0u}; float e6 = 0.f, e7 = 0.f;
    v1 = k1;
    bf16x8 csd = {};
#define EX2(x) __builtin_amdgcn_exp2f(x)
#define K2_GROUP(S_, B, PC, PP, QK, VP, QP, RD, gi) do { \
        QK; PP[3] = pk_bf16(e6, e7); SCHED0(); \
        const float x0 = EX2(S_[B + 0]), x1 = EX2(S_[B + 1]), x2 = EX2(S_[B + 2]); RD; SCHED0(); \
        o[QP] = MFMA32(VP, __builtin_bit_cast(bf16x8, PP), o[QP]); SCHED0(); \
        const float x3 = EX2(S_[B + 3]), x4 = EX2(S_[B + 4]), x5 = EX2(S_[B + 5]); PC[0] = pk_bf16(x0, x1); SCHED0(); \
        lacc[QP] = MFMA16(ones, __builtin_bit_cast(bf16x8, PP), lacc[QP]); K2_HOOK(gi, rd, fr); SCHED0(); \
        e6 = EX2(S_[B + 6]); e7 = EX2(S_[B + 7]); PC[1] = pk_bf16(x2, x3); PC[2] = pk_bf16(x4, x5); SCHED0(); } while (0)
#define K2_TILE(SC, SN, kn1, kn2, va, ti) do { \
        K2_GROUP(SC[0], 0, pa, pb, SN[0] = MFMA32(k0, qf[0][0], zero16), v1, 1, v0 = ldf(va), 4 * (ti)); \
        K2_GROUP(SC[1], 0, pb, pa, SN[1] = MFMA32(k0, qf[1][0], zero16), v0, 0, k1 = ldf((kn1) + 1024), 4 * (ti) + 1); \
        K2_GROUP(SC[0], 8, pa, pb, SN[0] = MFMA32(k1, qf[0][1], SN[0]), v0, 1, v1 = ldf((va) + 1024), 4 * (ti) + 2); \
        K2_GROUP(SC[1], 8, pb, pa, SN[1] = MFMA32(k1, qf[1][1], SN[1]), v1, 0, k0 = ldf(kn2), 4 * (ti) + 3); } while (0)
    int cur = 0, fr = 2 * K2_SLOT;
#pragma unroll 1
    for (int rd = 0; rd < 8; ++rd) {
        const int nx = (cur == 2 * K2_SLOT) ? 0 : cur + K2_SLOT;
        if (rd > 0) WAITBAR(0);
        LAS const char* pc = lds + cur + rd0; LAS const char* pn = lds + nx + rd0;
        K2_TILE(sa, sb, pc + 2048, pc + 4096, pc + 8192, 0);
        K2_TILE(sb, sa, pc + 4096, pc + 6144, pc + 8192 + 2048, 1);
        K2_TILE(sa, sb, pc + 6144, pn, pc + 8192 + 4096, 2);
        csd = ldf(lds + K2_SIDE + ((rd - 1) & 1) * 8192 + w * 1024 + lane * 16);
        if (rd == 0) csd = (bf16x8){};
        K2_TILE(sb, sa, pn, pn + 2048, pc + 8192 + 6144, 3);
        cs = MFMA16(sel, csd, cs);
        fr = cur; cur = nx;
    }
    pb[3] = pk_bf16(e6, e7);
    o[1] = MFMA32(v1, __builtin_bit_cast(bf16x8, pb), o[1]);
    lacc[1] = MFMA16(ones, __builtin_bit_cast(bf16x8, pb), lacc[1]);
    WAITBAR(0);
    cs = MFMA16(sel, ldf(lds + K2_SIDE + 8192 + w * 1024 + lane * 16), cs);
#undef K2_GROUP
#undef K2_TILE
#undef K2_DMA
#undef K2_HOOK
}
#define K2_STAGE(rd, slot) do { dma16c(d0 + (slot), lv, srk, s0 + (rd) * 8192); dma16c(d0 + (slot) + 1024, lv, srk, s0 + (rd) * 8192 + 1024); \
        dma16c(d0 + (slot) + 8192, lv, srv, s0 + (rd) * 8192); dma16c(d0 + (slot) + 9216, lv, srv, s0 + (rd) * 8192 + 1024); } while (0)
__device__ __forceinline__ void attn_safe(LAS char* lds, srd_t srk, srd_t srv, unsigned hbase, const bf16x8 (&qf)[2][2], int w, int lane, f32x16 (&o)[2], float (&mrun)[2], float (&lrun)[2]) {
    const int kh = w >> 2, tl = w & 3;
    const unsigned lds0 = (unsigned)(uintptr_t)lds;
    const unsigned lv = lane * 16;
    const unsigned s0 = hbase + (unsigned)(kh * 32 + tl) * 2048u, d0 = lds0 + kh * 16384 + tl * 2048;
    const int rd0 = kh * 16384 + lane * 16;
    const f32x16 zero16 = {};
#pragma unroll 1
    for (int rd = 0; rd < 8; ++rd) {
        K2_STAGE(rd, 0);
        WAITBAR(0);
#pragma unroll 1
        for (int i = 0; i < 4; ++i) {
            LAS const char* pk = lds + rd0 + i * 2048;
            const bf16x8 k0 = ldf(pk), k1 = ldf(pk + 1024), v0 = ldf(pk + 8192), v1 = ldf(pk + 9216);
#pragma unroll
            for (int qt = 0; qt < 2; ++qt) {
                f32x16 sc = MFMA32(k0, qf[qt][0], zero16); sc = MFMA32(k1, qf[qt][1], sc);
                float mx = sc[0];
#pragma unroll
                for (int j = 1; j < 16; ++j) mx = fmaxf(mx, sc[j]);
                mx = fmaxf(mx, __shfl_xor(mx, 32));
                const float mn = fmaxf(mrun[qt], mx);
                const float ms = (mn == -INFINITY) ? 0.f : mn;
                const float al = __builtin_amdgcn_exp2f(mrun[qt] - ms);
                mrun[qt] = mn;
                u32x4 p0, p1;
#pragma unroll
                for (int j = 0; j < 4; ++j) { p0[j] = pk_bf16(__builtin_amdgcn_exp2f(sc[2 * j] - ms), __builtin_amdgcn_exp2f(sc[2 * j + 1] - ms));
                                              p1[j] = pk_bf16(__builtin_amdgcn_exp2f(sc[8 + 2 * j] - ms), __builtin_amdgcn_exp2f(sc[9 + 2 * j] - ms)); }
                float s_ = 0.f;
#pragma unroll
                for (int j = 0; j < 4; ++j) { const unsigned a = p0[j], c = p1[j]; s_ += bf_lo(a) + bf_hi(a); s_ += bf_lo(c) + bf_hi(c); }
                lrun[qt] = lrun[qt] * al + s_;
#pragma unroll
                for (int j = 0; j < 16; ++j) o[qt][j] *= al;
                o[qt] = MFMA32(v0, __builtin_bit_cast(bf16x8, p0), o[qt]); o[qt] = MFMA32(v1, __builtin_bit_cast(bf16x8, p1), o[qt]);
            }
        }
        WAITBAR(0);
    }
}

template <bool SYNC>
__device__ __forceinline__ void phase2(LAS char* lds, const Params& P, unsigned tag STAMP_ARGS) {
    const int tid = threadIdx.x, lane = tid & 63, w = __builtin_amdgcn_readfirstlane(tid >> 6);
    const int b = blockIdx.x, xcd = b & 7, loc = b >> 3;
    const int head = xcd * 4 + (loc & 3), qb = loc >> 2;
    char* ws = P.ws;
    K2Side S;
    const int cgrp = b % 56, rq = b / 56;
    const bool do_cs = b < 224;
    S.src = mk_srd(cgrp < 28 ? P.wg : P.wu, DM * FF * 2);
    S.cv = (unsigned)(lane >> 4) * (FF * 2) + (lane & 15) * 16;
    S.cs0 = do_cs ? (unsigned)(rq * 256 + w * 32) * (FF * 2) + (unsigned)(cgrp < 28 ? cgrp : cgrp - 28) * 256u : 0x7ff00000u;
    S.srp = mk_srd(P.wd, FF * DM * 2); S.sro = mk_srd(P.wo, DM * DM * 2);
    S.ps0 = (unsigned)(b * 8 + w) * 4096u;
    const srd_t srk = mk_srd(ws + WS_K, SEQ * DM * 2), srv = mk_srd(ws + WS_V, SEQ * DM * 2);
    const unsigned hbase = (unsigned)head * 64u * 2048u;
    const int kh = w >> 2;
    if (SYNC) { if (w == 0) flags_wait(ws, lane < 16 ? FL_1 + (head >> 1) * 16 + lane : -1, tag); LBAR(); }
    bf16x8 qf[2][2];
    {
        const unsigned lds0 = (unsigned)(uintptr_t)lds, lv = lane * 16;
        const unsigned s0 = hbase + (unsigned)(kh * 32 + (w & 3)) * 2048u, d0 = lds0 + kh * 16384 + (w & 3) * 2048;
        K2_STAGE(0, 0);
        const srd_t srq = mk_srd(ws + WS_Q, SEQ * DM * 2);
        const unsigned qo = ((unsigned)head * 64 + qb * 8 + (w & 3) * 2) * 2048u + lane * 16;
#pragma unroll
        for (int qt = 0; qt < 2; ++qt)
#pragma unroll
            for (int s = 0; s < 2; ++s) qf[qt][s] = __builtin_bit_cast(bf16x8, ldc16(srq, qo + qt * 2048 + s * 1024));
        K2_STAGE(1, K2_SLOT);
    }
    WAITBAR(0);
    STAMP_AT(1);
    f32x16 o[2] = {}; f32x4 lacc[2] = {}; f32x4 cs = {0.f, 0.f, 0.f, 0.f};
    if (kh == 0) attn_fast<0>(lds, srk, srv, hbase, S, w, lane, qf, o, lacc, cs);
    else attn_fast<1>(lds, srk, srv, hbase, S, w, lane, qf, o, lacc, cs);
    STAMP_AT(2);
    LAS float* ob = (LAS float*)lds;
    LAS float* lb = (LAS float*)(lds + 32768);
    LAS unsigned* flag = (LAS unsigned*)(lds + 32768 + 4096);
    LAS float* cb = (LAS float*)(lds + 40960);
    const int hi = lane >> 5, q32 = lane & 31;
    const srd_t sro = mk_srd(ws + WS_O, SEQ * DM * 2);
    const unsigned dst = ((unsigned)(qb * 16 + (w & 3) * 4 + kh * 2 + (q32 >> 4)) * 32 + head) * 1024u + (hi * 16 + (q32 & 15)) * 16;
    bool bad;
    {
        if (tid == 0) flag[0] = 0u;
        if (lane < 16) { lb[w * 64 + lane] = lacc[0][0]; lb[w * 64 + 16 + lane] = lacc[0][1]; lb[w * 64 + 32 + lane] = lacc[1][0]; lb[w * 64 + 48 + lane] = lacc[1][1]; }
#pragma unroll
        for (int j = 0; j < 4; ++j) { const f32x4 v = kh ? (f32x4){o[0][4 * j], o[0][4 * j + 1], o[0][4 * j + 2], o[0][4 * j + 3]} : (f32x4){o[1][4 * j], o[1][4 * j + 1], o[1][4 * j + 2], o[1][4 * j + 3]};
            *(LAS f32x4*)(ob + ((w * 4 + j) * 64 + lane) * 4) = v; }
        if (lane < 32) *(LAS f32x4*)(cb + w * 128 + (lane & 15) * 8 + (lane >> 4) * 4) = cs;
        LBAR();
        const int pw = w ^ 4;
        f32x16 of;
#pragma unroll
        for (int j = 0; j < 4; ++j) { const f32x4 v = *(LAS const f32x4*)(ob + ((pw * 4 + j) * 64 + lane) * 4);
#pragma unroll
            for (int e = 0; e < 4; ++e) of[4 * j + e] = (kh ? o[1][4 * j + e] : o[0][4 * j + e]) + v[e]; }
        const float l = lb[w * 64 + kh * 32 + q32] + lb[pw * 64 + kh * 32 + q32];
        bool ok = (l >= 0x1p-60f) && (l <= 0x1p+80f);
#pragma unroll
        for (int j = 0; j < 16; ++j) ok = ok && (fabsf(of[j]) <= 0x1p+120f);
        if (!__all(ok)) { if (lane == 0) flag[0] = 1u; }
        if (do_cs && tid < 128) {
            float s = 0.f;
#pragma unroll
            for (int i = 0; i < 8; ++i) s += cb[i * 128 + tid];
            __hip_atomic_store((gu32*)(ws + WS_CS) + (rq * 7168 + cgrp * 128 + tid), __float_as_uint(s), RLX_AGENT);
        }
        LBAR();
        bad = flag[0] != 0u;
        if (!bad) {
            const float rl = 1.0f / l;
#pragma unroll
            for (int c = 0; c < 2; ++c) {
                u32x4 ov; ov.x = pk_bf16(of[8 * c + 0] * rl, of[8 * c + 1] * rl); ov.y = pk_bf16(of[8 * c + 2] * rl, of[8 * c + 3] * rl);
                ov.z = pk_bf16(of[8 * c + 4] * rl, of[8 * c + 5] * rl); ov.w = pk_bf16(of[8 * c + 6] * rl, of[8 * c + 7] * rl);
                st16(sro, dst + c * 512, ov);
            }
        }
    }
    if (bad) {
        WAITBAR(0);
        o[0] = (f32x16){}; o[1] = (f32x16){}; float mrun[2] = {-INFINITY, -INFINITY}, lrun[2] = {0.f, 0.f};
        attn_safe(lds, srk, srv, hbase, qf, w, lane, o, mrun, lrun);
        LAS float* mb = lb + 512;
#pragma unroll
        for (int qt = 0; qt < 2; ++qt) { const float lt = lrun[qt] + __shfl_xor(lrun[qt], 32); if (lane < 32) { lb[w * 64 + qt * 32 + lane] = lt; mb[w * 64 + qt * 32 + lane] = mrun[qt]; } }
#pragma unroll
        for (int j = 0; j < 16; ++j) ob[(w * 16 + j) * 64 + lane] = kh ? o[0][j] : o[1][j];
        WAITBAR(0);
        const int pw = w ^ 4;
        const float ma = mb[w * 64 + kh * 32 + q32], mp = mb[pw * 64 + kh * 32 + q32];
        const float mn = fmaxf(ma, mp), ms = (mn == -INFINITY) ? 0.f : mn;
        const float fa = __builtin_amdgcn_exp2f(ma - ms), fp = __builtin_amdgcn_exp2f(mp - ms);
        const float l = lb[w * 64 + kh * 32 + q32] * fa + lb[pw * 64 + kh * 32 + q32] * fp;
        const float rl = 1.0f / l;
        float of[16];
#pragma unroll
        for (int j = 0; j < 16; ++j) of[j] = ((kh ? o[1][j] : o[0][j]) * fa + ob[(pw * 16 + j) * 64 + lane] * fp) * rl;
#pragma unroll
        for (int c = 0; c < 2; ++c) {
            u32x4 ov; ov.x = pk_bf16(of[8 * c + 0], of[8 * c + 1]); ov.y = pk_bf16(of[8 * c + 2], of[8 * c + 3]);
            ov.z = pk_bf16(of[8 * c + 4], of[8 * c + 5]); ov.w = pk_bf16(of[8 * c + 6], of[8 * c + 7]);
            st16(sro, dst + c * 512, ov);
        }
    }
    WAITBAR(0);
    if (SYNC && tid == 0) flag_set(ws, FL_2 + qb * 32 + head, tag);
}
#undef K2_STAGE

constexpr int K3_SLOT = 24576, K3_NS = 6, K3_AUX = K3_NS * K3_SLOT;
template <int MODE, bool SYNC>
__device__ __forceinline__ void phase35(LAS char* lds, const Params& P, unsigned tag STAMP_ARGS) {
    constexpr int KD = MODE == 0 ? DM : FF, KT = KD / 32, NT = KD / 64;
    const unsigned lds0 = (unsigned)(uintptr_t)lds;
    const int tid = threadIdx.x, lane = tid & 63, w = __builtin_amdgcn_readfirstlane(tid >> 6), g = lane >> 4, r = lane & 15;
    const int b = blockIdx.x, xcd = b & 7, loc = b >> 3;
    const int rt = (xcd >> 1) * 4 + (loc & 3), ct = (xcd & 1) * 8 + (loc >> 2);
    char* ws = P.ws;
    if (w >= 4) {
        const int j = w - 4;
        const srd_t srx = mk_srd(ws + (MODE == 0 ? WS_O : WS_ACT), SEQ * KD * 2), srw = mk_srd(MODE == 0 ? P.wo : P.wd, KD * DM * 2);
        const unsigned xv = lane * 16, xs0 = (unsigned)(rt * 8 + 2 * j) * (KT * 1024u);
        const int wrow = lane >> 3, wch = (lane & 7) ^ (((wrow >> 1) & 3) << 1);
        const unsigned wv = (unsigned)wrow * 2048u + wch * 16, ws0 = (unsigned)ct * 128u + (unsigned)(16 * j) * 2048u;
        const unsigned ld0 = lds0 + j * 4096, ld1 = lds0 + 16384 + j * 2048;
#define K3_W(t, slot) do { dma16(ld1 + (slot), wv, srw, ws0 + (t) * (64 * 2048)); dma16(ld1 + (slot) + 1024, wv, srw, ws0 + 8 * 2048 + (t) * (64 * 2048)); } while (0)
#define K3_X(t, slot) do { dma16c(ld0 + (slot), xv, srx, xs0 + (t) * 2048); dma16c(ld0 + (slot) + 1024, xv, srx, xs0 + (t) * 2048 + 1024); \
        dma16c(ld0 + (slot) + 2048, xv, srx, xs0 + KT * 1024 + (t) * 2048); dma16c(ld0 + (slot) + 3072, xv, srx, xs0 + KT * 1024 + (t) * 2048 + 1024); } while (0)
        K3_W(0, 0); K3_W(1, K3_SLOT); K3_W(2, 2 * K3_SLOT); K3_W(3, 3 * K3_SLOT); K3_W(4, 4 * K3_SLOT);
        if (SYNC) LBAR();
        K3_X(0, 0); K3_X(1, K3_SLOT); K3_X(2, 2 * K3_SLOT);
        WAITBAR(8);  K3_X(3, 3 * K3_SLOT); K3_X(4, 4 * K3_SLOT); K3_X(5, 5 * K3_SLOT); K3_W(5, 5 * K3_SLOT);
        WAITBAR(18); K3_X(6, 0); K3_W(6, 0);
        WAITBAR(20); K3_X(7, K3_SLOT); K3_W(7, K3_SLOT);
        WAITBAR(22); K3_X(8, 2 * K3_SLOT); K3_W(8, 2 * K3_SLOT);
        int fr = 3 * K3_SLOT;
#pragma unroll 1
        for (int t = 4; t + K3_NS - 1 < NT; ++t) {
            WAITBAR(6 * (K3_NS - 2));
            K3_X(t + K3_NS - 1, fr); K3_W(t + K3_NS - 1, fr);
            fr = (fr == (K3_NS - 1) * K3_SLOT) ? 0 : fr + K3_SLOT;
        }
        WAITBAR(24); WAITBAR(18); WAITBAR(12); WAITBAR(6); WAITBAR(0);
        if (MODE == 0) LBAR();
    } else {
        const int wm = w >> 1, wn = w & 1;
        const int col0 = ct * 64 + wn * 32 + 4 * g;
        if (SYNC) { if (w == 0) flags_wait(ws, lane < 32 ? (MODE == 0 ? FL_2 : FL_4) + (rt >> 1) * 32 + lane : -1, tag); LBAR(); }
        u32x4 res[4];
#pragma unroll
        for (int m = 0; m < 4; ++m) {
            const int mrow = rt * 128 + wm * 64 + m * 16;
            if (MODE == 0) { const u32x2 a = *(const u32x2*)(P.x + (size_t)(mrow + r) * DM + col0), c = *(const u32x2*)(P.x + (size_t)(mrow + r) * DM + col0 + 16); res[m] = (u32x4){a.x, a.y, c.x, c.y}; }
            else res[m] = ldc16(mk_srd(ws + WS_H, SEQ * DM * 2), ((unsigned)(mrow >> 4) * 32 + ct * 2 + wn) * 1024u + lane * 16);
        }
        const int xrd = wm * 8192 + lane * 16;
        const int q4 = (lane >> 2) & 3, p4 = lane & 3;
        const int trow = (MODE == 0) ? (4 * (g & 1) + 16 * (g >> 1) + q4) : (4 * g + q4);
        constexpr int TR2 = (MODE == 0) ? 8 * 128 : 16 * 128;
        int wrd[2];
#pragma unroll
        for (int n = 0; n < 2; ++n) wrd[n] = 16384 + trow * 128 + (((wn * 2 + n) ^ ((trow >> 1) & 3)) * 32) + (p4 >> 1) * 16 + (p4 & 1) * 8;
        f32x4 acc[4][2];
#pragma unroll
        for (int m = 0; m < 4; ++m) { acc[m][0] = (f32x4){0.f, 0.f, 0.f, 0.f}; acc[m][1] = (f32x4){0.f, 0.f, 0.f, 0.f}; }
        bf16x8 xa[4], xb[4]; s16x4 wa[4], wb[4];
#define K3_RD(i, X, W, sl, s) do { if ((i) == 2) X[0] = ldf(lds + (sl) + xrd + (s) * 1024); else if ((i) >= 5) X[(i) - 4] = ldf(lds + (sl) + xrd + ((i) - 4) * 2048 + (s) * 1024); \
        else { const int k_ = (i) < 2 ? (i) : (i) - 1; W[k_] = trd(lds + (sl) + wrd[k_ >> 1] + (k_ & 1) * TR2 + (s) * 4096); } } while (0)
#define K3_MM(i, X, W) do { const int m_ = (i) >> 1, n_ = (i) & 1; acc[m_][n_] = MFMA16(cat8(W[2 * n_], W[2 * n_ + 1]), X[m_], acc[m_][n_]); } while (0)
#define K3_HALF(XM, WM, XR, WR, sl, s, DOMMA) do { _Pragma("unroll") for (int i_ = 0; i_ < 8; ++i_) { if (DOMMA) K3_MM(i_, XM, WM); K3_RD(i_, XR, WR, sl, s); SCHED0(); } } while (0)
        LBAR();
        STAMP_AT(1);
        K3_HALF(xb, wb, xa, wa, 0, 0, false);
        K3_HALF(xa, wa, xb, wb, 0, 1, true);
        int cur = K3_SLOT;
#pragma unroll 1
        for (int t = 1; t < NT; ++t) {
            LBAR();
            K3_HALF(xb, wb, xa, wa, cur, 0, true);
            K3_HALF(xa, wa, xb, wb, cur, 1, true);
            cur = (cur == (K3_NS - 1) * K3_SLOT) ? 0 : cur + K3_SLOT;
        }
#pragma unroll
        for (int i = 0; i < 8; ++i) K3_MM(i, xb, wb);
        STAMP_AT(2);
        if (MODE == 0) {
            LAS f32x2* sb = (LAS f32x2*)(lds + K3_AUX);
            const srd_t srh = mk_srd(ws + WS_H, SEQ * DM * 2);
#pragma unroll
            for (int m = 0; m < 4; ++m) {
                const int mrow = rt * 128 + wm * 64 + m * 16;
                const f32x4 a = {bf_lo(res[m].x) + acc[m][0][0], bf_hi(res[m].x) + acc[m][0][1], bf_lo(res[m].y) + acc[m][0][2], bf_hi(res[m].y) + acc[m][0][3]};
                const f32x4 c = {bf_lo(res[m].z) + acc[m][1][0], bf_hi(res[m].z) + acc[m][1][1], bf_lo(res[m].w) + acc[m][1][2], bf_hi(res[m].w) + acc[m][1][3]};
                const u32x4 hv = pk8(a, c);
                st16(srh, ((unsigned)(mrow >> 4) * 32 + ct * 2 + wn) * 1024u + lane * 16, hv);
                float s = 0.f, q = 0.f;
#pragma unroll
                for (int j = 0; j < 4; ++j) { const unsigned d = hv[j]; const float u = bf_lo(d), v = bf_hi(d); s += u + v; q += u * u + v * v; }
                s += __shfl_xor(s, 16); s += __shfl_xor(s, 32); q += __shfl_xor(q, 16); q += __shfl_xor(q, 32);
                if (g == 0) sb[(wm * 64 + m * 16 + r) * 2 + wn] = (f32x2){s, q};
            }
            LBAR();
            if (tid < 128) { const f32x2 a = sb[tid * 2] + sb[tid * 2 + 1];
                __hip_atomic_store((gu64*)(ws + WS_ST) + ((size_t)(rt * 128 + tid) * 16 + ct), ((unsigned long long)__float_as_uint(a[1]) << 32) | __float_as_uint(a[0]), RLX_AGENT); }
        } else {
#pragma unroll
            for (int m = 0; m < 4; ++m) {
                const int mrow = rt * 128 + wm * 64 + m * 16;
                unsigned a0 = pk_bf16(bf_lo(res[m].x) + acc[m][0][0], bf_hi(res[m].x) + acc[m][0][1]), a1 = pk_bf16(bf_lo(res[m].y) + acc[m][0][2], bf_hi(res[m].y) + acc[m][0][3]);
                unsigned b0 = pk_bf16(bf_lo(res[m].z) + acc[m][1][0], bf_hi(res[m].z) + acc[m][1][1]), b1 = pk_bf16(bf_lo(res[m].w) + acc[m][1][2], bf_hi(res[m].w) + acc[m][1][3]);
                const auto s0 = __builtin_amdgcn_permlane16_swap(b0, a0, false, false), s1 = __builtin_amdgcn_permlane16_swap(b1, a1, false, false);
                const u32x4 ov = {s0[0], s1[0], s0[1], s1[1]};
                const int c8 = ((g & 1) ? 0 : 16) + (g >> 1) * 8;
                *(u32x4*)(P.out + (size_t)(mrow + r) * DM + ct * 64 + wn * 32 + c8) = ov;
            }
        }
    }
    if (MODE == 0) {
        WAITBAR(0);
        if (SYNC && tid == 0) flag_set(ws, FL_3 + rt * 16 + ct, tag);
    }
}

constexpr int K4_SLOT = 30720, K4_NS = 4, K4_STAT = K4_NS * K4_SLOT, K4_CS = K4_STAT + 2048;
struct K4Dma { srd_t srx, srg, sru; unsigned xv, gv, xs0, gs0, ld0, ld1; };
#define K4_DMA(i, t, slot) do { if ((i) < 2) dma16c(D.ld0 + (slot) + (i) * 1024, D.xv, D.srx, D.xs0 + (t) * 1024 + (i) * 32768); \
        else dma16(D.ld1 + (slot) + ((i) - 2) * 7168, D.gv, (i) == 2 ? D.srg : D.sru, D.gs0 + (t) * (32 * FF * 2)); } while (0)
template <int NJ, int J0, bool HASW>
__device__ __forceinline__ void k4_body(LAS char* lds, const K4Dma& D, char* ws, int w, int lane, int rt, int ct STAMP_ARGS) {
    const int wm = w & 3, g = lane >> 4, r = lane & 15;
    constexpr int NP = HASW ? 4 : 2;
    const int xrd = wm * 4096 + lane * 16;
    const int q4 = (lane >> 2) & 3, p4 = lane & 3;
    const int wrd = 16384 + (4 * g + q4) * 224 + J0 * 32 + (p4 >> 1) * 16 + (p4 & 1) * 8;
    f32x4 ag[4][NJ], au[4][NJ];
#pragma unroll
    for (int m = 0; m < 4; ++m)
#pragma unroll
        for (int j = 0; j < NJ; ++j) { ag[m][j] = (f32x4){0.f, 0.f, 0.f, 0.f}; au[m][j] = (f32x4){0.f, 0.f, 0.f, 0.f}; }
    bf16x8 xa[4], xb[4]; s16x4 wgf[2 * NJ], wuf[2 * NJ];
#define K4_RW(i, W, sl, o) W[i] = trd(lds + (sl) + wrd + (o) + ((i) >> 1) * 32 + ((i) & 1) * (16 * 224))
#define K4_RX(i, X, sl) X[i] = ldf(lds + (sl) + xrd + (i) * 1024)
#define K4_MM(i, A, X, W) do { const int j_ = (i) >> 2, m_ = (i) & 3; A[m_][j_] = MFMA16(cat8(W[2 * j_], W[2 * j_ + 1]), X[m_], A[m_][j_]); } while (0)
#define K4_RD1(i, XC, sl) do { if ((i) == 0) K4_RX(0, XC, sl); else if ((i) < 3) K4_RW((i) - 1, wgf, sl, 0); else if ((i) < 6) K4_RX((i) - 2, XC, sl); else if ((i) < 4 + 2 * NJ) K4_RW((i) - 4, wgf, sl, 0); } while (0)
#define K4_H1(XC, XP, DOMMA, DODMA, t, sl, fsl) do { \
        _Pragma("unroll") for (int i_ = 0; i_ < 4 * NJ; ++i_) { \
            if (DOMMA) K4_MM(i_, au, XP, wuf); \
            K4_RD1(i_, XC, sl); \
            if (DODMA) { if (i_ == (NJ == 4 ? 13 : 11)) K4_DMA(0, t, fsl); } \
            SCHED0(); } } while (0)
#define K4_H2(XC, sl, DODMA, t, fsl) do { \
        _Pragma("unroll") for (int i_ = 0; i_ < 4 * NJ; ++i_) { \
            K4_MM(i_, ag, XC, wgf); \
            if (i_ < 2 * NJ) K4_RW(i_, wuf, sl, 7168); \
            if (DODMA) { if (NJ == 4) { if (i_ == 8) K4_DMA(1, t, fsl); if (i_ == 11 && NP == 4) K4_DMA(2, t, fsl); if (i_ == 14 && NP == 4) K4_DMA(3, t, fsl); } \
                         else { if (i_ == 6) K4_DMA(1, t, fsl); if (i_ == 8 && NP == 4) K4_DMA(2, t, fsl); if (i_ == 10 && NP == 4) K4_DMA(3, t, fsl); } } \
            SCHED0(); } } while (0)
    constexpr int NT = DM / 32;
    WAITBAR(4);
    STAMP_AT(1);
    K4_H1(xa, xb, false, true, 3, 0, 3 * K4_SLOT);
    K4_H2(xa, 0, true, 3, 3 * K4_SLOT);
    int cur = K4_SLOT, fr = 0;
#define K4_ADV() do { fr = (fr == (K4_NS - 1) * K4_SLOT) ? 0 : fr + K4_SLOT; cur = (cur == (K4_NS - 1) * K4_SLOT) ? 0 : cur + K4_SLOT; } while (0)
    WAITBAR(2 + NP); K4_H1(xb, xa, true, true, 4, cur, fr); K4_H2(xb, cur, true, 4, fr); K4_ADV();
    int t = 2;
#pragma unroll 1
    for (; t + 4 < NT; t += 2) {
        WAITBAR(2 * NP); K4_H1(xa, xb, true, true, t + 3, cur, fr); K4_H2(xa, cur, true, t + 3, fr); K4_ADV();
        WAITBAR(2 * NP); K4_H1(xb, xa, true, true, t + 4, cur, fr); K4_H2(xb, cur, true, t + 4, fr); K4_ADV();
    }
    WAITBAR(2 * NP); K4_H1(xa, xb, true, true, t + 3, cur, fr); K4_H2(xa, cur, true, t + 3, fr); K4_ADV();
    WAITBAR(2 * NP); K4_H1(xb, xa, true, false, 0, cur, fr); K4_H2(xb, cur, false, 0, fr); K4_ADV();
    WAITBAR(NP);     K4_H1(xa, xb, true, false, 0, cur, fr); K4_H2(xa, cur, false, 0, fr); K4_ADV();
    WAITBAR(0);      K4_H1(xb, xa, true, false, 0, cur, fr); K4_H2(xb, cur, false, 0, fr);
#pragma unroll
    for (int i = 0; i < 4 * NJ; ++i) K4_MM(i, au, xb, wuf);
    STAMP_AT(2);
    const LAS f32x2* st = (const LAS f32x2*)(lds + K4_STAT);
    const LAS float* cs = (const LAS float*)(lds + K4_CS);
    f32x2 ms[4];
#pragma unroll
    for (int m = 0; m < 4; ++m) ms[m] = st[wm * 64 + m * 16 + r];
    const srd_t sra = mk_srd(ws + WS_ACT, SEQ * FF * 2);
    const unsigned ap0 = (unsigned)(rt * 16 + wm * 4) * (112 * 1024) + lane * 16;
    const int ft0 = ct * 7 + J0;
    constexpr int LONE = (NJ == 3) ? (J0 == 4 ? 2 : 0) : -1;
    u32x2 keep[4];
#pragma unroll
    for (int j = 0; j < NJ; ++j) {
        const f32x4 cg = *(const LAS f32x4*)(cs + (J0 + j) * 16 + 4 * g), cu = *(const LAS f32x4*)(cs + 112 + (J0 + j) * 16 + 4 * g);
        const bool second = (LONE == 0) ? (j == 2) : ((j & 1) == 1);
#pragma unroll
        for (int m = 0; m < 4; ++m) {
            const float c1 = -LOG2E * ms[m].y;
            const f32x4 gz = (ag[m][j] - cg * ms[m].x) * c1;
            const f32x4 uu = (au[m][j] - cu * ms[m].x) * ms[m].y;
            float a[4];
#pragma unroll
            for (int e = 0; e < 4; ++e) a[e] = uu[e] * __builtin_amdgcn_rcpf(1.0f + __builtin_amdgcn_exp2f(gz[e]));
            u32x2 ov; ov.x = pk_bf16(a[0], a[1]); ov.y = pk_bf16(a[2], a[3]);
            const int ft = ft0 + j;
            if (j == LONE) st8(sra, ap0 + m * (112 * 1024) + (ft >> 1) * 1024 + (ft & 1) * 8, ov);
            else if (!second) keep[m] = ov;
            else st16(sra, ap0 + m * (112 * 1024) + (ft >> 1) * 1024, (u32x4){keep[m].x, keep[m].y, ov.x, ov.y});
        }
        SCHED0();
    }
}

template <bool SYNC>
__device__ __forceinline__ void phase4(LAS char* lds, const Params& P, unsigned tag STAMP_ARGS) {
    const unsigned lds0 = (unsigned)(uintptr_t)lds;
    const int tid = threadIdx.x, lane = tid & 63, w = __builtin_amdgcn_readfirstlane(tid >> 6);
    const int b = blockIdx.x, xcd = b & 7, loc = b >> 3;
    const int rt = (xcd >> 2) * 4 + (loc & 3), ct = (xcd & 3) * 8 + (loc >> 2);
    char* ws = P.ws;
    K4Dma D;
    D.srx = mk_srd(ws + WS_H, SEQ * DM * 2); D.srg = mk_srd(P.wg, DM * FF * 2); D.sru = mk_srd(P.wu, DM * FF * 2);
    D.xv = lane * 16; D.xs0 = (unsigned)(rt * 16 + w * 2) * 32768u;
    { const int ci = (w < 7 ? w : 6) * 64 + lane; const int row = ci / 14, c = ci % 14; D.gv = (unsigned)row * (FF * 2) + c * 16; }
    D.gs0 = (unsigned)ct * 224u; D.ld0 = lds0 + w * 2048; D.ld1 = lds0 + 16384 + w * 1024;
    if (w < 7) {
#pragma unroll
        for (int t = 0; t < 3; ++t) { K4_DMA(2, t, t * K4_SLOT); K4_DMA(3, t, t * K4_SLOT); }
    }
    if (SYNC) {
        if (w == 7) {
            int idx = -1;
            if (lane < 32) idx = FL_3 + rt * 32 + lane;
            else if (lane < 48) { const int k = lane - 32, gsel = k & 3, rq = k >> 2;
                const int c0 = (ct * 112 + ((gsel & 1) ? 111 : 0)) >> 7, bb = rq * 56 + (gsel >> 1) * 28 + c0;
                idx = FL_2 + (bb >> 5) * 32 + (bb & 7) * 4 + ((bb >> 3) & 3); }
            flags_wait(ws, idx, tag);
        }
        LBAR();
    }
    const int row = tid >> 1, hf = tid & 1;
    f32x4 sv[4]; float cv[4];
    { const char* sp = ws + WS_ST + ((size_t)(rt * 256 + row) * 16 + hf * 8) * 8;
      const int ci = tid < 224 ? tid : 0;
      const float* cp = (const float*)(ws + WS_CS) + (ci < 112 ? ct * 112 + ci : 3584 + ct * 112 + (ci - 112));
#pragma unroll
      for (int i = 0; i < 4; ++i) asm volatile("global_load_dwordx4 %0, %1, off offset:%2 sc1" : "=v"(sv[i]) : "v"(sp), "n"(i * 16));
#pragma unroll
      for (int i = 0; i < 4; ++i) asm volatile("global_load_dword %0, %1, off sc1" : "=v"(cv[i]) : "v"(cp + i * 7168)); }
#pragma unroll
    for (int t = 0; t < 3; ++t) { K4_DMA(0, t, t * K4_SLOT); K4_DMA(1, t, t * K4_SLOT); }
    asm volatile("s_waitcnt vmcnt(6)" : "+v"(sv[0]), "+v"(sv[1]), "+v"(sv[2]), "+v"(sv[3]), "+v"(cv[0]), "+v"(cv[1]), "+v"(cv[2]), "+v"(cv[3]));
    {
        float s = 0.f, q = 0.f;
#pragma unroll
        for (int i = 0; i < 4; ++i) { s += sv[i][0] + sv[i][2]; q += sv[i][1] + sv[i][3]; }
        s += __shfl_xor(s, 1); q += __shfl_xor(q, 1);
        const float mean = s * (1.0f / DM);
        const float var = fmaxf(q * (1.0f / DM) - mean * mean, 0.f);
        if (hf == 0) ((LAS f32x2*)(lds + K4_STAT))[row] = (f32x2){mean, 1.0f / sqrtf(var + LN_EPS)};
        if (tid < 224) ((LAS float*)(lds + K4_CS))[tid] = (cv[0] + cv[1]) + (cv[2] + cv[3]);
    }
    if ((ct & 1) == 0) {
        if (w < 4) k4_body<4, 0, true>(lds, D, ws, w, lane, rt, ct STAMP_PASS);
        else if (w < 7) k4_body<3, 4, true>(lds, D, ws, w, lane, rt, ct STAMP_PASS);
        else k4_body<3, 4, false>(lds, D, ws, w, lane, rt, ct STAMP_PASS);
    } else {
        if (w < 4) k4_body<3, 0, true>(lds, D, ws, w, lane, rt, ct STAMP_PASS);
        else if (w < 7) k4_body<4, 3, true>(lds, D, ws, w, lane, rt, ct STAMP_PASS);
        else k4_body<4, 3, false>(lds, D, ws, w, lane, rt, ct STAMP_PASS);
    }
    WAITBAR(0);
    if (SYNC && tid == 0) flag_set(ws, FL_4 + rt * 32 + ct, tag);
}

constexpr int LDS_BYTES = 160 * 1024;
__global__ __launch_bounds__(512, 2) void mega(Params P) {
    extern __shared__ __attribute__((aligned(16))) char smem_g[];
    LAS char* lds = (LAS char*)smem_g;
    char* ws = P.ws;
    const unsigned tag = (unsigned)__builtin_amdgcn_readfirstlane(__hip_atomic_load((gu32*)(ws + WS_FLG) + FL_EPOCH, RLX_AGENT)) + 1u;
    { STAMP_DECL STAMP_AT(0); phase1<true>(lds, P, tag STAMP_PASS); STAMP_OUT(1, 4); }
    { STAMP_DECL STAMP_AT(0); phase2<true>(lds, P, tag STAMP_PASS); STAMP_OUT(2, 4); }
    { STAMP_DECL STAMP_AT(0); phase35<0, true>(lds, P, tag STAMP_PASS); STAMP_OUT(3, 4); }
    { STAMP_DECL STAMP_AT(0); phase4<true>(lds, P, tag STAMP_PASS); STAMP_OUT(4, 4); }
    { STAMP_DECL STAMP_AT(0); phase35<1, true>(lds, P, tag STAMP_PASS); STAMP_OUT(5, 4); }
    if (blockIdx.x == 0 && threadIdx.x == 0) __hip_atomic_store((gu32*)(ws + WS_FLG) + FL_EPOCH, tag, RLX_AGENT);
}

extern "C" void kernel_launch(void* const* d_in, const int* in_sizes, int n_in, void* d_out, int out_size, void* d_ws, size_t ws_size, hipStream_t stream) {
    Params P;
    P.x = (const u16*)d_in[0]; P.wqkv = (const u16*)d_in[1]; P.wo = (const u16*)d_in[2];
    P.wg = (const u16*)d_in[4]; P.wu = (const u16*)d_in[5]; P.wd = (const u16*)d_in[6];
    P.out = (u16*)d_out; P.ws = (char*)d_ws;
    (void)hipFuncSetAttribute((const void*)mega, hipFuncAttributeMaxDynamicSharedMemorySize, LDS_BYTES);
    mega<<<256, 512, LDS_BYTES, stream>>>(P);
}
```

```cpp
#include <hip/hip_runtime.h>
#include <hip/hip_bf16.h>
#include <stdint.h>

#define LAS __attribute__((address_space(3)))
typedef unsigned short u16;
typedef short bf16x8 __attribute__((ext_vector_type(8)));
typedef short s16x4 __attribute__((ext_vector_type(4)));
typedef float f32x2 __attribute__((ext_vector_type(2)));
typedef float f32x4 __attribute__((ext_vector_type(4)));
typedef float f32x16 __attribute__((ext_vector_type(16)));
typedef unsigned u32x2 __attribute__((ext_vector_type(2)));
typedef unsigned u32x4 __attribute__((ext_vector_type(4)));
typedef __bf16 bf16x2_t __attribute__((ext_vector_type(2)));
typedef __amdgpu_buffer_rsrc_t srd_t;

constexpr int SEQ = 2048, DM = 1024, NH = 32, FF = 3584;
constexpr float LN_EPS = 1e-5f;
constexpr float QSCALE = 0.17677669529663687f * 1.4426950408889634f;
constexpr float LOG2E = 1.4426950408889634f;

constexpr size_t WS_Q = 0;
constexpr size_t WS_K = WS_Q + (4u << 20);
constexpr size_t WS_V = WS_K + (4u << 20);
constexpr size_t WS_O = WS_V + (4u << 20);
constexpr size_t WS_H = WS_O + (4u << 20);
constexpr size_t WS_ACT = WS_H + (4u << 20);
constexpr size_t WS_ST = WS_ACT + (size_t)SEQ * FF * 2;
constexpr size_t WS_CS = WS_ST + (size_t)SEQ * 16 * 8;
constexpr size_t WS_S1 = WS_CS + 4 * 7168 * 4;
constexpr size_t WS_C1 = WS_S1 + SEQ * 8;
constexpr size_t WS_FLG = WS_C1 + 16 * 3072 * 4;
constexpr size_t WS_XCH = WS_FLG + 16384;
constexpr size_t WS_END = WS_XCH + (8u << 20);
constexpr int FL_EPOCH = 0, FL_ERR = 32, FL_0 = 64, FL_1 = FL_0 + 256, FL_2 = FL_1 + 256, FL_3 = FL_2 + 256, FL_4 = FL_3 + 256, FL_5 = FL_4 + 256;

#define FRAG(mt, kt, KT) (((unsigned)(kt) * 128u + (unsigned)(mt)) * 1024u)
#define ST_AUX 16

__device__ __forceinline__ unsigned pk_bf16(float lo, float hi) { f32x2 v = {lo, hi}; bf16x2_t b = __builtin_convertvector(v, bf16x2_t); return __builtin_bit_cast(unsigned, b); }
__device__ __forceinline__ float bf_lo(unsigned u) { return __uint_as_float(u << 16); }
__device__ __forceinline__ float bf_hi(unsigned u) { return __uint_as_float(u & 0xffff0000u); }
__device__ __forceinline__ s16x4 trd(LAS const char* p) { return __builtin_bit_cast(s16x4, __builtin_amdgcn_ds_read_tr16_b64_v4i16((LAS s16x4*)p)); }
__device__ __forceinline__ bf16x8 cat8(s16x4 a, s16x4 b) { return (bf16x8){a[0], a[1], a[2], a[3], b[0], b[1], b[2], b[3]}; }
__device__ __forceinline__ bf16x8 ldf(LAS const char* p) { return *(LAS const bf16x8*)p; }
__device__ __forceinline__ srd_t mk_srd(const void* p, unsigned bytes) { return __builtin_amdgcn_make_buffer_rsrc((void*)p, (short)0, (int)bytes, 0x00020000); }
__device__ __forceinline__ void dma16(unsigned ldsa, unsigned voff, srd_t srd, unsigned soff) {
    asm volatile("s_mov_b32 m0, %0\n\ts_nop 0\n\tbuffer_load_dwordx4 %1, %2, %3 offen lds" :: "s"(ldsa), "v"(voff), "s"(srd), "s"(soff));
}
__device__ __forceinline__ void dma16c(unsigned ldsa, unsigned voff, srd_t srd, unsigned soff) {
    asm volatile("s_mov_b32 m0, %0\n\ts_nop 0\n\tbuffer_load_dwordx4 %1, %2, %3 offen sc1 lds" :: "s"(ldsa), "v"(voff), "s"(srd), "s"(soff));
}
__device__ __forceinline__ void st8(srd_t srd, unsigned off, u32x2 v) { __builtin_amdgcn_raw_buffer_store_b64(v, srd, off, 0, ST_AUX); }
__device__ __forceinline__ void st16(srd_t srd, unsigned off, u32x4 v) { __builtin_amdgcn_raw_buffer_store_b128(v, srd, off, 0, ST_AUX); }
__device__ __forceinline__ u32x4 pk8(const f32x4 a, const f32x4 b) { u32x4 o; o.x = pk_bf16(a[0], a[1]); o.y = pk_bf16(a[2], a[3]); o.z = pk_bf16(b[0], b[1]); o.w = pk_bf16(b[2], b[3]); return o; }
#define MFMA16(a, b, c) __builtin_amdgcn_mfma_f32_16x16x32_bf16(a, b, c, 0, 0, 0)
#define MFMA32(a, b, c) __builtin_amdgcn_mfma_f32_32x32x16_bf16(a, b, c, 0, 0, 0)
#define WAITVL(n) asm volatile("s_waitcnt vmcnt(%0) lgkmcnt(0)" :: "n"(n) : "memory")
#define WAITBAR(n) asm volatile("s_waitcnt vmcnt(%0) lgkmcnt(0)\n\ts_barrier" :: "n"(n) : "memory")
#define LBAR() asm volatile("s_waitcnt lgkmcnt(0)\n\ts_barrier" ::: "memory")
#define SCHED0() __builtin_amdgcn_sched_barrier(0)
#define WAITV(n) asm volatile("s_waitcnt vmcnt(%0)" :: "n"(n) : "memory")
#define RLX_AGENT __ATOMIC_RELAXED, __HIP_MEMORY_SCOPE_AGENT
typedef __attribute__((address_space(1))) unsigned gu32;
typedef __attribute__((address_space(1))) unsigned long long gu64;
struct Params { const u16 *x, *wqkv, *wo, *wg, *wu, *wd; u16* out; char* ws; };
__device__ __forceinline__ void flag_set(char* ws, int idx, unsigned tag) { __hip_atomic_store((gu32*)(ws + WS_FLG) + idx, tag, RLX_AGENT); }
__device__ __forceinline__ void flags_wait(char* ws, int idx, unsigned tag) {
    gu32* fl = (gu32*)(ws + WS_FLG);
    const unsigned long long t0 = __builtin_amdgcn_s_memrealtime();
    for (unsigned spins = 1;; ++spins) {
        const unsigned v = idx >= 0 ? __hip_atomic_load(fl + idx, RLX_AGENT) : tag;
        if (__all(v == tag)) break;
        if ((spins & 63u) == 0u) {
            if (__any(__hip_atomic_load(fl + FL_ERR, RLX_AGENT) == tag)) break;
            if (__builtin_amdgcn_s_memrealtime() - t0 > 2000000ull) { __hip_atomic_store(fl + FL_ERR, tag, RLX_AGENT); break; }
        }
        __builtin_amdgcn_s_sleep(1);
    }
    asm volatile("" ::: "memory");
}
template <int CTRL, int RM> __device__ __forceinline__ float dpp_add(float v) { return v + __int_as_float(__builtin_amdgcn_update_dpp(0, __float_as_int(v), CTRL, RM, 0xf, true)); }
__device__ __forceinline__ float sum32(float v) { v = dpp_add<0x111, 0xf>(v); v = dpp_add<0x112, 0xf>(v); v = dpp_add<0x114, 0xf>(v); v = dpp_add<0x118, 0xf>(v); return dpp_add<0x142, 0xa>(v); }
__device__ __forceinline__ u32x4 ldc16(srd_t srd, unsigned off) { return __builtin_amdgcn_raw_buffer_load_b128(srd, off, 0, 16); }
__device__ __forceinline__ float ldc4(srd_t srd, unsigned off) { return __uint_as_float(__builtin_amdgcn_raw_buffer_load_b32(srd, off, 0, 16)); }

#define STAMP_DECL
#define STAMP_AT(i)
#define STAMP_OUT(kid)
#define STAMP_ARGS
#define STAMP_PASS

constexpr int K1_SLOT = 40960, K1_NS = 4, K1_AUX = 0;
template <bool SYNC>
__device__ __forceinline__ void phase1(LAS char* lds, const Params& P, unsigned tag STAMP_ARGS) {
    const unsigned lds0 = (unsigned)(uintptr_t)lds;
    const int tid = threadIdx.x, lane = tid & 63, w = __builtin_amdgcn_readfirstlane(tid >> 6), g = lane >> 4, r = lane & 15;
    const int b = blockIdx.x, xcd = b & 7, loc = b >> 3;
        const int p = (xcd & 3) * 4 + (loc & 3), rt = (xcd >> 2) * 8 + (loc >> 2);
    char* ws = P.ws;
    constexpr int NT = DM / 64;
    LAS f32x2* stat = (LAS f32x2*)(lds + K1_AUX);
    LAS float* csl = (LAS float*)(lds + K1_AUX + 1024);
    if (w >= 4) {
        const srd_t srx = mk_srd(P.x, SEQ * DM * 2), srw = mk_srd(P.wqkv, DM * 3 * DM * 2);
        const int j = w - 4, rr = lane >> 3, c8 = lane & 7;
        unsigned xv[2], wv[2];
#pragma unroll
        for (int i = 0; i < 2; ++i) {
            xv[i] = rr * 2048 + ((c8 ^ (4 * i + (rr >> 1))) * 16);
            const int f = ((rr >> 1) & 1) | (i << 1);
            wv[i] = rr * 6144 + (((c8 >> 1) ^ f) * 32) + (c8 & 1) * 16;
        }
        const unsigned xs0 = (unsigned)(rt * 128 + 32 * j) * 2048u, ws0 = (unsigned)p * 128u + (unsigned)(16 * j) * 6144u;
        const unsigned xl0 = lds0 + j * 4096, wl0 = lds0 + 16384 + j * 2048;
#define K1_DMA(i, t, slot) do { if ((i) < 4) dma16(xl0 + (slot) + (i) * 1024, xv[(i) & 1], srx, xs0 + (i) * (8 * 2048) + (t) * 128); \
        else dma16(wl0 + (slot) + (((i) - 4) >> 1) * 8192 + (((i) - 4) & 1) * 1024, wv[((i) - 4) & 1], srw, ws0 + (((i) - 4) >> 1) * 2048 + (((i) - 4) & 1) * (8 * 6144) + (t) * (64 * 6144)); } while (0)
#define K1_STAGE(t, slot) do { _Pragma("unroll") for (int i_ = 0; i_ < 10; ++i_) K1_DMA(i_, t, slot); } while (0)
        K1_STAGE(0, 0); K1_STAGE(1, K1_SLOT); K1_STAGE(2, 2 * K1_SLOT);
        WAITBAR(20); K1_STAGE(3, 3 * K1_SLOT);
        const bool docs = b < 192 && j < 2;
        const int grp0 = b % 24, part = 2 * (b / 24) + j, srow = 8 * b + 2 * j + (lane >> 5);
        u32x4 xr[4], cw[16];
        { const unsigned xo = (unsigned)srow * 2048u + (lane & 31) * 16, co = docs ? (unsigned)(part * 64 + g) * 6144u + grp0 * 256 + r * 16 : 0x7ff00000u;
#pragma unroll
          for (int k = 0; k < 4; ++k) asm volatile("buffer_load_dwordx4 %0, %1, %2, 0 offen offset:%3" : "=v"(xr[k]) : "v"(xo), "s"(srx), "n"(k * 512));
#pragma unroll
          for (int i = 0; i < 16; ++i) asm volatile("buffer_load_dwordx4 %0, %1, %2, %3 offen" : "=v"(cw[i]) : "v"(co), "s"(srw), "s"(i * 4 * 6144)); }
        WAITBAR(40); K1_STAGE(4, 0);
        WAITBAR(40); K1_STAGE(5, K1_SLOT);
        WAITBAR(40); K1_STAGE(6, 2 * K1_SLOT);
        WAITBAR(20);
        asm volatile("" : "+v"(xr[0]), "+v"(xr[1]), "+v"(xr[2]), "+v"(xr[3]), "+v"(cw[0]), "+v"(cw[1]), "+v"(cw[2]), "+v"(cw[3]), "+v"(cw[4]), "+v"(cw[5]), "+v"(cw[6]), "+v"(cw[7]));
        asm volatile("" : "+v"(cw[8]), "+v"(cw[9]), "+v"(cw[10]), "+v"(cw[11]), "+v"(cw[12]), "+v"(cw[13]), "+v"(cw[14]), "+v"(cw[15]));
        K1_STAGE(7, 3 * K1_SLOT);
        {
            const unsigned x00 = xr[0][0];
            const float pv = bf_lo(lane < 32 ? __builtin_amdgcn_readlane(x00, 0) : __builtin_amdgcn_readlane(x00, 32));
            float s = 0.f, q = 0.f;
#pragma unroll
            for (int k = 0; k < 4; ++k)
#pragma unroll
                for (int e = 0; e < 4; ++e) { const unsigned d = xr[k][e]; const float u = bf_lo(d) - pv, v = bf_hi(d) - pv; s += u + v; q += u * u + v * v; }
            s = sum32(s); q = sum32(q);
            const float dm = s * (1.0f / DM), var = fmaxf(q * (1.0f / DM) - dm * dm, 0.f);
            const u32x2 sv = {__float_as_uint(pv + dm), __float_as_uint(1.0f / sqrtf(var + LN_EPS))};
            __builtin_amdgcn_raw_buffer_store_b64(sv, mk_srd(ws + WS_S1, SEQ * 8), (lane & 31) == 31 ? (unsigned)srow * 8u : 0x7ff00000u, 0, 16);
            bf16x8 sel;
#pragma unroll
            for (int e = 0; e < 8; ++e) sel[e] = (r == e) ? (short)0x3f80 : (short)0;
            f32x4 c0 = {0.f, 0.f, 0.f, 0.f}, c1 = {0.f, 0.f, 0.f, 0.f};
#pragma unroll
            for (int i = 0; i < 16; i += 2) { c0 = MFMA16(sel, __builtin_bit_cast(bf16x8, cw[i]), c0); c1 = MFMA16(sel, __builtin_bit_cast(bf16x8, cw[i + 1]), c1); }
            __builtin_amdgcn_raw_buffer_store_b128(__builtin_bit_cast(u32x4, c0 + c1), mk_srd(ws + WS_C1, 16 * 3072 * 4), (docs && lane < 32) ? (unsigned)(part * 3072 + grp0 * 128 + r * 8 + g * 4) * 4u : 0x7ff00000u, 0, 16);
        }
        WAITBAR(22); K1_STAGE(8, 0);
        WAITBAR(22); K1_STAGE(9, K1_SLOT);
        WAITBAR(22); K1_STAGE(10, 2 * K1_SLOT);
        WAITBAR(20);
        if (SYNC && tid == 256) flag_set(ws, FL_0 + b, tag);
        K1_STAGE(11, 3 * K1_SLOT);
        int fr = 0;
#pragma unroll 1
        for (int t = 9; t + 3 < NT; ++t) {
            WAITBAR(20);
            K1_STAGE(t + 3, fr);
            fr = (fr == 3 * K1_SLOT) ? 0 : fr + K1_SLOT;
        }
        WAITBAR(20);
        WAITBAR(10);
        if (j == 0) {
            if (SYNC) flags_wait(ws, lane < 16 ? FL_0 + 16 * rt + lane : -1, tag);
            const u32x4 v = ldc16(mk_srd(ws + WS_S1, SEQ * 8), (unsigned)(rt * 128) * 8u + lane * 16);
            *(LAS u32x4*)(lds + K1_AUX + lane * 16) = v;
        } else {
            const int grp = (j - 1) * 8 + (p >> 1);
            if (SYNC) flags_wait(ws, lane < 8 ? FL_0 + grp + 24 * lane : -1, tag);
            const srd_t src = mk_srd(ws + WS_C1, 16 * 3072 * 4);
            float a[16];
#pragma unroll
            for (int i = 0; i < 16; ++i) a[i] = ldc4(src, (unsigned)(i * 3072 + (j - 1) * 1024 + p * 64 + lane) * 4u);
            float s = 0.f;
#pragma unroll
            for (int i = 0; i < 16; ++i) s += a[i];
            csl[(j - 1) * 64 + lane] = s;
        }
        WAITBAR(0);
        LBAR();
    } else {
        const int wr = w >> 1, wc = w & 1;
        int xrd[2], wrd[2];
        { const int q4 = (lane >> 2) & 3, p4 = lane & 3, trow = 8 * g + q4, f = ((trow >> 1) & 1) | (((trow >> 3) & 1) << 1);
#pragma unroll
          for (int s = 0; s < 2; ++s) { xrd[s] = (64 * wr + r) * 128 + (((4 * s + g) ^ ((r >> 1) & 7)) * 16); wrd[s] = 16384 + trow * 128 + (((2 * wc + s) ^ f) * 32) + (p4 >> 1) * 16 + (p4 & 1) * 8; } }
        f32x4 acc[4][6];
#pragma unroll
        for (int m = 0; m < 4; ++m)
#pragma unroll
            for (int n = 0; n < 6; ++n) acc[m][n] = (f32x4){0.f, 0.f, 0.f, 0.f};
        bf16x8 xa[4], xb[4]; s16x4 wa[12], wb[12];
#define K1_RW(i, W, sl, s) W[i] = trd(lds + (sl) + wrd[((i) >> 1) & 1] + ((i) >> 2) * 8192 + (s) * 4096 + ((i) & 1) * 512)
#define K1_RX(i, X, sl, s) X[i] = ldf(lds + (sl) + xrd[s] + (i) * 2048)
#define K1_RD(i, X, W, sl, s) do { if ((i) == 2) K1_RX(0, X, sl, s); else if ((i) == 7) K1_RX(1, X, sl, s); else if ((i) == 12) K1_RX(2, X, sl, s); else if ((i) == 15) K1_RX(3, X, sl, s); \
        else K1_RW((i) < 2 ? (i) : (i) < 7 ? (i) - 1 : (i) < 12 ? (i) - 2 : (i) - 3, W, sl, s); } while (0)
#define K1_MM(i, X, W) do { const int m_ = (i) / 6, n_ = (i) % 6; const bf16x8 wf_ = cat8(W[2 * n_], W[2 * n_ + 1]); \
        acc[m_][n_] = (n_ < 4) ? MFMA16(wf_, X[m_], acc[m_][n_]) : MFMA16(X[m_], wf_, acc[m_][n_]); } while (0)
#define K1_HALF(XM, WM, XR, WR, sl, s, DOMMA) do { \
        _Pragma("unroll") for (int i_ = 0; i_ < 24; ++i_) { \
            if (DOMMA) K1_MM(i_, XM, WM); \
            if (i_ < 16) K1_RD(i_, XR, WR, sl, s); \
            SCHED0(); } } while (0)
        LBAR();
        STAMP_AT(2);
        K1_HALF(xb, wb, xa, wa, 0, 0, false);
        K1_HALF(xa, wa, xb, wb, 0, 1, true);
        int cur = K1_SLOT;
#pragma unroll 1
        for (int t = 1; t < NT; ++t) {
            LBAR();
            K1_HALF(xb, wb, xa, wa, cur, 0, true);
            K1_HALF(xa, wa, xb, wb, cur, 1, true);
            cur = (cur == 3 * K1_SLOT) ? 0 : cur + K1_SLOT;
        }
#pragma unroll
        for (int i = 0; i < 24; ++i) K1_MM(i, xb, wb);
        STAMP_AT(3);
        LBAR();
        const int head = 2 * p + wc;
        const srd_t srq = mk_srd(ws + WS_Q, 12u << 20);
        f32x4 cq[4]; float cv[2];
#pragma unroll
        for (int n = 0; n < 4; ++n) cq[n] = *(LAS const f32x4*)(csl + (n >> 1) * 64 + wc * 32 + (n & 1) * 16 + 4 * g);
#pragma unroll
        for (int n = 0; n < 2; ++n) cv[n] = csl[128 + wc * 32 + n * 16 + r];
        const int rho = 16 * (r >> 3) + 4 * ((r >> 2) & 1) + (r & 3);
        f32x4 vv[2][2];
#pragma unroll
        for (int m = 0; m < 4; ++m) {
            const int lrow = wr * 64 + m * 16, row0 = rt * 128 + lrow;
            const unsigned blk = ((unsigned)head * 64 + (row0 >> 5)) * 2048u + (g >> 1) * 1024 + (g & 1) * 512;
            const f32x2 st = stat[lrow + r];
            const float qs = st.y * QSCALE;
            st16(srq, (4u << 20) + blk + (rho + (m & 1) * 8) * 16, pk8((acc[m][2] - st.x * cq[2]) * st.y, (acc[m][3] - st.x * cq[3]) * st.y));
            const f32x4 s01 = *(LAS const f32x4*)(stat + lrow + 4 * g), s23 = *(LAS const f32x4*)(stat + lrow + 4 * g + 2);
            const f32x4 mean4 = {s01[0], s01[2], s23[0], s23[2]}, rstd4 = {s01[1], s01[3], s23[1], s23[3]};
#pragma unroll
            for (int n = 0; n < 2; ++n) vv[m & 1][n] = (acc[m][4 + n] - mean4 * cv[n]) * rstd4;
            if (m & 1) {
#pragma unroll
                for (int n = 0; n < 2; ++n) st16(srq, (8u << 20) + blk + (16 * n + r) * 16, pk8(vv[0][n], vv[1][n]));
            }
            st16(srq, blk + ((m & 1) * 16 + r) * 16, pk8((acc[m][0] - st.x * cq[0]) * qs, (acc[m][1] - st.x * cq[1]) * qs));
        }
    }
    STAMP_AT(4);
    WAITBAR(0);
    if (SYNC && tid == 0) flag_set(ws, FL_1 + p * 16 + rt, tag);
}

constexpr int K2_SLOT = 32768, K2_SIDE = 3 * K2_SLOT;
struct K2Side { srd_t src, srp, sro; unsigned cv, cs0, ps0; };
template <int KH>
__device__ __forceinline__ void attn_fast(LAS char* lds, srd_t srk, srd_t srv, unsigned hbase, const K2Side& S, int w, int lane, const bf16x8 (&qf)[2][2], f32x16 (&o)[2], f32x4 (&lacc)[2], f32x4& cs) {
    const int tl = w & 3;
    const unsigned lds0 = (unsigned)(uintptr_t)lds;
    const unsigned lv = lane * 16;
    const unsigned s0 = hbase + (unsigned)(KH * 32 + tl) * 2048u, d0 = lds0 + KH * 16384 + tl * 2048;
    const int rd0 = KH * 16384 + lane * 16;
    bf16x8 ones, sel;
    { const short v = ((lane & 15) == ((lane >> 4) & 1)) ? (short)0x3f80 : (short)0;
#pragma unroll
      for (int j = 0; j < 8; ++j) { ones[j] = v; sel[j] = ((lane & 15) == j) ? (short)0x3f80 : (short)0; } }
    const unsigned sd = lds0 + K2_SIDE + w * 1024;
#define K2_DMA(k, rd, fr) do { \
        if ((k) < 4) dma16c(d0 + (fr) + ((k) >> 1) * 8192 + ((k) & 1) * 1024, lv, (k) < 2 ? srk : srv, s0 + ((rd) + 2) * 8192 + ((k) & 1) * 1024); \
        else if ((k) == 4) dma16(sd + ((rd) & 1) * 8192, S.cv, S.src, S.cs0 + (rd) * (4 * FF * 2)); \
        else dma16(sd + 16384, lv, (rd) < 4 ? S.srp : S.sro, (rd) < 4 ? S.ps0 + (rd) * 1024 : (rd) == 4 ? (S.ps0 >> 2) : 0x7ff00000u); } while (0)
#define K2_HOOK(gi, rd, fr) do { if (((gi) >> 1) < 6 && ((gi) & 1) == KH) K2_DMA((gi) >> 1, rd, fr); } while (0)
    f32x16 sa[2], sb[2];
    const f32x16 zero16 = {};
    bf16x8 k0 = ldf(lds + rd0), k1 = ldf(lds + rd0 + 1024), v0, v1;
#pragma unroll
    for (int qt = 0; qt < 2; ++qt) { sa[qt] = MFMA32(k0, qf[qt][0], zero16); sa[qt] = MFMA32(k1, qf[qt][1], sa[qt]); }
    k0 = ldf(lds + rd0 + 2048);
    u32x4 pa = {0u, 0u, 0u, 0u}, pb = {0u, 0u, 0u, 0u}; float e6 = 0.f, e7 = 0.f;
    v1 = k1;
    bf16x8 csd = {};
#define EX2(x) __builtin_amdgcn_exp2f(x)
#define K2_GROUP(S_, B, PC, PP, QK, VP, QP, RD, gi) do { \
        QK; PP[3] = pk_bf16(e6, e7); SCHED0(); \
        const float x0 = EX2(S_[B + 0]), x1 = EX2(S_[B + 1]), x2 = EX2(S_[B + 2]); RD; SCHED0(); \
        o[QP] = MFMA32(VP, __builtin_bit_cast(bf16x8, PP), o[QP]); SCHED0(); \
        const float x3 = EX2(S_[B + 3]), x4 = EX2(S_[B + 4]), x5 = EX2(S_[B + 5]); PC[0] = pk_bf16(x0, x1); SCHED0(); \
        lacc[QP] = MFMA16(ones, __builtin_bit_cast(bf16x8, PP), lacc[QP]); K2_HOOK(gi, rd, fr); SCHED0(); \
        e6 = EX2(S_[B + 6]); e7 = EX2(S_[B + 7]); PC[1] = pk_bf16(x2, x3); PC[2] = pk_bf16(x4, x5); SCHED0(); } while (0)
#define K2_TILE(SC, SN, kn1, kn2, va, ti) do { \
        K2_GROUP(SC[0], 0, pa, pb, SN[0] = MFMA32(k0, qf[0][0], zero16), v1, 1, v0 = ldf(va), 4 * (ti)); \
        K2_GROUP(SC[1], 0, pb, pa, SN[1] = MFMA32(k0, qf[1][0], zero16), v0, 0, k1 = ldf((kn1) + 1024), 4 * (ti) + 1); \
        K2_GROUP(SC[0], 8, pa, pb, SN[0] = MFMA32(k1, qf[0][1], SN[0]), v0, 1, v1 = ldf((va) + 1024), 4 * (ti) + 2); \
        K2_GROUP(SC[1], 8, pb, pa, SN[1] = MFMA32(k1, qf[1][1], SN[1]), v1, 0, k0 = ldf(kn2), 4 * (ti) + 3); } while (0)
    int cur = 0, fr = 2 * K2_SLOT;
#pragma unroll 1
    for (int rd = 0; rd < 8; ++rd) {
        const int nx = (cur == 2 * K2_SLOT) ? 0 : cur + K2_SLOT;
        if (rd > 0) WAITBAR(0);
        LAS const char* pc = lds + cur + rd0; LAS const char* pn = lds + nx + rd0;
        K2_TILE(sa, sb, pc + 2048, pc + 4096, pc + 8192, 0);
        K2_TILE(sb, sa, pc + 4096, pc + 6144, pc + 8192 + 2048, 1);
        K2_TILE(sa, sb, pc + 6144, pn, pc + 8192 + 4096, 2);
        csd = ldf(lds + K2_SIDE + ((rd - 1) & 1) * 8192 + w * 1024 + lane * 16);
        if (rd == 0) csd = (bf16x8){};
        K2_TILE(sb, sa, pn, pn + 2048, pc + 8192 + 6144, 3);
        cs = MFMA16(sel, csd, cs);
        fr = cur; cur = nx;
    }
    pb[3] = pk_bf16(e6, e7);
    o[1] = MFMA32(v1, __builtin_bit_cast(bf16x8, pb), o[1]);
    lacc[1] = MFMA16(ones, __builtin_bit_cast(bf16x8, pb), lacc[1]);
    WAITBAR(0);
    cs = MFMA16(sel, ldf(lds + K2_SIDE + 8192 + w * 1024 + lane * 16), cs);
#undef K2_GROUP
#undef K2_TILE
#undef K2_DMA
#undef K2_HOOK
}
#define K2_STAGE(rd, slot) do { dma16c(d0 + (slot), lv, srk, s0 + (rd) * 8192); dma16c(d0 + (slot) + 1024, lv, srk, s0 + (rd) * 8192 + 1024); \
        dma16c(d0 + (slot) + 8192, lv, srv, s0 + (rd) * 8192); dma16c(d0 + (slot) + 9216, lv, srv, s0 + (rd) * 8192 + 1024); } while (0)
__device__ __forceinline__ void attn_safe(LAS char* lds, srd_t srk, srd_t srv, unsigned hbase, const bf16x8 (&qf)[2][2], int w, int lane, f32x16 (&o)[2], float (&mrun)[2], float (&lrun)[2]) {
    const int kh = w >> 2, tl = w & 3;
    const unsigned lds0 = (unsigned)(uintptr_t)lds;
    const unsigned lv = lane * 16;
    const unsigned s0 = hbase + (unsigned)(kh * 32 + tl) * 2048u, d0 = lds0 + kh * 16384 + tl * 2048;
    const int rd0 = kh * 16384 + lane * 16;
    const f32x16 zero16 = {};
#pragma unroll 1
    for (int rd = 0; rd < 8; ++rd) {
        K2_STAGE(rd, 0);
        WAITBAR(0);
#pragma unroll 1
        for (int i = 0; i < 4; ++i) {
            LAS const char* pk = lds + rd0 + i * 2048;
            const bf16x8 k0 = ldf(pk), k1 = ldf(pk + 1024), v0 = ldf(pk + 8192), v1 = ldf(pk + 9216);
#pragma unroll
            for (int qt = 0; qt < 2; ++qt) {
                f32x16 sc = MFMA32(k0, qf[qt][0], zero16); sc = MFMA32(k1, qf[qt][1], sc);
                float mx = sc[0];
#pragma unroll
                for (int j = 1; j < 16; ++j) mx = fmaxf(mx, sc[j]);
                mx = fmaxf(mx, __shfl_xor(mx, 32));
                const float mn = fmaxf(mrun[qt], mx);
                const float ms = (mn == -INFINITY) ? 0.f : mn;
                const float al = __builtin_amdgcn_exp2f(mrun[qt] - ms);
                mrun[qt] = mn;
                u32x4 p0, p1;
#pragma unroll
                for (int j = 0; j < 4; ++j) { p0[j] = pk_bf16(__builtin_amdgcn_exp2f(sc[2 * j] - ms), __builtin_amdgcn_exp2f(sc[2 * j + 1] - ms));
                                              p1[j] = pk_bf16(__builtin_amdgcn_exp2f(sc[8 + 2 * j] - ms), __builtin_amdgcn_exp2f(sc[9 + 2 * j] - ms)); }
                float s_ = 0.f;
#pragma unroll
                for (int j = 0; j < 4; ++j) { const unsigned a = p0[j], c = p1[j]; s_ += bf_lo(a) + bf_hi(a); s_ += bf_lo(c) + bf_hi(c); }
                lrun[qt] = lrun[qt] * al + s_;
#pragma unroll
                for (int j = 0; j < 16; ++j) o[qt][j] *= al;
                o[qt] = MFMA32(v0, __builtin_bit_cast(bf16x8, p0), o[qt]); o[qt] = MFMA32(v1, __builtin_bit_cast(bf16x8, p1), o[qt]);
            }
        }
        WAITBAR(0);
    }
}

template <bool SYNC>
__device__ __forceinline__ void phase2(LAS char* lds, const Params& P, unsigned tag STAMP_ARGS) {
    const int tid = threadIdx.x, lane = tid & 63, w = __builtin_amdgcn_readfirstlane(tid >> 6);
    const int b = blockIdx.x, xcd = b & 7, loc = b >> 3;
    const int head = xcd * 4 + (loc & 3), qb = loc >> 2;
    char* ws = P.ws;
    K2Side S;
    const int cgrp = b % 56, rq = b / 56;
    const bool do_cs = b < 224;
    S.src = mk_srd(cgrp < 28 ? P.wg : P.wu, DM * FF * 2);
    S.cv = (unsigned)(lane >> 4) * (FF * 2) + (lane & 15) * 16;
    S.cs0 = do_cs ? (unsigned)(rq * 256 + w * 32) * (FF * 2) + (unsigned)(cgrp < 28 ? cgrp : cgrp - 28) * 256u : 0x7ff00000u;
    S.srp = mk_srd(P.wd, FF * DM * 2); S.sro = mk_srd(P.wo, DM * DM * 2);
    S.ps0 = (unsigned)(b * 8 + w) * 4096u;
    const srd_t srk = mk_srd(ws + WS_K, SEQ * DM * 2), srv = mk_srd(ws + WS_V, SEQ * DM * 2);
    const unsigned hbase = (unsigned)head * 64u * 2048u;
    const int kh = w >> 2;
    if (SYNC) { if (w == 0) flags_wait(ws, lane < 16 ? FL_1 + (head >> 1) * 16 + lane : -1, tag); LBAR(); }
    STAMP_AT(1);
    bf16x8 qf[2][2];
    {
        const unsigned lds0 = (unsigned)(uintptr_t)lds, lv = lane * 16;
        const unsigned s0 = hbase + (unsigned)(kh * 32 + (w & 3)) * 2048u, d0 = lds0 + kh * 16384 + (w & 3) * 2048;
        K2_STAGE(0, 0);
        const srd_t srq = mk_srd(ws + WS_Q, SEQ * DM * 2);
        const unsigned qo = ((unsigned)head * 64 + qb * 8 + (w & 3) * 2) * 2048u + lane * 16;
#pragma unroll
        for (int qt = 0; qt < 2; ++qt)
#pragma unroll
            for (int s = 0; s < 2; ++s) qf[qt][s] = __builtin_bit_cast(bf16x8, ldc16(srq, qo + qt * 2048 + s * 1024));
        K2_STAGE(1, K2_SLOT);
    }
    WAITBAR(0);
    STAMP_AT(2);
    f32x16 o[2] = {}; f32x4 lacc[2] = {}; f32x4 cs = {0.f, 0.f, 0.f, 0.f};
    if (kh == 0) attn_fast<0>(lds, srk, srv, hbase, S, w, lane, qf, o, lacc, cs);
    else attn_fast<1>(lds, srk, srv, hbase, S, w, lane, qf, o, lacc, cs);
    STAMP_AT(3);
    LAS float* ob = (LAS float*)lds;
    LAS float* lb = (LAS float*)(lds + 32768);
    LAS unsigned* flag = (LAS unsigned*)(lds + 32768 + 4096);
    LAS float* cb = (LAS float*)(lds + 40960);
    const int hi = lane >> 5, q32 = lane & 31;
    const srd_t sro = mk_srd(ws + WS_O, SEQ * DM * 2);
    const unsigned dst = FRAG(qb * 16 + (w & 3) * 4 + kh * 2 + (q32 >> 4), head, 32) + (hi * 16 + (q32 & 15)) * 16;
    bool bad;
    {
        if (tid == 0) flag[0] = 0u;
        if (lane < 16) { lb[w * 64 + lane] = lacc[0][0]; lb[w * 64 + 16 + lane] = lacc[0][1]; lb[w * 64 + 32 + lane] = lacc[1][0]; lb[w * 64 + 48 + lane] = lacc[1][1]; }
#pragma unroll
        for (int j = 0; j < 4; ++j) { const f32x4 v = kh ? (f32x4){o[0][4 * j], o[0][4 * j + 1], o[0][4 * j + 2], o[0][4 * j + 3]} : (f32x4){o[1][4 * j], o[1][4 * j + 1], o[1][4 * j + 2], o[1][4 * j + 3]};
            *(LAS f32x4*)(ob + ((w * 4 + j) * 64 + lane) * 4) = v; }
        if (lane < 32) *(LAS f32x4*)(cb + w * 128 + (lane & 15) * 8 + (lane >> 4) * 4) = cs;
        LBAR();
        const int pw = w ^ 4;
        f32x16 of;
#pragma unroll
        for (int j = 0; j < 4; ++j) { const f32x4 v = *(LAS const f32x4*)(ob + ((pw * 4 + j) * 64 + lane) * 4);
#pragma unroll
            for (int e = 0; e < 4; ++e) of[4 * j + e] = (kh ? o[1][4 * j + e] : o[0][4 * j + e]) + v[e]; }
        const float l = lb[w * 64 + kh * 32 + q32] + lb[pw * 64 + kh * 32 + q32];
        bool ok = (l >= 0x1p-60f) && (l <= 0x1p+80f);
#pragma unroll
        for (int j = 0; j < 16; ++j) ok = ok && (fabsf(of[j]) <= 0x1p+120f);
        if (!__all(ok)) { if (lane == 0) flag[0] = 1u; }
        if (do_cs && tid < 128) {
            float s = 0.f;
#pragma unroll
            for (int i = 0; i < 8; ++i) s += cb[i * 128 + tid];
            __hip_atomic_store((gu32*)(ws + WS_CS) + (rq * 7168 + cgrp * 128 + tid), __float_as_uint(s), RLX_AGENT);
        }
        LBAR();
        bad = flag[0] != 0u;
        if (!bad) {
            const float rl = 1.0f / l;
#pragma unroll
            for (int c = 0; c < 2; ++c) {
                u32x4 ov; ov.x = pk_bf16(of[8 * c + 0] * rl, of[8 * c + 1] * rl); ov.y = pk_bf16(of[8 * c + 2] * rl, of[8 * c + 3] * rl);
                ov.z = pk_bf16(of[8 * c + 4] * rl, of[8 * c + 5] * rl); ov.w = pk_bf16(of[8 * c + 6] * rl, of[8 * c + 7] * rl);
                st16(sro, dst + c * 512, ov);
            }
        }
    }
    if (bad) {
        WAITBAR(0);
        o[0] = (f32x16){}; o[1] = (f32x16){}; float mrun[2] = {-INFINITY, -INFINITY}, lrun[2] = {0.f, 0.f};
        attn_safe(lds, srk, srv, hbase, qf, w, lane, o, mrun, lrun);
        LAS float* mb = lb + 512;
#pragma unroll
        for (int qt = 0; qt < 2; ++qt) { const float lt = lrun[qt] + __shfl_xor(lrun[qt], 32); if (lane < 32) { lb[w * 64 + qt * 32 + lane] = lt; mb[w * 64 + qt * 32 + lane] = mrun[qt]; } }
#pragma unroll
        for (int j = 0; j < 16; ++j) ob[(w * 16 + j) * 64 + lane] = kh ? o[0][j] : o[1][j];
        WAITBAR(0);
        const int pw = w ^ 4;
        const float ma = mb[w * 64 + kh * 32 + q32], mp = mb[pw * 64 + kh * 32 + q32];
        const float mn = fmaxf(ma, mp), ms = (mn == -INFINITY) ? 0.f : mn;
        const float fa = __builtin_amdgcn_exp2f(ma - ms), fp = __builtin_amdgcn_exp2f(mp - ms);
        const float l = lb[w * 64 + kh * 32 + q32] * fa + lb[pw * 64 + kh * 32 + q32] * fp;
        const float rl = 1.0f / l;
        float of[16];
#pragma unroll
        for (int j = 0; j < 16; ++j) of[j] = ((kh ? o[1][j] : o[0][j]) * fa + ob[(pw * 16 + j) * 64 + lane] * fp) * rl;
#pragma unroll
        for (int c = 0; c < 2; ++c) {
            u32x4 ov; ov.x = pk_bf16(of[8 * c + 0], of[8 * c + 1]); ov.y = pk_bf16(of[8 * c + 2], of[8 * c + 3]);
            ov.z = pk_bf16(of[8 * c + 4], of[8 * c + 5]); ov.w = pk_bf16(of[8 * c + 6], of[8 * c + 7]);
            st16(sro, dst + c * 512, ov);
        }
    }
    STAMP_AT(4);
    WAITBAR(0);
    if (SYNC && tid == 0) flag_set(ws, FL_2 + qb * 32 + head, tag);
}
#undef K2_STAGE

constexpr int K3_SLOT = 24576, K3_NS = 6, K3_AUX = K3_NS * K3_SLOT;
template <int MODE, bool SYNC>
__device__ __forceinline__ void phase35(LAS char* lds, const Params& P, unsigned tag STAMP_ARGS) {
    constexpr int KD = MODE == 0 ? DM : FF, KT = KD / 32, NT = KD / 64;
    const unsigned lds0 = (unsigned)(uintptr_t)lds;
    const int tid = threadIdx.x, lane = tid & 63, w = __builtin_amdgcn_readfirstlane(tid >> 6), g = lane >> 4, r = lane & 15;
    const int b = blockIdx.x, xcd = b & 7, loc = b >> 3;
    const int rt = (xcd >> 1) * 4 + (loc & 3), ct = (xcd & 1) * 8 + (loc >> 2);
    char* ws = P.ws;
    if (w >= 4) {
        const int j = w - 4;
        const srd_t srx = mk_srd(ws + (MODE == 0 ? WS_O : WS_ACT), SEQ * KD * 2), srw = mk_srd(MODE == 0 ? P.wo : P.wd, KD * DM * 2);
        const unsigned xv = lane * 16, xs0 = FRAG(rt * 8 + 2 * j, 0, KT);
        constexpr unsigned XM = FRAG(1, 0, KT), XK = FRAG(0, 1, KT);
        const int wrow = lane >> 3, wch = (lane & 7) ^ (((wrow >> 1) & 3) << 1);
        const unsigned wv = (unsigned)wrow * 2048u + wch * 16, ws0 = (unsigned)ct * 128u + (unsigned)(16 * j) * 2048u;
        const unsigned ld0 = lds0 + j * 4096, ld1 = lds0 + 16384 + j * 2048;
#define K3_W(t, slot) do { dma16(ld1 + (slot), wv, srw, ws0 + (t) * (64 * 2048)); dma16(ld1 + (slot) + 1024, wv, srw, ws0 + 8 * 2048 + (t) * (64 * 2048)); } while (0)
#define K3_X(t, slot) do { dma16c(ld0 + (slot), xv, srx, xs0 + (t) * (2 * XK)); dma16c(ld0 + (slot) + 1024, xv, srx, xs0 + (t) * (2 * XK) + XK); \
        dma16c(ld0 + (slot) + 2048, xv, srx, xs0 + XM + (t) * (2 * XK)); dma16c(ld0 + (slot) + 3072, xv, srx, xs0 + XM + (t) * (2 * XK) + XK); } while (0)
        K3_W(0, 0); K3_W(1, K3_SLOT); K3_W(2, 2 * K3_SLOT); K3_W(3, 3 * K3_SLOT); K3_W(4, 4 * K3_SLOT);
        if (SYNC) LBAR();
        K3_X(0, 0); K3_X(1, K3_SLOT); K3_X(2, 2 * K3_SLOT);
        WAITBAR(8);  K3_X(3, 3 * K3_SLOT); K3_X(4, 4 * K3_SLOT); K3_X(5, 5 * K3_SLOT); K3_W(5, 5 * K3_SLOT);
        WAITBAR(18); K3_X(6, 0); K3_W(6, 0);
        WAITBAR(20); K3_X(7, K3_SLOT); K3_W(7, K3_SLOT);
        WAITBAR(22); K3_X(8, 2 * K3_SLOT); K3_W(8, 2 * K3_SLOT);
        int fr = 3 * K3_SLOT;
#pragma unroll 1
        for (int t = 4; t + K3_NS - 1 < NT; ++t) {
            WAITBAR(6 * (K3_NS - 2));
            K3_X(t + K3_NS - 1, fr); K3_W(t + K3_NS - 1, fr);
            fr = (fr == (K3_NS - 1) * K3_SLOT) ? 0 : fr + K3_SLOT;
        }
        WAITBAR(24); WAITBAR(18); WAITBAR(12); WAITBAR(6); WAITBAR(0);
        if (MODE == 0) LBAR();
    } else {
        const int wm = w >> 1, wn = w & 1;
        const int col0 = ct * 64 + wn * 32 + 4 * g;
        if (SYNC) { if (w == 0) flags_wait(ws, lane < 32 ? (MODE == 0 ? FL_2 : FL_4) + (rt >> 1) * 32 + lane : -1, tag); LBAR(); }
        STAMP_AT(1);
        u32x4 res[4];
#pragma unroll
        for (int m = 0; m < 4; ++m) {
            const int mrow = rt * 128 + wm * 64 + m * 16;
            if (MODE == 0) { const u32x2 a = *(const u32x2*)(P.x + (size_t)(mrow + r) * DM + col0), c = *(const u32x2*)(P.x + (size_t)(mrow + r) * DM + col0 + 16); res[m] = (u32x4){a.x, a.y, c.x, c.y}; }
            else res[m] = ldc16(mk_srd(ws + WS_H, SEQ * DM * 2), FRAG(mrow >> 4, ct * 2 + wn, 32) + lane * 16);
        }
        const int xrd = wm * 8192 + lane * 16;
        const int q4 = (lane >> 2) & 3, p4 = lane & 3;
        const int trow = (MODE == 0) ? (4 * (g & 1) + 16 * (g >> 1) + q4) : (4 * g + q4);
        constexpr int TR2 = (MODE == 0) ? 8 * 128 : 16 * 128;
        int wrd[2];
#pragma unroll
        for (int n = 0; n < 2; ++n) wrd[n] = 16384 + trow * 128 + (((wn * 2 + n) ^ ((trow >> 1) & 3)) * 32) + (p4 >> 1) * 16 + (p4 & 1) * 8;
        f32x4 acc[4][2];
#pragma unroll
        for (int m = 0; m < 4; ++m) { acc[m][0] = (f32x4){0.f, 0.f, 0.f, 0.f}; acc[m][1] = (f32x4){0.f, 0.f, 0.f, 0.f}; }
        bf16x8 xa[4], xb[4]; s16x4 wa[4], wb[4];
#define K3_RD(i, X, W, sl, s) do { if ((i) == 2) X[0] = ldf(lds + (sl) + xrd + (s) * 1024); else if ((i) >= 5) X[(i) - 4] = ldf(lds + (sl) + xrd + ((i) - 4) * 2048 + (s) * 1024); \
        else { const int k_ = (i) < 2 ? (i) : (i) - 1; W[k_] = trd(lds + (sl) + wrd[k_ >> 1] + (k_ & 1) * TR2 + (s) * 4096); } } while (0)
#define K3_MM(i, X, W) do { const int m_ = (i) >> 1, n_ = (i) & 1; acc[m_][n_] = MFMA16(cat8(W[2 * n_], W[2 * n_ + 1]), X[m_], acc[m_][n_]); } while (0)
#define K3_HALF(XM, WM, XR, WR, sl, s, DOMMA) do { _Pragma("unroll") for (int i_ = 0; i_ < 8; ++i_) { if (DOMMA) K3_MM(i_, XM, WM); K3_RD(i_, XR, WR, sl, s); SCHED0(); } } while (0)
        LBAR();
        STAMP_AT(2);
        K3_HALF(xb, wb, xa, wa, 0, 0, false);
        K3_HALF(xa, wa, xb, wb, 0, 1, true);
        int cur = K3_SLOT;
#pragma unroll 1
        for (int t = 1; t < NT; ++t) {
            LBAR();
            K3_HALF(xb, wb, xa, wa, cur, 0, true);
            K3_HALF(xa, wa, xb, wb, cur, 1, true);
            cur = (cur == (K3_NS - 1) * K3_SLOT) ? 0 : cur + K3_SLOT;
        }
#pragma unroll
        for (int i = 0; i < 8; ++i) K3_MM(i, xb, wb);
        STAMP_AT(3);
        if (MODE == 0) {
            LAS f32x2* sb = (LAS f32x2*)(lds + K3_AUX);
            const srd_t srh = mk_srd(ws + WS_H, SEQ * DM * 2);
#pragma unroll
            for (int m = 0; m < 4; ++m) {
                const int mrow = rt * 128 + wm * 64 + m * 16;
                const f32x4 a = {bf_lo(res[m].x) + acc[m][0][0], bf_hi(res[m].x) + acc[m][0][1], bf_lo(res[m].y) + acc[m][0][2], bf_hi(res[m].y) + acc[m][0][3]};
                const f32x4 c = {bf_lo(res[m].z) + acc[m][1][0], bf_hi(res[m].z) + acc[m][1][1], bf_lo(res[m].w) + acc[m][1][2], bf_hi(res[m].w) + acc[m][1][3]};
                const u32x4 hv = pk8(a, c);
                st16(srh, FRAG(mrow >> 4, ct * 2 + wn, 32) + lane * 16, hv);
                float s = 0.f, q = 0.f;
#pragma unroll
                for (int j = 0; j < 4; ++j) { const unsigned d = hv[j]; const float u = bf_lo(d), v = bf_hi(d); s += u + v; q += u * u + v * v; }
                s += __shfl_xor(s, 16); s += __shfl_xor(s, 32); q += __shfl_xor(q, 16); q += __shfl_xor(q, 32);
                if (g == 0) sb[(wm * 64 + m * 16 + r) * 2 + wn] = (f32x2){s, q};
            }
            LBAR();
            if (tid < 128) { const f32x2 a = sb[tid * 2] + sb[tid * 2 + 1];
                __hip_atomic_store((gu64*)(ws + WS_ST) + ((size_t)(rt * 128 + tid) * 16 + ct), ((unsigned long long)__float_as_uint(a[1]) << 32) | __float_as_uint(a[0]), RLX_AGENT); }
        } else {
#pragma unroll
            for (int m = 0; m < 4; ++m) {
                const int mrow = rt * 128 + wm * 64 + m * 16;
                unsigned a0 = pk_bf16(bf_lo(res[m].x) + acc[m][0][0], bf_hi(res[m].x) + acc[m][0][1]), a1 = pk_bf16(bf_lo(res[m].y) + acc[m][0][2], bf_hi(res[m].y) + acc[m][0][3]);
                unsigned b0 = pk_bf16(bf_lo(res[m].z) + acc[m][1][0], bf_hi(res[m].z) + acc[m][1][1]), b1 = pk_bf16(bf_lo(res[m].w) + acc[m][1][2], bf_hi(res[m].w) + acc[m][1][3]);
                const auto s0 = __builtin_amdgcn_permlane16_swap(b0, a0, false, false), s1 = __builtin_amdgcn_permlane16_swap(b1, a1, false, false);
                const u32x4 ov = {s0[0], s1[0], s0[1], s1[1]};
                const int c8 = ((g & 1) ? 0 : 16) + (g >> 1) * 8;
                *(u32x4*)(P.out + (size_t)(mrow + r) * DM + ct * 64 + wn * 32 + c8) = ov;
            }
        }
    }
    STAMP_AT(4);
    if (MODE == 0) {
        WAITBAR(0);
        if (SYNC && tid == 0) flag_set(ws, FL_3 + rt * 16 + ct, tag);
    }
}

constexpr int K5_SLOT = 32768, K5_NS = 4;
template <int KH>
__device__ __forceinline__ void p5_finish(const Params& P, const f32x4 (&acc)[4][4], const u32x4 (&res)[4], unsigned tag, int b, int w, int lane, int rt, int ct STAMP_ARGS) {
    char* ws = P.ws;
    const int g = lane >> 4, r = lane & 15, wm = w >> 1, wn = w & 1;
    const srd_t sre = mk_srd(ws + WS_XCH, 8u << 20);
    const unsigned eo = (unsigned)(w * 8192) + lane * 16;
#pragma unroll
    for (int m = 0; m < 4; ++m)
#pragma unroll
        for (int n = 0; n < 2; ++n) st16(sre, (unsigned)b * 32768u + eo + (m * 2 + n) * 1024, __builtin_bit_cast(u32x4, acc[m][2 * (1 - KH) + n]));
    WAITV(0);
    if (lane == 0) flag_set(ws, FL_5 + b * 4 + w, tag);
    flags_wait(ws, lane == 0 ? FL_5 + (b ^ 1) * 4 + w : -1, tag);
    STAMP_AT(4);
    u32x4 pv[4][2];
#pragma unroll
    for (int m = 0; m < 4; ++m)
#pragma unroll
        for (int n = 0; n < 2; ++n) pv[m][n] = ldc16(sre, (unsigned)(b ^ 1) * 32768u + eo + (m * 2 + n) * 1024);
#pragma unroll
    for (int m = 0; m < 4; ++m) {
        const int mrow = rt * 128 + wm * 64 + m * 16;
        const f32x4 t0 = acc[m][2 * KH] + __builtin_bit_cast(f32x4, pv[m][0]), t1 = acc[m][2 * KH + 1] + __builtin_bit_cast(f32x4, pv[m][1]);
        unsigned a0 = pk_bf16(bf_lo(res[m].x) + t0[0], bf_hi(res[m].x) + t0[1]), a1 = pk_bf16(bf_lo(res[m].y) + t0[2], bf_hi(res[m].y) + t0[3]);
        unsigned b0 = pk_bf16(bf_lo(res[m].z) + t1[0], bf_hi(res[m].z) + t1[1]), b1 = pk_bf16(bf_lo(res[m].w) + t1[2], bf_hi(res[m].w) + t1[3]);
        const auto s0 = __builtin_amdgcn_permlane16_swap(b0, a0, false, false), s1 = __builtin_amdgcn_permlane16_swap(b1, a1, false, false);
        const u32x4 ov = {s0[0], s1[0], s0[1], s1[1]};
        const int c8 = ((g & 1) ? 0 : 16) + (g >> 1) * 8;
        *(u32x4*)(P.out + (size_t)(mrow + r) * DM + ct * 128 + wn * 64 + KH * 32 + c8) = ov;
    }
}
template <bool SYNC>
__device__ __forceinline__ void phase5(LAS char* lds, const Params& P, unsigned tag STAMP_ARGS) {
    constexpr int KT = FF / 32, NT = FF / 128;
    const unsigned lds0 = (unsigned)(uintptr_t)lds;
    const int tid = threadIdx.x, lane = tid & 63, w = __builtin_amdgcn_readfirstlane(tid >> 6), g = lane >> 4, r = lane & 15;
    const int b = blockIdx.x, xcd = b & 7, loc = b >> 3;
    const int kh = xcd & 1, rt = (xcd >> 1) * 4 + (loc & 3), ct = loc >> 2;
    char* ws = P.ws;
    if (w >= 4) {
        const int j = w - 4;
        const srd_t srx = mk_srd(ws + WS_ACT, SEQ * FF * 2), srw = mk_srd(P.wd, FF * DM * 2);
        constexpr unsigned XM = FRAG(1, 0, KT), XK = FRAG(0, 1, KT);
        const unsigned xv = lane * 16, xs0 = FRAG(rt * 8 + 2 * j, kh * (KT / 2), KT);
        const int wrow = lane >> 3, wch = (lane & 7) ^ (((wrow >> 1) & 3) << 1);
        const unsigned wv = (unsigned)wrow * 2048u + wch * 16, ws0 = (unsigned)ct * 256u + (unsigned)(kh * (FF / 2) + 16 * j) * 2048u;
        const unsigned ld0 = lds0 + j * 4096, ld1 = lds0 + 16384 + j * 2048;
#define K5_W(t, slot) do { dma16(ld1 + (slot), wv, srw, ws0 + (t) * (64 * 2048)); dma16(ld1 + (slot) + 1024, wv, srw, ws0 + 8 * 2048 + (t) * (64 * 2048)); \
        dma16(ld1 + (slot) + 8192, wv, srw, ws0 + 128 + (t) * (64 * 2048)); dma16(ld1 + (slot) + 9216, wv, srw, ws0 + 128 + 8 * 2048 + (t) * (64 * 2048)); } while (0)
#define K5_X(t, slot) do { dma16c(ld0 + (slot), xv, srx, xs0 + (t) * (2 * XK)); dma16c(ld0 + (slot) + 1024, xv, srx, xs0 + (t) * (2 * XK) + XK); \
        dma16c(ld0 + (slot) + 2048, xv, srx, xs0 + XM + (t) * (2 * XK)); dma16c(ld0 + (slot) + 3072, xv, srx, xs0 + XM + (t) * (2 * XK) + XK); } while (0)
        K5_W(0, 0); K5_W(1, K5_SLOT); K5_W(2, 2 * K5_SLOT); K5_W(3, 3 * K5_SLOT);
        if (SYNC) LBAR();
        K5_X(0, 0); K5_X(1, K5_SLOT); K5_X(2, 2 * K5_SLOT); K5_X(3, 3 * K5_SLOT);
        WAITBAR(12);
        WAITBAR(8);  K5_X(4, 0); K5_W(4, 0);
        WAITBAR(12); K5_X(5, K5_SLOT); K5_W(5, K5_SLOT);
        WAITBAR(16); K5_X(6, 2 * K5_SLOT); K5_W(6, 2 * K5_SLOT);
        int fr = 3 * K5_SLOT;
#pragma unroll 1
        for (int t = 4; t + 3 < NT; ++t) {
            WAITBAR(16);
            K5_X(t + 3, fr); K5_W(t + 3, fr);
            fr = (fr == 3 * K5_SLOT) ? 0 : fr + K5_SLOT;
        }
        WAITBAR(16); WAITBAR(8); WAITBAR(0);
    } else {
        const int wm = w >> 1, wn = w & 1;
        if (SYNC) { if (w == 0) flags_wait(ws, lane < 16 ? FL_4 + (rt >> 1) * 32 + kh * 16 + lane : -1, tag); LBAR(); }
        STAMP_AT(1);
        u32x4 res[4];
#pragma unroll
        for (int m = 0; m < 4; ++m) res[m] = ldc16(mk_srd(ws + WS_H, SEQ * DM * 2), FRAG(rt * 8 + wm * 4 + m, ct * 4 + wn * 2 + kh, 32) + lane * 16);
        const int xrd = wm * 8192 + lane * 16;
        const int q4 = (lane >> 2) & 3, p4 = lane & 3, trow = 4 * g + q4;
        int wrd[4];
#pragma unroll
        for (int n = 0; n < 4; ++n) wrd[n] = 16384 + wn * 8192 + trow * 128 + ((n ^ ((trow >> 1) & 3)) * 32) + (p4 >> 1) * 16 + (p4 & 1) * 8;
        f32x4 acc[4][4];
#pragma unroll
        for (int m = 0; m < 4; ++m)
#pragma unroll
            for (int n = 0; n < 4; ++n) acc[m][n] = (f32x4){0.f, 0.f, 0.f, 0.f};
        bf16x8 xa[4], xb[4]; s16x4 wa[8], wb[8];
#define K5_RD(i, X, W, sl, s) do { if ((i) == 2) X[0] = ldf(lds + (sl) + xrd + (s) * 1024); else if ((i) >= 9) X[(i) - 8] = ldf(lds + (sl) + xrd + ((i) - 8) * 2048 + (s) * 1024); \
        else { const int k_ = (i) < 2 ? (i) : (i) - 1; W[k_] = trd(lds + (sl) + wrd[k_ >> 1] + (k_ & 1) * 2048 + (s) * 4096); } } while (0)
#define K5_MM(i, X, W) do { const int m_ = (i) >> 2, n_ = (i) & 3; acc[m_][n_] = MFMA16(cat8(W[2 * n_], W[2 * n_ + 1]), X[m_], acc[m_][n_]); } while (0)
#define K5_HALF(XM_, WM_, XR, WR, sl, s, DOMMA) do { _Pragma("unroll") for (int i_ = 0; i_ < 16; ++i_) { if (DOMMA) K5_MM(i_, XM_, WM_); if (i_ < 12) K5_RD(i_, XR, WR, sl, s); SCHED0(); } } while (0)
        LBAR();
        STAMP_AT(2);
        K5_HALF(xb, wb, xa, wa, 0, 0, false);
        K5_HALF(xa, wa, xb, wb, 0, 1, true);
        int cur = K5_SLOT;
#pragma unroll 1
        for (int t = 1; t < NT; ++t) {
            LBAR();
            K5_HALF(xb, wb, xa, wa, cur, 0, true);
            K5_HALF(xa, wa, xb, wb, cur, 1, true);
            cur = (cur == 3 * K5_SLOT) ? 0 : cur + K5_SLOT;
        }
#pragma unroll
        for (int i = 0; i < 16; ++i) K5_MM(i, xb, wb);
        STAMP_AT(3);
        if (kh) p5_finish<1>(P, acc, res, tag, b, w, lane, rt, ct STAMP_PASS); else p5_finish<0>(P, acc, res, tag, b, w, lane, rt, ct STAMP_PASS);
    }
}

constexpr int K4_SLOT = 30720, K4_NS = 4, K4_STRAW = K4_NS * K4_SLOT, K4_STAT = K4_STRAW + 32768, K4_CS = K4_STAT + 2048, K4_CSRAW = K4_CS + 1024;
struct K4Dma { srd_t srx, srg, sru; unsigned xv, gv, xs0, gs0, ld0, ld1; };
#define K4_DMA(i, t, slot) do { if ((i) < 2) dma16c(D.ld0 + (slot) + (i) * 1024, D.xv, D.srx, D.xs0 + (t) * FRAG(0, 1, 32) + (i) * FRAG(1, 0, 32)); \
        else dma16(D.ld1 + (slot) + ((i) - 2) * 7168, D.gv, (i) == 2 ? D.srg : D.sru, D.gs0 + (t) * (32 * FF * 2)); } while (0)
template <int NJ, int J0, bool HASW>
__device__ __forceinline__ void k4_body(LAS char* lds, const K4Dma& D, char* ws, int w, int lane, int rt, int ct STAMP_ARGS) {
    const int wm = w & 3, g = lane >> 4, r = lane & 15;
    constexpr int NP = HASW ? 4 : 2;
    const int xrd = wm * 4096 + lane * 16;
    const int q4 = (lane >> 2) & 3, p4 = lane & 3;
    const int wrd = 16384 + (4 * g + q4) * 224 + J0 * 32 + (p4 >> 1) * 16 + (p4 & 1) * 8;
    f32x4 ag[4][NJ], au[4][NJ];
#pragma unroll
    for (int m = 0; m < 4; ++m)
#pragma unroll
        for (int j = 0; j < NJ; ++j) { ag[m][j] = (f32x4){0.f, 0.f, 0.f, 0.f}; au[m][j] = (f32x4){0.f, 0.f, 0.f, 0.f}; }
    bf16x8 xa[4], xb[4]; s16x4 wgf[2 * NJ], wuf[2 * NJ];
#define K4_RW(i, W, sl, o) W[i] = trd(lds + (sl) + wrd + (o) + ((i) >> 1) * 32 + ((i) & 1) * (16 * 224))
#define K4_RX(i, X, sl) X[i] = ldf(lds + (sl) + xrd + (i) * 1024)
#define K4_MM(i, A, X, W) do { const int j_ = (i) >> 2, m_ = (i) & 3; A[m_][j_] = MFMA16(cat8(W[2 * j_], W[2 * j_ + 1]), X[m_], A[m_][j_]); } while (0)
#define K4_RD1(i, XC, sl) do { if ((i) == 0) K4_RX(0, XC, sl); else if ((i) < 3) K4_RW((i) - 1, wgf, sl, 0); else if ((i) < 6) K4_RX((i) - 2, XC, sl); else if ((i) < 4 + 2 * NJ) K4_RW((i) - 4, wgf, sl, 0); } while (0)
#define K4_H1(XC, XP, DOMMA, DODMA, t, sl, fsl) do { \
        _Pragma("unroll") for (int i_ = 0; i_ < 4 * NJ; ++i_) { \
            if (DOMMA) K4_MM(i_, au, XP, wuf); \
            K4_RD1(i_, XC, sl); \
            if (DODMA) { if (i_ == (NJ == 4 ? 13 : 11)) K4_DMA(0, t, fsl); } \
            SCHED0(); } } while (0)
#define K4_H2(XC, sl, DODMA, t, fsl) do { \
        _Pragma("unroll") for (int i_ = 0; i_ < 4 * NJ; ++i_) { \
            K4_MM(i_, ag, XC, wgf); \
            if (i_ < 2 * NJ) K4_RW(i_, wuf, sl, 7168); \
            if (DODMA) { if (NJ == 4) { if (i_ == 8) K4_DMA(1, t, fsl); if (i_ == 11 && NP == 4) K4_DMA(2, t, fsl); if (i_ == 14 && NP == 4) K4_DMA(3, t, fsl); } \
                         else { if (i_ == 6) K4_DMA(1, t, fsl); if (i_ == 8 && NP == 4) K4_DMA(2, t, fsl); if (i_ == 10 && NP == 4) K4_DMA(3, t, fsl); } } \
            SCHED0(); } } while (0)
    constexpr int NT = DM / 32;
    constexpr int NX = HASW ? 4 : 8;
    WAITBAR(4 + NX);
    STAMP_AT(2);
    K4_H1(xa, xb, false, true, 3, 0, 3 * K4_SLOT);
    K4_H2(xa, 0, true, 3, 3 * K4_SLOT);
    int cur = K4_SLOT, fr = 0;
#define K4_ADV() do { fr = (fr == (K4_NS - 1) * K4_SLOT) ? 0 : fr + K4_SLOT; cur = (cur == (K4_NS - 1) * K4_SLOT) ? 0 : cur + K4_SLOT; } while (0)
    WAITBAR(2 + NX + NP); K4_H1(xb, xa, true, true, 4, cur, fr); K4_H2(xb, cur, true, 4, fr); K4_ADV();
    WAITBAR(NX + 2 * NP); K4_H1(xa, xb, true, true, 5, cur, fr); K4_H2(xa, cur, true, 5, fr); K4_ADV();
    WAITBAR(2 * NP);
    {
        const int tid = w * 64 + lane, row = tid >> 1, hf = tid & 1;
        float s = 0.f, q = 0.f;
#pragma unroll
        for (int i = 0; i < 4; ++i) { const f32x4 v = *(const LAS f32x4*)(lds + K4_STRAW + row * 128 + hf * 64 + i * 16); s += v[0] + v[2]; q += v[1] + v[3]; }
        s += __shfl_xor(s, 1); q += __shfl_xor(q, 1);
        const float mean = s * (1.0f / DM);
        const float var = fmaxf(q * (1.0f / DM) - mean * mean, 0.f);
        if (hf == 0) ((LAS f32x2*)(lds + K4_STAT))[row] = (f32x2){mean, 1.0f / sqrtf(var + LN_EPS)};
        if (tid < 224) { const LAS float* cr = (const LAS float*)(lds + K4_CSRAW) + tid; ((LAS float*)(lds + K4_CS))[tid] = (cr[0] + cr[256]) + (cr[512] + cr[768]); }
        SCHED0();
    }
    K4_H1(xb, xa, true, true, 6, cur, fr); K4_H2(xb, cur, true, 6, fr); K4_ADV();
    int t = 4;
#pragma unroll 1
    for (; t + 4 < NT; t += 2) {
        WAITBAR(2 * NP); K4_H1(xa, xb, true, true, t + 3, cur, fr); K4_H2(xa, cur, true, t + 3, fr); K4_ADV();
        WAITBAR(2 * NP); K4_H1(xb, xa, true, true, t + 4, cur, fr); K4_H2(xb, cur, true, t + 4, fr); K4_ADV();
    }
    WAITBAR(2 * NP); K4_H1(xa, xb, true, true, t + 3, cur, fr); K4_H2(xa, cur, true, t + 3, fr); K4_ADV();
    WAITBAR(2 * NP); K4_H1(xb, xa, true, false, 0, cur, fr); K4_H2(xb, cur, false, 0, fr); K4_ADV();
    WAITBAR(NP);     K4_H1(xa, xb, true, false, 0, cur, fr); K4_H2(xa, cur, false, 0, fr); K4_ADV();
    WAITBAR(0);      K4_H1(xb, xa, true, false, 0, cur, fr); K4_H2(xb, cur, false, 0, fr);
#pragma unroll
    for (int i = 0; i < 4 * NJ; ++i) K4_MM(i, au, xb, wuf);
    STAMP_AT(3);
    const LAS f32x2* st = (const LAS f32x2*)(lds + K4_STAT);
    const LAS float* cs = (const LAS float*)(lds + K4_CS);
    f32x2 ms[4];
#pragma unroll
    for (int m = 0; m < 4; ++m) ms[m] = st[wm * 64 + m * 16 + r];
    const srd_t sra = mk_srd(ws + WS_ACT, SEQ * FF * 2);
    const unsigned ap0 = FRAG(rt * 16 + wm * 4, 0, 112) + lane * 16;
    const int ft0 = ct * 7 + J0;
    constexpr int LONE = (NJ == 3) ? (J0 == 4 ? 2 : 0) : -1;
    u32x2 keep[4];
#pragma unroll
    for (int j = 0; j < NJ; ++j) {
        const f32x4 cg = *(const LAS f32x4*)(cs + (J0 + j) * 16 + 4 * g), cu = *(const LAS f32x4*)(cs + 112 + (J0 + j) * 16 + 4 * g);
        const bool second = (LONE == 0) ? (j == 2) : ((j & 1) == 1);
#pragma unroll
        for (int m = 0; m < 4; ++m) {
            const float c1 = -LOG2E * ms[m].y;
            const f32x4 gz = (ag[m][j] - cg * ms[m].x) * c1;
            const f32x4 uu = (au[m][j] - cu * ms[m].x) * ms[m].y;
            float a[4];
#pragma unroll
            for (int e = 0; e < 4; ++e) a[e] = uu[e] * __builtin_amdgcn_rcpf(1.0f + __builtin_amdgcn_exp2f(gz[e]));
            u32x2 ov; ov.x = pk_bf16(a[0], a[1]); ov.y = pk_bf16(a[2], a[3]);
            const int ft = ft0 + j;
            if (j == LONE) st8(sra, ap0 + FRAG(m, ft >> 1, 112) + (ft & 1) * 8, ov);
            else if (!second) keep[m] = ov;
            else st16(sra, ap0 + FRAG(m, ft >> 1, 112), (u32x4){keep[m].x, keep[m].y, ov.x, ov.y});
        }
        SCHED0();
    }
}

template <bool SYNC>
__device__ __forceinline__ void phase4(LAS char* lds, const Params& P, unsigned tag STAMP_ARGS) {
    const unsigned lds0 = (unsigned)(uintptr_t)lds;
    const int tid = threadIdx.x, lane = tid & 63, w = __builtin_amdgcn_readfirstlane(tid >> 6);
    const int b = blockIdx.x, xcd = b & 7, loc = b >> 3;
    const int rt = (xcd >> 2) * 4 + (loc & 3), ct = (xcd & 3) * 8 + (loc >> 2);
    char* ws = P.ws;
    K4Dma D;
    D.srx = mk_srd(ws + WS_H, SEQ * DM * 2); D.srg = mk_srd(P.wg, DM * FF * 2); D.sru = mk_srd(P.wu, DM * FF * 2);
    D.xv = lane * 16; D.xs0 = FRAG(rt * 16 + w * 2, 0, 32);
    { const int ci = (w < 7 ? w : 6) * 64 + lane; const int row = ci / 14, c = ci % 14; D.gv = (unsigned)row * (FF * 2) + c * 16; }
    D.gs0 = (unsigned)ct * 224u; D.ld0 = lds0 + w * 2048; D.ld1 = lds0 + 16384 + w * 1024;
    if (w < 7) {
#pragma unroll
        for (int t = 0; t < 3; ++t) { K4_DMA(2, t, t * K4_SLOT); K4_DMA(3, t, t * K4_SLOT); }
    }
    if (SYNC) {
        if (w == 7) {
            int idx = -1;
            if (lane < 32) idx = FL_3 + rt * 32 + lane;
            else if (lane < 48) { const int k = lane - 32, gsel = k & 3, rq = k >> 2;
                const int c0 = (ct * 112 + ((gsel & 1) ? 111 : 0)) >> 7, bb = rq * 56 + (gsel >> 1) * 28 + c0;
                idx = FL_2 + (bb >> 5) * 32 + (bb & 7) * 4 + ((bb >> 3) & 3); }
            flags_wait(ws, idx, tag);
        }
        LBAR();
    }
    STAMP_AT(1);
#pragma unroll
    for (int t = 0; t < 3; ++t) { K4_DMA(0, t, t * K4_SLOT); K4_DMA(1, t, t * K4_SLOT); }
    {
        const srd_t srs = mk_srd(ws + WS_ST, SEQ * 128);
#pragma unroll
        for (int i = 0; i < 4; ++i) dma16c(lds0 + K4_STRAW + w * 4096 + i * 1024, lane * 16, srs, (unsigned)(rt * 256 + w * 32) * 128u + i * 1024);
        if (w == 7) {
            const srd_t src = mk_srd(ws + WS_CS, 4 * 7168 * 4);
            const unsigned cv = lane < 28 ? (unsigned)(ct * 112 + 4 * lane) * 4u : lane < 56 ? (unsigned)(3584 + ct * 112 + 4 * (lane - 28)) * 4u : 0x7ff00000u;
#pragma unroll
            for (int i = 0; i < 4; ++i) dma16c(lds0 + K4_CSRAW + i * 1024, cv, src, i * 7168 * 4);
        }
    }
    if ((ct & 1) == 0) {
        if (w < 4) k4_body<4, 0, true>(lds, D, ws, w, lane, rt, ct STAMP_PASS);
        else if (w < 7) k4_body<3, 4, true>(lds, D, ws, w, lane, rt, ct STAMP_PASS);
        else k4_body<3, 4, false>(lds, D, ws, w, lane, rt, ct STAMP_PASS);
    } else {
        if (w < 4) k4_body<3, 0, true>(lds, D, ws, w, lane, rt, ct STAMP_PASS);
        else if (w < 7) k4_body<4, 3, true>(lds, D, ws, w, lane, rt, ct STAMP_PASS);
        else k4_body<4, 3, false>(lds, D, ws, w, lane, rt, ct STAMP_PASS);
    }
    STAMP_AT(4);
    WAITBAR(0);
    if (SYNC && tid == 0) flag_set(ws, FL_4 + rt * 32 + ct, tag);
}

constexpr int LDS_BYTES = 160 * 1024;
__global__ __launch_bounds__(512, 2) void mega(Params P) {
    extern __shared__ __attribute__((aligned(16))) char smem_g[];
    LAS char* lds = (LAS char*)smem_g;
    char* ws = P.ws;
    const unsigned tag = __hip_atomic_load((gu32*)(ws + WS_FLG) + FL_EPOCH, RLX_AGENT) + 1u;
    { STAMP_DECL STAMP_AT(0); phase1<true>(lds, P, tag STAMP_PASS); STAMP_OUT(1); }
    { STAMP_DECL STAMP_AT(0); phase2<true>(lds, P, tag STAMP_PASS); STAMP_OUT(2); }
    { STAMP_DECL STAMP_AT(0); phase35<0, true>(lds, P, tag STAMP_PASS); STAMP_OUT(3); }
    { STAMP_DECL STAMP_AT(0); phase4<true>(lds, P, tag STAMP_PASS); STAMP_OUT(4); }
    { STAMP_DECL STAMP_AT(0); phase5<true>(lds, P, tag STAMP_PASS); STAMP_OUT(5); }
    if (blockIdx.x == 0 && threadIdx.x == 0) __hip_atomic_store((gu32*)(ws + WS_FLG) + FL_EPOCH, tag, RLX_AGENT);
}

extern "C" void kernel_launch(void* const* d_in, const int* in_sizes, int n_in, void* d_out, int out_size, void* d_ws, size_t ws_size, hipStream_t stream) {
    Params P;
    P.x = (const u16*)d_in[0]; P.wqkv = (const u16*)d_in[1]; P.wo = (const u16*)d_in[2];
    P.wg = (const u16*)d_in[4]; P.wu = (const u16*)d_in[5]; P.wd = (const u16*)d_in[6];
    P.out = (u16*)d_out; P.ws = (char*)d_ws;
    (void)hipFuncSetAttribute((const void*)mega, hipFuncAttributeMaxDynamicSharedMemorySize, LDS_BYTES);
    mega<<<256, 512, LDS_BYTES, stream>>>(P);
}
```

```cpp
#include <hip/hip_runtime.h>
#include <hip/hip_bf16.h>
#include <stdint.h>

#define LAS __attribute__((address_space(3)))
typedef unsigned short u16;
typedef short bf16x8 __attribute__((ext_vector_type(8)));
typedef short s16x4 __attribute__((ext_vector_type(4)));
typedef float f32x2 __attribute__((ext_vector_type(2)));
typedef float f32x4 __attribute__((ext_vector_type(4)));
typedef float f32x16 __attribute__((ext_vector_type(16)));
typedef unsigned u32x2 __attribute__((ext_vector_type(2)));
typedef unsigned u32x4 __attribute__((ext_vector_type(4)));
typedef __bf16 bf16x2_t __attribute__((ext_vector_type(2)));
typedef __amdgpu_buffer_rsrc_t srd_t;

constexpr int SEQ = 2048, DM = 1024, NH = 32, FF = 3584;
constexpr float LN_EPS = 1e-5f;
constexpr float QSCALE = 0.17677669529663687f * 1.4426950408889634f;
constexpr float LOG2E = 1.4426950408889634f;

constexpr size_t WS_Q = 0;
constexpr size_t WS_K = WS_Q + (4u << 20);
constexpr size_t WS_V = WS_K + (4u << 20);
constexpr size_t WS_O = WS_V + (4u << 20);
constexpr size_t WS_H = WS_O + (4u << 20);
constexpr size_t WS_ACT = WS_H + (4u << 20);
constexpr size_t WS_ST = WS_ACT + (size_t)SEQ * FF * 2;
constexpr size_t WS_CS = WS_ST + (size_t)SEQ * 16 * 8;
constexpr size_t WS_S1 = WS_CS + 4 * 7168 * 4;
constexpr size_t WS_C1 = WS_S1 + SEQ * 8;
constexpr size_t WS_FLG = WS_C1 + 16 * 3072 * 4;
constexpr size_t WS_XCH = WS_FLG + 16384;
constexpr size_t WS_END = WS_XCH + (8u << 20);
constexpr int FL_EPOCH = 0, FL_ERR = 32, FL_0 = 64, FL_1 = FL_0 + 256, FL_2 = FL_1 + 256, FL_3 = FL_2 + 256, FL_4 = FL_3 + 256, FL_5 = FL_4 + 256;

#define FRAG(mt, kt, KT) (((unsigned)(kt) * 128u + (unsigned)(mt)) * 1024u)
#define ST_AUX 16

__device__ __forceinline__ unsigned pk_bf16(float lo, float hi) { f32x2 v = {lo, hi}; bf16x2_t b = __builtin_convertvector(v, bf16x2_t); return __builtin_bit_cast(unsigned, b); }
__device__ __forceinline__ float bf_lo(unsigned u) { return __uint_as_float(u << 16); }
__device__ __forceinline__ float bf_hi(unsigned u) { return __uint_as_float(u & 0xffff0000u); }
__device__ __forceinline__ s16x4 trd(LAS const char* p) { return __builtin_bit_cast(s16x4, __builtin_amdgcn_ds_read_tr16_b64_v4i16((LAS s16x4*)p)); }
__device__ __forceinline__ bf16x8 cat8(s16x4 a, s16x4 b) { return (bf16x8){a[0], a[1], a[2], a[3], b[0], b[1], b[2], b[3]}; }
__device__ __forceinline__ bf16x8 ldf(LAS const char* p) { return *(LAS const bf16x8*)p; }
__device__ __forceinline__ srd_t mk_srd(const void* p, unsigned bytes) { return __builtin_amdgcn_make_buffer_rsrc((void*)p, (short)0, (int)bytes, 0x00020000); }
__device__ __forceinline__ void dma16(unsigned ldsa, unsigned voff, srd_t srd, unsigned soff) {
    asm volatile("s_mov_b32 m0, %0\n\ts_nop 0\n\tbuffer_load_dwordx4 %1, %2, %3 offen lds" :: "s"(ldsa), "v"(voff), "s"(srd), "s"(soff));
}
__device__ __forceinline__ void dma16c(unsigned ldsa, unsigned voff, srd_t srd, unsigned soff) {
    asm volatile("s_mov_b32 m0, %0\n\ts_nop 0\n\tbuffer_load_dwordx4 %1, %2, %3 offen sc1 lds" :: "s"(ldsa), "v"(voff), "s"(srd), "s"(soff));
}
__device__ __forceinline__ void st8(srd_t srd, unsigned off, u32x2 v) { __builtin_amdgcn_raw_buffer_store_b64(v, srd, off, 0, ST_AUX); }
__device__ __forceinline__ void st16(srd_t srd, unsigned off, u32x4 v) { __builtin_amdgcn_raw_buffer_store_b128(v, srd, off, 0, ST_AUX); }
__device__ __forceinline__ u32x4 pk8(const f32x4 a, const f32x4 b) { u32x4 o; o.x = pk_bf16(a[0], a[1]); o.y = pk_bf16(a[2], a[3]); o.z = pk_bf16(b[0], b[1]); o.w = pk_bf16(b[2], b[3]); return o; }
#define MFMA16(a, b, c) __builtin_amdgcn_mfma_f32_16x16x32_bf16(a, b, c, 0, 0, 0)
#define MFMA32(a, b, c) __builtin_amdgcn_mfma_f32_32x32x16_bf16(a, b, c, 0, 0, 0)
#define WAITVL(n) asm volatile("s_waitcnt vmcnt(%0) lgkmcnt(0)" :: "n"(n) : "memory")
#define WAITBAR(n) asm volatile("s_waitcnt vmcnt(%0) lgkmcnt(0)\n\ts_barrier" :: "n"(n) : "memory")
#define LBAR() asm volatile("s_waitcnt lgkmcnt(0)\n\ts_barrier" ::: "memory")
#define SCHED0() __builtin_amdgcn_sched_barrier(0)
#define WAITV(n) asm volatile("s_waitcnt vmcnt(%0)" :: "n"(n) : "memory")
#define RLX_AGENT __ATOMIC_RELAXED, __HIP_MEMORY_SCOPE_AGENT
typedef __attribute__((address_space(1))) unsigned gu32;
typedef __attribute__((address_space(1))) unsigned long long gu64;
struct Params { const u16 *x, *wqkv, *wo, *wg, *wu, *wd; u16* out; char* ws; };
__device__ __forceinline__ void flag_set(char* ws, int idx, unsigned tag) { __hip_atomic_store((gu32*)(ws + WS_FLG) + idx, tag, RLX_AGENT); }
__device__ __forceinline__ void flags_wait(char* ws, int idx, unsigned tag) {
    gu32* fl = (gu32*)(ws + WS_FLG);
    const unsigned long long t0 = __builtin_amdgcn_s_memrealtime();
    for (unsigned spins = 1;; ++spins) {
        const unsigned v = idx >= 0 ? __hip_atomic_load(fl + idx, RLX_AGENT) : tag;
        if (__all(v == tag)) break;
        if ((spins & 63u) == 0u) {
            if (__any(__hip_atomic_load(fl + FL_ERR, RLX_AGENT) == tag)) break;
            if (__builtin_amdgcn_s_memrealtime() - t0 > 2000000ull) { __hip_atomic_store(fl + FL_ERR, tag, RLX_AGENT); break; }
        }
        __builtin_amdgcn_s_sleep(1);
    }
    asm volatile("" ::: "memory");
}
template <int CTRL, int RM> __device__ __forceinline__ float dpp_add(float v) { return v + __int_as_float(__builtin_amdgcn_update_dpp(0, __float_as_int(v), CTRL, RM, 0xf, true)); }
__device__ __forceinline__ float sum32(float v) { v = dpp_add<0x111, 0xf>(v); v = dpp_add<0x112, 0xf>(v); v = dpp_add<0x114, 0xf>(v); v = dpp_add<0x118, 0xf>(v); return dpp_add<0x142, 0xa>(v); }
__device__ __forceinline__ u32x4 ldc16(srd_t srd, unsigned off) { return __builtin_amdgcn_raw_buffer_load_b128(srd, off, 0, 16); }
__device__ __forceinline__ float ldc4(srd_t srd, unsigned off) { return __uint_as_float(__builtin_amdgcn_raw_buffer_load_b32(srd, off, 0, 16)); }

#define STAMP_DECL
#define STAMP_AT(i)
#define STAMP_OUT(kid)
#define STAMP_ARGS
#define STAMP_PASS

constexpr int K1_SLOT = 40960, K1_NS = 4, K1_AUX = 0;
template <bool SYNC>
__device__ __forceinline__ void phase1(LAS char* lds, const Params& P, unsigned tag STAMP_ARGS) {
    const unsigned lds0 = (unsigned)(uintptr_t)lds;
    const int tid = threadIdx.x, lane = tid & 63, w = __builtin_amdgcn_readfirstlane(tid >> 6), g = lane >> 4, r = lane & 15;
    const int b = blockIdx.x, xcd = b & 7, loc = b >> 3;
        const int p = (xcd & 3) * 4 + (loc & 3), rt = (xcd >> 2) * 8 + (loc >> 2);
    char* ws = P.ws;
    constexpr int NT = DM / 64;
    LAS f32x2* stat = (LAS f32x2*)(lds + K1_AUX);
    LAS float* csl = (LAS float*)(lds + K1_AUX + 1024);
    if (w >= 4) {
        const srd_t srx = mk_srd(P.x, SEQ * DM * 2), srw = mk_srd(P.wqkv, DM * 3 * DM * 2);
        const int j = w - 4, rr = lane >> 3, c8 = lane & 7;
        unsigned xv[2], wv[2];
#pragma unroll
        for (int i = 0; i < 2; ++i) {
            xv[i] = rr * 2048 + ((c8 ^ (4 * i + (rr >> 1))) * 16);
            const int f = ((rr >> 1) & 1) | (i << 1);
            wv[i] = rr * 6144 + (((c8 >> 1) ^ f) * 32) + (c8 & 1) * 16;
        }
        const unsigned xs0 = (unsigned)(rt * 128 + 32 * j) * 2048u, ws0 = (unsigned)p * 128u + (unsigned)(16 * j) * 6144u;
        const unsigned xl0 = lds0 + j * 4096, wl0 = lds0 + 16384 + j * 2048;
#define K1_DMA(i, t, slot) do { if ((i) < 4) dma16(xl0 + (slot) + (i) * 1024, xv[(i) & 1], srx, xs0 + (i) * (8 * 2048) + (((t) + rt) & 15) * 128); \
        else dma16(wl0 + (slot) + (((i) - 4) >> 1) * 8192 + (((i) - 4) & 1) * 1024, wv[((i) - 4) & 1], srw, ws0 + (((i) - 4) >> 1) * 2048 + (((i) - 4) & 1) * (8 * 6144) + (((t) + rt) & 15) * (64 * 6144)); } while (0)
#define K1_STAGE(t, slot) do { _Pragma("unroll") for (int i_ = 0; i_ < 10; ++i_) K1_DMA(i_, t, slot); } while (0)
        const int srow = 8 * b + 2 * j + (lane >> 5);
        u32x4 xr[4];
        { const unsigned xo = (unsigned)srow * 2048u + (lane & 31) * 16;
#pragma unroll
          for (int k = 0; k < 4; ++k) asm volatile("buffer_load_dwordx4 %0, %1, %2, 0 offen offset:%3" : "=v"(xr[k]) : "v"(xo), "s"(srx), "n"(k * 512)); }
        K1_STAGE(0, 0); K1_STAGE(1, K1_SLOT); K1_STAGE(2, 2 * K1_SLOT);
        WAITBAR(20);
        asm volatile("" : "+v"(xr[0]), "+v"(xr[1]), "+v"(xr[2]), "+v"(xr[3]));
        K1_STAGE(3, 3 * K1_SLOT);
        {
            const unsigned x00 = xr[0][0];
            const float pv = bf_lo(lane < 32 ? __builtin_amdgcn_readlane(x00, 0) : __builtin_amdgcn_readlane(x00, 32));
            float s = 0.f, q = 0.f;
#pragma unroll
            for (int k = 0; k < 4; ++k)
#pragma unroll
                for (int e = 0; e < 4; ++e) { const unsigned d = xr[k][e]; const float u = bf_lo(d) - pv, v = bf_hi(d) - pv; s += u + v; q += u * u + v * v; }
            s = sum32(s); q = sum32(q);
            const float dm = s * (1.0f / DM), var = fmaxf(q * (1.0f / DM) - dm * dm, 0.f);
            const u32x2 sv = {__float_as_uint(pv + dm), __float_as_uint(1.0f / sqrtf(var + LN_EPS))};
            __builtin_amdgcn_raw_buffer_store_b64(sv, mk_srd(ws + WS_S1, SEQ * 8), (lane & 31) == 31 ? (unsigned)srow * 8u : 0x7ff00000u, 0, 16);
        }
        WAITBAR(21); K1_STAGE(4, 0);
        WAITBAR(21); K1_STAGE(5, K1_SLOT);
        WAITBAR(21); K1_STAGE(6, 2 * K1_SLOT);
        WAITBAR(20);
        if (SYNC && tid == 256) flag_set(ws, FL_0 + b, tag);
        K1_STAGE(7, 3 * K1_SLOT);
        WAITBAR(20); K1_STAGE(8, 0);
        WAITBAR(20); K1_STAGE(9, K1_SLOT);
        WAITBAR(20); K1_STAGE(10, 2 * K1_SLOT);
        WAITBAR(20);
        const int fidx = lane < 16 ? FL_0 + 16 * rt + lane : lane < 32 ? FL_0 + (((lane - 16) & 7) * 4 + (p & 3)) * 8 + ((lane - 16) >> 3) * 4 + (p >> 2) : -1;
        unsigned fseen;
        asm volatile("global_load_dword %0, %1, off sc1" : "=v"(fseen) : "v"((const char*)(ws + WS_FLG) + (fidx >= 0 ? fidx : FL_0 + b) * 4));
        K1_STAGE(11, 3 * K1_SLOT);
        WAITBAR(21); K1_STAGE(12, 0);
        WAITBAR(21); K1_STAGE(13, K1_SLOT);
        WAITBAR(20);
        asm volatile("" : "+v"(fseen));
        K1_STAGE(14, 2 * K1_SLOT);
        if (SYNC && !__all(fseen == tag)) flags_wait(ws, fidx, tag);
        u32x4 gs, gp[16];
        const int tc = 48 * j + 4 * lane;
        { const srd_t srs = mk_srd(ws + WS_S1, SEQ * 8), src1 = mk_srd(ws + WS_C1, 16 * 3072 * 4);
          const unsigned so = j == 0 ? (unsigned)(rt * 128) * 8u + lane * 16 : 0x7ff00000u, po = lane < 12 ? (unsigned)((tc >> 6) * 1024 + p * 64 + (tc & 63)) * 4u : 0x7ff00000u;
          asm volatile("buffer_load_dwordx4 %0, %1, %2, 0 offen sc1" : "=v"(gs) : "v"(so), "s"(srs));
#pragma unroll
          for (int i = 0; i < 16; ++i) asm volatile("buffer_load_dwordx4 %0, %1, %2, %3 offen sc1" : "=v"(gp[i]) : "v"(po), "s"(src1), "s"(i * 3072 * 4)); }
        WAITBAR(37); K1_STAGE(15, 3 * K1_SLOT);
        WAITBAR(37);
        WAITBAR(10);
        asm volatile("" : "+v"(gs), "+v"(gp[0]), "+v"(gp[1]), "+v"(gp[2]), "+v"(gp[3]), "+v"(gp[4]), "+v"(gp[5]), "+v"(gp[6]), "+v"(gp[7]));
        asm volatile("" : "+v"(gp[8]), "+v"(gp[9]), "+v"(gp[10]), "+v"(gp[11]), "+v"(gp[12]), "+v"(gp[13]), "+v"(gp[14]), "+v"(gp[15]));
        {
            f32x4 v = {0.f, 0.f, 0.f, 0.f};
#pragma unroll
            for (int i = 0; i < 16; ++i) v += __builtin_bit_cast(f32x4, gp[i]);
            if (lane < 12) *(LAS f32x4*)(csl + tc) = v;
            if (j == 0) *(LAS u32x4*)(lds + K1_AUX + lane * 16) = gs;
        }
        WAITBAR(0);
    } else {
        const int wr = w >> 1, wc = w & 1;
        int xrd[2], wrd[2];
        { const int q4 = (lane >> 2) & 3, p4 = lane & 3, trow = 8 * g + q4, f = ((trow >> 1) & 1) | (((trow >> 3) & 1) << 1);
#pragma unroll
          for (int s = 0; s < 2; ++s) { xrd[s] = (64 * wr + r) * 128 + (((4 * s + g) ^ ((r >> 1) & 7)) * 16); wrd[s] = 16384 + trow * 128 + (((2 * wc + s) ^ f) * 32) + (p4 >> 1) * 16 + (p4 & 1) * 8; } }
        f32x4 acc[4][6];
#pragma unroll
        for (int m = 0; m < 4; ++m)
#pragma unroll
            for (int n = 0; n < 6; ++n) acc[m][n] = (f32x4){0.f, 0.f, 0.f, 0.f};
        bf16x8 xa[4], xb[4]; s16x4 wa[12], wb[12];
#define K1_RW(i, W, sl, s) W[i] = trd(lds + (sl) + wrd[((i) >> 1) & 1] + ((i) >> 2) * 8192 + (s) * 4096 + ((i) & 1) * 512)
#define K1_RX(i, X, sl, s) X[i] = ldf(lds + (sl) + xrd[s] + (i) * 2048)
#define K1_RD(i, X, W, sl, s) do { if ((i) == 2) K1_RX(0, X, sl, s); else if ((i) == 7) K1_RX(1, X, sl, s); else if ((i) == 12) K1_RX(2, X, sl, s); else if ((i) == 15) K1_RX(3, X, sl, s); \
        else K1_RW((i) < 2 ? (i) : (i) < 7 ? (i) - 1 : (i) < 12 ? (i) - 2 : (i) - 3, W, sl, s); } while (0)
#define K1_MM(i, X, W) do { const int m_ = (i) / 6, n_ = (i) % 6; const bf16x8 wf_ = cat8(W[2 * n_], W[2 * n_ + 1]); \
        acc[m_][n_] = (n_ < 4) ? MFMA16(wf_, X[m_], acc[m_][n_]) : MFMA16(X[m_], wf_, acc[m_][n_]); } while (0)
#define K1_HALF(XM, WM, XR, WR, sl, s, DOMMA) do { \
        _Pragma("unroll") for (int i_ = 0; i_ < 24; ++i_) { \
            if (DOMMA) K1_MM(i_, XM, WM); \
            if (i_ < 16) K1_RD(i_, XR, WR, sl, s); \
            SCHED0(); } } while (0)
        LBAR();
        STAMP_AT(2);
        K1_HALF(xb, wb, xa, wa, 0, 0, false);
        K1_HALF(xa, wa, xb, wb, 0, 1, true);
        if (wr == 0) {
            bf16x8 one8;
#pragma unroll
            for (int e = 0; e < 8; ++e) one8[e] = (short)0x3f80;
            const srd_t src1 = mk_srd(ws + WS_C1, 16 * 3072 * 4);
#pragma unroll
            for (int n = 0; n < 6; ++n) {
                f32x4 c = MFMA16(cat8(wa[2 * n], wa[2 * n + 1]), one8, ((f32x4){0.f, 0.f, 0.f, 0.f}));
                c = MFMA16(cat8(wb[2 * n], wb[2 * n + 1]), one8, c);
                __builtin_amdgcn_raw_buffer_store_b128(__builtin_bit_cast(u32x4, c), src1, r == 0 ? (unsigned)(rt * 3072 + (n >> 1) * 1024 + p * 64 + wc * 32 + (n & 1) * 16 + 4 * g) * 4u : 0x7ff00000u, 0, 16);
            }
        }
        int cur = K1_SLOT;
        LBAR();
        K1_HALF(xb, wb, xa, wa, cur, 0, true);
        K1_HALF(xa, wa, xb, wb, cur, 1, true);
        cur = 2 * K1_SLOT;
        LBAR();
        K1_HALF(xb, wb, xa, wa, cur, 0, true);
        K1_HALF(xa, wa, xb, wb, cur, 1, true);
        cur = 3 * K1_SLOT;
        WAITBAR(0);
        K1_HALF(xb, wb, xa, wa, cur, 0, true);
        K1_HALF(xa, wa, xb, wb, cur, 1, true);
        cur = 0;
#pragma unroll 1
        for (int t = 4; t < NT; ++t) {
            LBAR();
            K1_HALF(xb, wb, xa, wa, cur, 0, true);
            K1_HALF(xa, wa, xb, wb, cur, 1, true);
            cur = (cur == 3 * K1_SLOT) ? 0 : cur + K1_SLOT;
        }
#pragma unroll
        for (int i = 0; i < 24; ++i) K1_MM(i, xb, wb);
        STAMP_AT(3);
        const int head = 2 * p + wc;
        const srd_t srq = mk_srd(ws + WS_Q, 12u << 20);
        f32x4 cq[4]; float cv[2];
#pragma unroll
        for (int n = 0; n < 4; ++n) cq[n] = *(LAS const f32x4*)(csl + (n >> 1) * 64 + wc * 32 + (n & 1) * 16 + 4 * g);
#pragma unroll
        for (int n = 0; n < 2; ++n) cv[n] = csl[128 + wc * 32 + n * 16 + r];
        const int rho = 16 * (r >> 3) + 4 * ((r >> 2) & 1) + (r & 3);
        f32x4 vv[2][2];
#pragma unroll
        for (int m = 0; m < 4; ++m) {
            const int lrow = wr * 64 + m * 16, row0 = rt * 128 + lrow;
            const unsigned blk = ((unsigned)head * 64 + (row0 >> 5)) * 2048u + (g >> 1) * 1024 + (g & 1) * 512;
            const f32x2 st = stat[lrow + r];
            const float qs = st.y * QSCALE;
            st16(srq, (4u << 20) + blk + (rho + (m & 1) * 8) * 16, pk8((acc[m][2] - st.x * cq[2]) * st.y, (acc[m][3] - st.x * cq[3]) * st.y));
            const f32x4 s01 = *(LAS const f32x4*)(stat + lrow + 4 * g), s23 = *(LAS const f32x4*)(stat + lrow + 4 * g + 2);
            const f32x4 mean4 = {s01[0], s01[2], s23[0], s23[2]}, rstd4 = {s01[1], s01[3], s23[1], s23[3]};
#pragma unroll
            for (int n = 0; n < 2; ++n) vv[m & 1][n] = (acc[m][4 + n] - mean4 * cv[n]) * rstd4;
            if (m & 1) {
#pragma unroll
                for (int n = 0; n < 2; ++n) st16(srq, (8u << 20) + blk + (16 * n + r) * 16, pk8(vv[0][n], vv[1][n]));
            }
            st16(srq, blk + ((m & 1) * 16 + r) * 16, pk8((acc[m][0] - st.x * cq[0]) * qs, (acc[m][1] - st.x * cq[1]) * qs));
        }
    }
    STAMP_AT(4);
    WAITBAR(0);
    if (SYNC && tid == 0) flag_set(ws, FL_1 + p * 16 + rt, tag);
}

constexpr int K2_SLOT = 32768, K2_SIDE = 3 * K2_SLOT;
struct K2Side { srd_t src, srp, sro; unsigned cv, cs0, ps0; };
template <int KH>
__device__ __forceinline__ void attn_fast(LAS char* lds, srd_t srk, srd_t srv, unsigned hbase, const K2Side& S, int w, int lane, const bf16x8 (&qf)[2][2], f32x16 (&o)[2], f32x4 (&lacc)[2], f32x4& cs) {
    const int tl = w & 3;
    const unsigned lds0 = (unsigned)(uintptr_t)lds;
    const unsigned lv = lane * 16;
    const unsigned s0 = hbase + (unsigned)(KH * 32 + tl) * 2048u, d0 = lds0 + KH * 16384 + tl * 2048;
    const int rd0 = KH * 16384 + lane * 16;
    bf16x8 ones, sel;
    { const short v = ((lane & 15) == ((lane >> 4) & 1)) ? (short)0x3f80 : (short)0;
#pragma unroll
      for (int j = 0; j < 8; ++j) { ones[j] = v; sel[j] = ((lane & 15) == j) ? (short)0x3f80 : (short)0; } }
    const unsigned sd = lds0 + K2_SIDE + w * 1024;
#define K2_DMA(k, rd, fr) do { \
        if ((k) < 4) dma16c(d0 + (fr) + ((k) >> 1) * 8192 + ((k) & 1) * 1024, lv, (k) < 2 ? srk : srv, s0 + ((rd) + 2) * 8192 + ((k) & 1) * 1024); \
        else if ((k) == 4) dma16(sd + sc, S.cv, S.src, S.cs0 + (rd) * (4 * FF * 2)); \
        else dma16(sd + 24576, lv, (rd) < 4 ? S.srp : S.sro, (rd) < 4 ? S.ps0 + (rd) * 1024 : (rd) == 4 ? (S.ps0 >> 2) : 0x7ff00000u); } while (0)
#define K2_HOOK(gi, rd, fr) do { if (((gi) >> 1) < 6 && ((gi) & 1) == KH) K2_DMA((gi) >> 1, rd, fr); } while (0)
    f32x16 sa[2], sb[2];
    const f32x16 zero16 = {};
    bf16x8 k0 = ldf(lds + rd0), k1 = ldf(lds + rd0 + 1024), v0, v1;
#pragma unroll
    for (int qt = 0; qt < 2; ++qt) { sa[qt] = MFMA32(k0, qf[qt][0], zero16); sa[qt] = MFMA32(k1, qf[qt][1], sa[qt]); }
    k0 = ldf(lds + rd0 + 2048);
    u32x4 pa = {0u, 0u, 0u, 0u}, pb = {0u, 0u, 0u, 0u}; float e6 = 0.f, e7 = 0.f;
    v1 = k1;
    bf16x8 csd = {};
#define EX2(x) __builtin_amdgcn_exp2f(x)
#define K2_GROUP(S_, B, PC, PP, QK, VP, QP, RD, gi) do { \
        QK; PP[3] = pk_bf16(e6, e7); SCHED0(); \
        const float x0 = EX2(S_[B + 0]), x1 = EX2(S_[B + 1]), x2 = EX2(S_[B + 2]); RD; SCHED0(); \
        o[QP] = MFMA32(VP, __builtin_bit_cast(bf16x8, PP), o[QP]); SCHED0(); \
        const float x3 = EX2(S_[B + 3]), x4 = EX2(S_[B + 4]), x5 = EX2(S_[B + 5]); PC[0] = pk_bf16(x0, x1); SCHED0(); \
        lacc[QP] = MFMA16(ones, __builtin_bit_cast(bf16x8, PP), lacc[QP]); K2_HOOK(gi, rd, fr); SCHED0(); \
        e6 = EX2(S_[B + 6]); e7 = EX2(S_[B + 7]); PC[1] = pk_bf16(x2, x3); PC[2] = pk_bf16(x4, x5); SCHED0(); } while (0)
#define K2_TILE(SC, SN, kn1, kn2, va, ti) do { \
        K2_GROUP(SC[0], 0, pa, pb, SN[0] = MFMA32(k0, qf[0][0], zero16), v1, 1, v0 = ldf(va), 4 * (ti)); \
        K2_GROUP(SC[1], 0, pb, pa, SN[1] = MFMA32(k0, qf[1][0], zero16), v0, 0, k1 = ldf((kn1) + 1024), 4 * (ti) + 1); \
        K2_GROUP(SC[0], 8, pa, pb, SN[0] = MFMA32(k1, qf[0][1], SN[0]), v0, 1, v1 = ldf((va) + 1024), 4 * (ti) + 2); \
        K2_GROUP(SC[1], 8, pb, pa, SN[1] = MFMA32(k1, qf[1][1], SN[1]), v1, 0, k0 = ldf(kn2), 4 * (ti) + 3); } while (0)
    int cur = 0, fr = 2 * K2_SLOT, sc = 0;
#pragma unroll 1
    for (int rd = 0; rd < 8; ++rd) {
        const int nx = (cur == 2 * K2_SLOT) ? 0 : cur + K2_SLOT;
        const int sn = (sc == 16384) ? 0 : sc + 8192;
        if (rd > 0) WAITBAR(2);
        LAS const char* pc = lds + cur + rd0; LAS const char* pn = lds + nx + rd0;
        K2_TILE(sa, sb, pc + 2048, pc + 4096, pc + 8192, 0);
        csd = ldf(lds + K2_SIDE + sn + w * 1024 + lane * 16);
        if (rd < 2) csd = (bf16x8){};
        K2_TILE(sb, sa, pc + 4096, pc + 6144, pc + 8192 + 2048, 1);
        cs = MFMA16(sel, csd, cs);
        K2_TILE(sa, sb, pc + 6144, pn, pc + 8192 + 4096, 2);
        K2_TILE(sb, sa, pn, pn + 2048, pc + 8192 + 6144, 3);
        fr = cur; cur = nx; sc = sn;
    }
    pb[3] = pk_bf16(e6, e7);
    o[1] = MFMA32(v1, __builtin_bit_cast(bf16x8, pb), o[1]);
    lacc[1] = MFMA16(ones, __builtin_bit_cast(bf16x8, pb), lacc[1]);
    WAITBAR(0);
    cs = MFMA16(sel, ldf(lds + K2_SIDE + w * 1024 + lane * 16), cs);
    cs = MFMA16(sel, ldf(lds + K2_SIDE + 8192 + w * 1024 + lane * 16), cs);
#undef K2_GROUP
#undef K2_TILE
#undef K2_DMA
#undef K2_HOOK
}
#define K2_STAGE(rd, slot) do { dma16c(d0 + (slot), lv, srk, s0 + (rd) * 8192); dma16c(d0 + (slot) + 1024, lv, srk, s0 + (rd) * 8192 + 1024); \
        dma16c(d0 + (slot) + 8192, lv, srv, s0 + (rd) * 8192); dma16c(d0 + (slot) + 9216, lv, srv, s0 + (rd) * 8192 + 1024); } while (0)
__device__ __forceinline__ void attn_safe(LAS char* lds, srd_t srk, srd_t srv, unsigned hbase, const bf16x8 (&qf)[2][2], int w, int lane, f32x16 (&o)[2], float (&mrun)[2], float (&lrun)[2]) {
    const int kh = w >> 2, tl = w & 3;
    const unsigned lds0 = (unsigned)(uintptr_t)lds;
    const unsigned lv = lane * 16;
    const unsigned s0 = hbase + (unsigned)(kh * 32 + tl) * 2048u, d0 = lds0 + kh * 16384 + tl * 2048;
    const int rd0 = kh * 16384 + lane * 16;
    const f32x16 zero16 = {};
#pragma unroll 1
    for (int rd = 0; rd < 8; ++rd) {
        K2_STAGE(rd, 0);
        WAITBAR(0);
#pragma unroll 1
        for (int i = 0; i < 4; ++i) {
            LAS const char* pk = lds + rd0 + i * 2048;
            const bf16x8 k0 = ldf(pk), k1 = ldf(pk + 1024), v0 = ldf(pk + 8192), v1 = ldf(pk + 9216);
#pragma unroll
            for (int qt = 0; qt < 2; ++qt) {
                f32x16 sc = MFMA32(k0, qf[qt][0], zero16); sc = MFMA32(k1, qf[qt][1], sc);
                float mx = sc[0];
#pragma unroll
                for (int j = 1; j < 16; ++j) mx = fmaxf(mx, sc[j]);
                mx = fmaxf(mx, __shfl_xor(mx, 32));
                const float mn = fmaxf(mrun[qt], mx);
                const float ms = (mn == -INFINITY) ? 0.f : mn;
                const float al = __builtin_amdgcn_exp2f(mrun[qt] - ms);
                mrun[qt] = mn;
                u32x4 p0, p1;
#pragma unroll
                for (int j = 0; j < 4; ++j) { p0[j] = pk_bf16(__builtin_amdgcn_exp2f(sc[2 * j] - ms), __builtin_amdgcn_exp2f(sc[2 * j + 1] - ms));
                                              p1[j] = pk_bf16(__builtin_amdgcn_exp2f(sc[8 + 2 * j] - ms), __builtin_amdgcn_exp2f(sc[9 + 2 * j] - ms)); }
                float s_ = 0.f;
#pragma unroll
                for (int j = 0; j < 4; ++j) { const unsigned a = p0[j], c = p1[j]; s_ += bf_lo(a) + bf_hi(a); s_ += bf_lo(c) + bf_hi(c); }
                lrun[qt] = lrun[qt] * al + s_;
#pragma unroll
                for (int j = 0; j < 16; ++j) o[qt][j] *= al;
                o[qt] = MFMA32(v0, __builtin_bit_cast(bf16x8, p0), o[qt]); o[qt] = MFMA32(v1, __builtin_bit_cast(bf16x8, p1), o[qt]);
            }
        }
        WAITBAR(0);
    }
}

template <bool SYNC>
__device__ __forceinline__ void phase2(LAS char* lds, const Params& P, unsigned tag STAMP_ARGS) {
    const int tid = threadIdx.x, lane = tid & 63, w = __builtin_amdgcn_readfirstlane(tid >> 6);
    const int b = blockIdx.x, xcd = b & 7, loc = b >> 3;
    const int head = xcd * 4 + (loc & 3), qb = loc >> 2;
    char* ws = P.ws;
    K2Side S;
    const int cgrp = b % 56, rq = b / 56;
    const bool do_cs = b < 224;
    S.src = mk_srd(cgrp < 28 ? P.wg : P.wu, DM * FF * 2);
    S.cv = (unsigned)(lane >> 4) * (FF * 2) + (lane & 15) * 16;
    S.cs0 = do_cs ? (unsigned)(rq * 256 + w * 32) * (FF * 2) + (unsigned)(cgrp < 28 ? cgrp : cgrp - 28) * 256u : 0x7ff00000u;
    S.srp = mk_srd(P.wd, FF * DM * 2); S.sro = mk_srd(P.wo, DM * DM * 2);
    S.ps0 = (unsigned)(b * 8 + w) * 4096u;
    const srd_t srk = mk_srd(ws + WS_K, SEQ * DM * 2), srv = mk_srd(ws + WS_V, SEQ * DM * 2);
    const unsigned hbase = (unsigned)head * 64u * 2048u;
    const int kh = w >> 2;
    if (SYNC) { if (w == 0) flags_wait(ws, lane < 16 ? FL_1 + (head >> 1) * 16 + lane : -1, tag); LBAR(); }
    STAMP_AT(1);
    bf16x8 qf[2][2];
    {
        const unsigned lds0 = (unsigned)(uintptr_t)lds, lv = lane * 16;
        const unsigned s0 = hbase + (unsigned)(kh * 32 + (w & 3)) * 2048u, d0 = lds0 + kh * 16384 + (w & 3) * 2048;
        K2_STAGE(0, 0);
        const srd_t srq = mk_srd(ws + WS_Q, SEQ * DM * 2);
        const unsigned qo = ((unsigned)head * 64 + qb * 8 + (w & 3) * 2) * 2048u + lane * 16;
#pragma unroll
        for (int qt = 0; qt < 2; ++qt)
#pragma unroll
            for (int s = 0; s < 2; ++s) qf[qt][s] = __builtin_bit_cast(bf16x8, ldc16(srq, qo + qt * 2048 + s * 1024));
        K2_STAGE(1, K2_SLOT);
    }
    WAITBAR(0);
    STAMP_AT(2);
    f32x16 o[2] = {}; f32x4 lacc[2] = {}; f32x4 cs = {0.f, 0.f, 0.f, 0.f};
    if (kh == 0) attn_fast<0>(lds, srk, srv, hbase, S, w, lane, qf, o, lacc, cs);
    else attn_fast<1>(lds, srk, srv, hbase, S, w, lane, qf, o, lacc, cs);
    STAMP_AT(3);
    LAS float* ob = (LAS float*)lds;
    LAS float* lb = (LAS float*)(lds + 32768);
    LAS unsigned* flag = (LAS unsigned*)(lds + 32768 + 4096);
    LAS float* cb = (LAS float*)(lds + 40960);
    const int hi = lane >> 5, q32 = lane & 31;
    const srd_t sro = mk_srd(ws + WS_O, SEQ * DM * 2);
    const unsigned dst = FRAG(qb * 16 + (w & 3) * 4 + kh * 2 + (q32 >> 4), head, 32) + (hi * 16 + (q32 & 15)) * 16;
    bool bad;
    {
        if (tid == 0) flag[0] = 0u;
        if (lane < 16) { lb[w * 64 + lane] = lacc[0][0]; lb[w * 64 + 16 + lane] = lacc[0][1]; lb[w * 64 + 32 + lane] = lacc[1][0]; lb[w * 64 + 48 + lane] = lacc[1][1]; }
#pragma unroll
        for (int j = 0; j < 4; ++j) { const f32x4 v = kh ? (f32x4){o[0][4 * j], o[0][4 * j + 1], o[0][4 * j + 2], o[0][4 * j + 3]} : (f32x4){o[1][4 * j], o[1][4 * j + 1], o[1][4 * j + 2], o[1][4 * j + 3]};
            *(LAS f32x4*)(ob + ((w * 4 + j) * 64 + lane) * 4) = v; }
        if (lane < 32) *(LAS f32x4*)(cb + w * 128 + (lane & 15) * 8 + (lane >> 4) * 4) = cs;
        LBAR();
        const int pw = w ^ 4;
        f32x16 of;
#pragma unroll
        for (int j = 0; j < 4; ++j) { const f32x4 v = *(LAS const f32x4*)(ob + ((pw * 4 + j) * 64 + lane) * 4);
#pragma unroll
            for (int e = 0; e < 4; ++e) of[4 * j + e] = (kh ? o[1][4 * j + e] : o[0][4 * j + e]) + v[e]; }
        const float l = lb[w * 64 + kh * 32 + q32] + lb[pw * 64 + kh * 32 + q32];
        bool ok = (l >= 0x1p-60f) && (l <= 0x1p+80f);
#pragma unroll
        for (int j = 0; j < 16; ++j) ok = ok && (fabsf(of[j]) <= 0x1p+120f);
        if (!__all(ok)) { if (lane == 0) flag[0] = 1u; }
        if (do_cs && tid < 128) {
            float s = 0.f;
#pragma unroll
            for (int i = 0; i < 8; ++i) s += cb[i * 128 + tid];
            __hip_atomic_store((gu32*)(ws + WS_CS) + (rq * 7168 + cgrp * 128 + tid), __float_as_uint(s), RLX_AGENT);
        }
        LBAR();
        bad = flag[0] != 0u;
        if (!bad) {
            const float rl = 1.0f / l;
#pragma unroll
            for (int c = 0; c < 2; ++c) {
                u32x4 ov; ov.x = pk_bf16(of[8 * c + 0] * rl, of[8 * c + 1] * rl); ov.y = pk_bf16(of[8 * c + 2] * rl, of[8 * c + 3] * rl);
                ov.z = pk_bf16(of[8 * c + 4] * rl, of[8 * c + 5] * rl); ov.w = pk_bf16(of[8 * c + 6] * rl, of[8 * c + 7] * rl);
                st16(sro, dst + c * 512, ov);
            }
        }
    }
    if (bad) {
        WAITBAR(0);
        o[0] = (f32x16){}; o[1] = (f32x16){}; float mrun[2] = {-INFINITY, -INFINITY}, lrun[2] = {0.f, 0.f};
        attn_safe(lds, srk, srv, hbase, qf, w, lane, o, mrun, lrun);
        LAS float* mb = lb + 512;
#pragma unroll
        for (int qt = 0; qt < 2; ++qt) { const float lt = lrun[qt] + __shfl_xor(lrun[qt], 32); if (lane < 32) { lb[w * 64 + qt * 32 + lane] = lt; mb[w * 64 + qt * 32 + lane] = mrun[qt]; } }
#pragma unroll
        for (int j = 0; j < 16; ++j) ob[(w * 16 + j) * 64 + lane] = kh ? o[0][j] : o[1][j];
        WAITBAR(0);
        const int pw = w ^ 4;
        const float ma = mb[w * 64 + kh * 32 + q32], mp = mb[pw * 64 + kh * 32 + q32];
        const float mn = fmaxf(ma, mp), ms = (mn == -INFINITY) ? 0.f : mn;
        const float fa = __builtin_amdgcn_exp2f(ma - ms), fp = __builtin_amdgcn_exp2f(mp - ms);
        const float l = lb[w * 64 + kh * 32 + q32] * fa + lb[pw * 64 + kh * 32 + q32] * fp;
        const float rl = 1.0f / l;
        float of[16];
#pragma unroll
        for (int j = 0; j < 16; ++j) of[j] = ((kh ? o[1][j] : o[0][j]) * fa + ob[(pw * 16 + j) * 64 + lane] * fp) * rl;
#pragma unroll
        for (int c = 0; c < 2; ++c) {
            u32x4 ov; ov.x = pk_bf16(of[8 * c + 0], of[8 * c + 1]); ov.y = pk_bf16(of[8 * c + 2], of[8 * c + 3]);
            ov.z = pk_bf16(of[8 * c + 4], of[8 * c + 5]); ov.w = pk_bf16(of[8 * c + 6], of[8 * c + 7]);
            st16(sro, dst + c * 512, ov);
        }
    }
    STAMP_AT(4);
    WAITBAR(0);
    if (SYNC && tid == 0) flag_set(ws, FL_2 + qb * 32 + head, tag);
}
#undef K2_STAGE

constexpr int K3_SLOT = 24576, K3_NS = 6, K3_AUX = K3_NS * K3_SLOT;
template <int MODE, bool SYNC>
__device__ __forceinline__ void phase35(LAS char* lds, const Params& P, unsigned tag STAMP_ARGS) {
    constexpr int KD = MODE == 0 ? DM : FF, KT = KD / 32, NT = KD / 64;
    const unsigned lds0 = (unsigned)(uintptr_t)lds;
    const int tid = threadIdx.x, lane = tid & 63, w = __builtin_amdgcn_readfirstlane(tid >> 6), g = lane >> 4, r = lane & 15;
    const int b = blockIdx.x, xcd = b & 7, loc = b >> 3;
    const int rt = (xcd >> 1) * 4 + (loc & 3), ct = (xcd & 1) * 8 + (loc >> 2);
    char* ws = P.ws;
    if (w >= 4) {
        const int j = w - 4;
        const srd_t srx = mk_srd(ws + (MODE == 0 ? WS_O : WS_ACT), SEQ * KD * 2), srw = mk_srd(MODE == 0 ? P.wo : P.wd, KD * DM * 2);
        const unsigned xv = lane * 16, xs0 = FRAG(rt * 8 + 2 * j, 0, KT);
        constexpr unsigned XM = FRAG(1, 0, KT), XK = FRAG(0, 1, KT);
        const int wrow = lane >> 3, wch = (lane & 7) ^ (((wrow >> 1) & 3) << 1);
        const unsigned wv = (unsigned)wrow * 2048u + wch * 16, ws0 = (unsigned)ct * 128u + (unsigned)(16 * j) * 2048u;
        const unsigned ld0 = lds0 + j * 4096, ld1 = lds0 + 16384 + j * 2048;
#define K3_W(t, slot) do { dma16(ld1 + (slot), wv, srw, ws0 + (t) * (64 * 2048)); dma16(ld1 + (slot) + 1024, wv, srw, ws0 + 8 * 2048 + (t) * (64 * 2048)); } while (0)
#define K3_X(t, slot) do { dma16c(ld0 + (slot), xv, srx, xs0 + (t) * (2 * XK)); dma16c(ld0 + (slot) + 1024, xv, srx, xs0 + (t) * (2 * XK) + XK); \
        dma16c(ld0 + (slot) + 2048, xv, srx, xs0 + XM + (t) * (2 * XK)); dma16c(ld0 + (slot) + 3072, xv, srx, xs0 + XM + (t) * (2 * XK) + XK); } while (0)
        K3_W(0, 0); K3_W(1, K3_SLOT); K3_W(2, 2 * K3_SLOT); K3_W(3, 3 * K3_SLOT); K3_W(4, 4 * K3_SLOT);
        if (SYNC) LBAR();
        K3_X(0, 0); K3_X(1, K3_SLOT); K3_X(2, 2 * K3_SLOT);
        WAITBAR(8);  K3_X(3, 3 * K3_SLOT); K3_X(4, 4 * K3_SLOT); K3_X(5, 5 * K3_SLOT); K3_W(5, 5 * K3_SLOT);
        WAITBAR(18); K3_X(6, 0); K3_W(6, 0);
        WAITBAR(20); K3_X(7, K3_SLOT); K3_W(7, K3_SLOT);
        WAITBAR(22); K3_X(8, 2 * K3_SLOT); K3_W(8, 2 * K3_SLOT);
        int fr = 3 * K3_SLOT;
#pragma unroll 1
        for (int t = 4; t + K3_NS - 1 < NT; ++t) {
            WAITBAR(6 * (K3_NS - 2));
            K3_X(t + K3_NS - 1, fr); K3_W(t + K3_NS - 1, fr);
            fr = (fr == (K3_NS - 1) * K3_SLOT) ? 0 : fr + K3_SLOT;
        }
        WAITBAR(24); WAITBAR(18); WAITBAR(12); WAITBAR(6); WAITBAR(0);
        if (MODE == 0) LBAR();
    } else {
        const int wm = w >> 1, wn = w & 1;
        const int col0 = ct * 64 + wn * 32 + 4 * g;
        if (SYNC) { if (w == 0) flags_wait(ws, lane < 32 ? (MODE == 0 ? FL_2 : FL_4) + (rt >> 1) * 32 + lane : -1, tag); LBAR(); }
        STAMP_AT(1);
        u32x4 res[4];
#pragma unroll
        for (int m = 0; m < 4; ++m) {
            const int mrow = rt * 128 + wm * 64 + m * 16;
            if (MODE == 0) { const u32x2 a = *(const u32x2*)(P.x + (size_t)(mrow + r) * DM + col0), c = *(const u32x2*)(P.x + (size_t)(mrow + r) * DM + col0 + 16); res[m] = (u32x4){a.x, a.y, c.x, c.y}; }
            else res[m] = ldc16(mk_srd(ws + WS_H, SEQ * DM * 2), FRAG(mrow >> 4, ct * 2 + wn, 32) + lane * 16);
        }
        const int xrd = wm * 8192 + lane * 16;
        const int q4 = (lane >> 2) & 3, p4 = lane & 3;
        const int trow = (MODE == 0) ? (4 * (g & 1) + 16 * (g >> 1) + q4) : (4 * g + q4);
        constexpr int TR2 = (MODE == 0) ? 8 * 128 : 16 * 128;
        int wrd[2];
#pragma unroll
        for (int n = 0; n < 2; ++n) wrd[n] = 16384 + trow * 128 + (((wn * 2 + n) ^ ((trow >> 1) & 3)) * 32) + (p4 >> 1) * 16 + (p4 & 1) * 8;
        f32x4 acc[4][2];
#pragma unroll
        for (int m = 0; m < 4; ++m) { acc[m][0] = (f32x4){0.f, 0.f, 0.f, 0.f}; acc[m][1] = (f32x4){0.f, 0.f, 0.f, 0.f}; }
        bf16x8 xa[4], xb[4]; s16x4 wa[4], wb[4];
#define K3_RD(i, X, W, sl, s) do { if ((i) == 2) X[0] = ldf(lds + (sl) + xrd + (s) * 1024); else if ((i) >= 5) X[(i) - 4] = ldf(lds + (sl) + xrd + ((i) - 4) * 2048 + (s) * 1024); \
        else { const int k_ = (i) < 2 ? (i) : (i) - 1; W[k_] = trd(lds + (sl) + wrd[k_ >> 1] + (k_ & 1) * TR2 + (s) * 4096); } } while (0)
#define K3_MM(i, X, W) do { const int m_ = (i) >> 1, n_ = (i) & 1; acc[m_][n_] = MFMA16(cat8(W[2 * n_], W[2 * n_ + 1]), X[m_], acc[m_][n_]); } while (0)
#define K3_HALF(XM, WM, XR, WR, sl, s, DOMMA) do { _Pragma("unroll") for (int i_ = 0; i_ < 8; ++i_) { if (DOMMA) K3_MM(i_, XM, WM); K3_RD(i_, XR, WR, sl, s); SCHED0(); } } while (0)
        LBAR();
        STAMP_AT(2);
        K3_HALF(xb, wb, xa, wa, 0, 0, false);
        K3_HALF(xa, wa, xb, wb, 0, 1, true);
        int cur = K3_SLOT;
#pragma unroll 1
        for (int t = 1; t < NT; ++t) {
            LBAR();
            K3_HALF(xb, wb, xa, wa, cur, 0, true);
            K3_HALF(xa, wa, xb, wb, cur, 1, true);
            cur = (cur == (K3_NS - 1) * K3_SLOT) ? 0 : cur + K3_SLOT;
        }
#pragma unroll
        for (int i = 0; i < 8; ++i) K3_MM(i, xb, wb);
        STAMP_AT(3);
        if (MODE == 0) {
            LAS f32x2* sb = (LAS f32x2*)(lds + K3_AUX);
            const srd_t srh = mk_srd(ws + WS_H, SEQ * DM * 2);
#pragma unroll
            for (int m = 0; m < 4; ++m) {
                const int mrow = rt * 128 + wm * 64 + m * 16;
                const f32x4 a = {bf_lo(res[m].x) + acc[m][0][0], bf_hi(res[m].x) + acc[m][0][1], bf_lo(res[m].y) + acc[m][0][2], bf_hi(res[m].y) + acc[m][0][3]};
                const f32x4 c = {bf_lo(res[m].z) + acc[m][1][0], bf_hi(res[m].z) + acc[m][1][1], bf_lo(res[m].w) + acc[m][1][2], bf_hi(res[m].w) + acc[m][1][3]};
                const u32x4 hv = pk8(a, c);
                st16(srh, FRAG(mrow >> 4, ct * 2 + wn, 32) + lane * 16, hv);
                float s = 0.f, q = 0.f;
#pragma unroll
                for (int j = 0; j < 4; ++j) { const unsigned d = hv[j]; const float u = bf_lo(d), v = bf_hi(d); s += u + v; q += u * u + v * v; }
                s += __shfl_xor(s, 16); s += __shfl_xor(s, 32); q += __shfl_xor(q, 16); q += __shfl_xor(q, 32);
                if (g == 0) sb[(wm * 64 + m * 16 + r) * 2 + wn] = (f32x2){s, q};
            }
            LBAR();
            if (tid < 128) { const f32x2 a = sb[tid * 2] + sb[tid * 2 + 1];
                __hip_atomic_store((gu64*)(ws + WS_ST) + ((size_t)(rt * 128 + tid) * 16 + ct), ((unsigned long long)__float_as_uint(a[1]) << 32) | __float_as_uint(a[0]), RLX_AGENT); }
        } else {
#pragma unroll
            for (int m = 0; m < 4; ++m) {
                const int mrow = rt * 128 + wm * 64 + m * 16;
                unsigned a0 = pk_bf16(bf_lo(res[m].x) + acc[m][0][0], bf_hi(res[m].x) + acc[m][0][1]), a1 = pk_bf16(bf_lo(res[m].y) + acc[m][0][2], bf_hi(res[m].y) + acc[m][0][3]);
                unsigned b0 = pk_bf16(bf_lo(res[m].z) + acc[m][1][0], bf_hi(res[m].z) + acc[m][1][1]), b1 = pk_bf16(bf_lo(res[m].w) + acc[m][1][2], bf_hi(res[m].w) + acc[m][1][3]);
                const auto s0 = __builtin_amdgcn_permlane16_swap(b0, a0, false, false), s1 = __builtin_amdgcn_permlane16_swap(b1, a1, false, false);
                const u32x4 ov = {s0[0], s1[0], s0[1], s1[1]};
                const int c8 = ((g & 1) ? 0 : 16) + (g >> 1) * 8;
                *(u32x4*)(P.out + (size_t)(mrow + r) * DM + ct * 64 + wn * 32 + c8) = ov;
            }
        }
    }
    STAMP_AT(4);
    if (MODE == 0) {
        WAITBAR(0);
        if (SYNC && tid == 0) flag_set(ws, FL_3 + rt * 16 + ct, tag);
    }
}

constexpr int K5_SLOT = 32768, K5_NS = 4;
template <int KH>
__device__ __forceinline__ void p5_finish(const Params& P, const f32x4 (&acc)[4][4], const u32x4 (&res)[4], unsigned tag, int b, int w, int lane, int rt, int ct STAMP_ARGS) {
    char* ws = P.ws;
    const int g = lane >> 4, r = lane & 15, wm = w >> 1, wn = w & 1;
    const srd_t sre = mk_srd(ws + WS_XCH, 8u << 20);
    const unsigned eo = (unsigned)(w * 8192) + lane * 16;
#pragma unroll
    for (int m = 0; m < 4; ++m)
#pragma unroll
        for (int n = 0; n < 2; ++n) st16(sre, (unsigned)b * 32768u + eo + (m * 2 + n) * 1024, __builtin_bit_cast(u32x4, acc[m][2 * (1 - KH) + n]));
    WAITV(0);
    if (lane == 0) flag_set(ws, FL_5 + b * 4 + w, tag);
    flags_wait(ws, lane == 0 ? FL_5 + (b ^ 1) * 4 + w : -1, tag);
    STAMP_AT(4);
    u32x4 pv[4][2];
#pragma unroll
    for (int m = 0; m < 4; ++m)
#pragma unroll
        for (int n = 0; n < 2; ++n) pv[m][n] = ldc16(sre, (unsigned)(b ^ 1) * 32768u + eo + (m * 2 + n) * 1024);
#pragma unroll
    for (int m = 0; m < 4; ++m) {
        const int mrow = rt * 128 + wm * 64 + m * 16;
        const f32x4 t0 = acc[m][2 * KH] + __builtin_bit_cast(f32x4, pv[m][0]), t1 = acc[m][2 * KH + 1] + __builtin_bit_cast(f32x4, pv[m][1]);
        unsigned a0 = pk_bf16(bf_lo(res[m].x) + t0[0], bf_hi(res[m].x) + t0[1]), a1 = pk_bf16(bf_lo(res[m].y) + t0[2], bf_hi(res[m].y) + t0[3]);
        unsigned b0 = pk_bf16(bf_lo(res[m].z) + t1[0], bf_hi(res[m].z) + t1[1]), b1 = pk_bf16(bf_lo(res[m].w) + t1[2], bf_hi(res[m].w) + t1[3]);
        const auto s0 = __builtin_amdgcn_permlane16_swap(b0, a0, false, false), s1 = __builtin_amdgcn_permlane16_swap(b1, a1, false, false);
        const u32x4 ov = {s0[0], s1[0], s0[1], s1[1]};
        const int c8 = ((g & 1) ? 0 : 16) + (g >> 1) * 8;
        *(u32x4*)(P.out + (size_t)(mrow + r) * DM + ct * 128 + wn * 64 + KH * 32 + c8) = ov;
    }
}
template <bool SYNC>
__device__ __forceinline__ void phase5(LAS char* lds, const Params& P, unsigned tag STAMP_ARGS) {
    constexpr int KT = FF / 32, NT = FF / 128;
    const unsigned lds0 = (unsigned)(uintptr_t)lds;
    const int tid = threadIdx.x, lane = tid & 63, w = __builtin_amdgcn_readfirstlane(tid >> 6), g = lane >> 4, r = lane & 15;
    const int b = blockIdx.x, xcd = b & 7, loc = b >> 3;
    const int kh = xcd & 1, rt = (xcd >> 1) * 4 + (loc & 3), ct = loc >> 2;
    char* ws = P.ws;
    if (w >= 4) {
        const int j = w - 4;
        const srd_t srx = mk_srd(ws + WS_ACT, SEQ * FF * 2), srw = mk_srd(P.wd, FF * DM * 2);
        constexpr unsigned XM = FRAG(1, 0, KT), XK = FRAG(0, 1, KT);
        const unsigned xv = lane * 16, xs0 = FRAG(rt * 8 + 2 * j, kh * (KT / 2), KT);
        const int wrow = lane >> 3, wch = (lane & 7) ^ (((wrow >> 1) & 3) << 1);
        const unsigned wv = (unsigned)wrow * 2048u + wch * 16, ws0 = (unsigned)ct * 256u + (unsigned)(kh * (FF / 2) + 16 * j) * 2048u;
        const unsigned ld0 = lds0 + j * 4096, ld1 = lds0 + 16384 + j * 2048;
#define K5_W(t, slot) do { dma16(ld1 + (slot), wv, srw, ws0 + (t) * (64 * 2048)); dma16(ld1 + (slot) + 1024, wv, srw, ws0 + 8 * 2048 + (t) * (64 * 2048)); \
        dma16(ld1 + (slot) + 8192, wv, srw, ws0 + 128 + (t) * (64 * 2048)); dma16(ld1 + (slot) + 9216, wv, srw, ws0 + 128 + 8 * 2048 + (t) * (64 * 2048)); } while (0)
#define K5_X(t, slot) do { dma16c(ld0 + (slot), xv, srx, xs0 + (t) * (2 * XK)); dma16c(ld0 + (slot) + 1024, xv, srx, xs0 + (t) * (2 * XK) + XK); \
        dma16c(ld0 + (slot) + 2048, xv, srx, xs0 + XM + (t) * (2 * XK)); dma16c(ld0 + (slot) + 3072, xv, srx, xs0 + XM + (t) * (2 * XK) + XK); } while (0)
        K5_W(0, 0); K5_W(1, K5_SLOT); K5_W(2, 2 * K5_SLOT); K5_W(3, 3 * K5_SLOT);
        if (SYNC) LBAR();
        K5_X(0, 0); K5_X(1, K5_SLOT); K5_X(2, 2 * K5_SLOT); K5_X(3, 3 * K5_SLOT);
        WAITBAR(12);
        WAITBAR(8);  K5_X(4, 0); K5_W(4, 0);
        WAITBAR(12); K5_X(5, K5_SLOT); K5_W(5, K5_SLOT);
        WAITBAR(16); K5_X(6, 2 * K5_SLOT); K5_W(6, 2 * K5_SLOT);
        int fr = 3 * K5_SLOT;
#pragma unroll 1
        for (int t = 4; t + 3 < NT; ++t) {
            WAITBAR(16);
            K5_X(t + 3, fr); K5_W(t + 3, fr);
            fr = (fr == 3 * K5_SLOT) ? 0 : fr + K5_SLOT;
        }
        WAITBAR(16); WAITBAR(8); WAITBAR(0);
    } else {
        const int wm = w >> 1, wn = w & 1;
        if (SYNC) { if (w == 0) flags_wait(ws, lane < 16 ? FL_4 + (rt >> 1) * 32 + kh * 16 + lane : -1, tag); LBAR(); }
        STAMP_AT(1);
        u32x4 res[4];
#pragma unroll
        for (int m = 0; m < 4; ++m) res[m] = ldc16(mk_srd(ws + WS_H, SEQ * DM * 2), FRAG(rt * 8 + wm * 4 + m, ct * 4 + wn * 2 + kh, 32) + lane * 16);
        const int xrd = wm * 8192 + lane * 16;
        const int q4 = (lane >> 2) & 3, p4 = lane & 3, trow = 4 * g + q4;
        int wrd[4];
#pragma unroll
        for (int n = 0; n < 4; ++n) wrd[n] = 16384 + wn * 8192 + trow * 128 + ((n ^ ((trow >> 1) & 3)) * 32) + (p4 >> 1) * 16 + (p4 & 1) * 8;
        f32x4 acc[4][4];
#pragma unroll
        for (int m = 0; m < 4; ++m)
#pragma unroll
            for (int n = 0; n < 4; ++n) acc[m][n] = (f32x4){0.f, 0.f, 0.f, 0.f};
        bf16x8 xa[4], xb[4]; s16x4 wa[8], wb[8];
#define K5_RD(i, X, W, sl, s) do { if ((i) == 2) X[0] = ldf(lds + (sl) + xrd + (s) * 1024); else if ((i) >= 9) X[(i) - 8] = ldf(lds + (sl) + xrd + ((i) - 8) * 2048 + (s) * 1024); \
        else { const int k_ = (i) < 2 ? (i) : (i) - 1; W[k_] = trd(lds + (sl) + wrd[k_ >> 1] + (k_ & 1) * 2048 + (s) * 4096); } } while (0)
#define K5_MM(i, X, W) do { const int m_ = (i) >> 2, n_ = (i) & 3; acc[m_][n_] = MFMA16(cat8(W[2 * n_], W[2 * n_ + 1]), X[m_], acc[m_][n_]); } while (0)
#define K5_HALF(XM_, WM_, XR, WR, sl, s, DOMMA) do { _Pragma("unroll") for (int i_ = 0; i_ < 16; ++i_) { if (DOMMA) K5_MM(i_, XM_, WM_); if (i_ < 12) K5_RD(i_, XR, WR, sl, s); SCHED0(); } } while (0)
        LBAR();
        STAMP_AT(2);
        K5_HALF(xb, wb, xa, wa, 0, 0, false);
        K5_HALF(xa, wa, xb, wb, 0, 1, true);
        int cur = K5_SLOT;
#pragma unroll 1
        for (int t = 1; t < NT; ++t) {
            LBAR();
            K5_HALF(xb, wb, xa, wa, cur, 0, true);
            K5_HALF(xa, wa, xb, wb, cur, 1, true);
            cur = (cur == 3 * K5_SLOT) ? 0 : cur + K5_SLOT;
        }
#pragma unroll
        for (int i = 0; i < 16; ++i) K5_MM(i, xb, wb);
        STAMP_AT(3);
        if (kh) p5_finish<1>(P, acc, res, tag, b, w, lane, rt, ct STAMP_PASS); else p5_finish<0>(P, acc, res, tag, b, w, lane, rt, ct STAMP_PASS);
    }
}

constexpr int K4_SLOT = 30720, K4_NS = 4, K4_STRAW = K4_NS * K4_SLOT, K4_STAT = K4_STRAW + 32768, K4_CS = K4_STAT + 2048, K4_CSRAW = K4_CS + 1024;
struct K4Dma { srd_t srx, srg, sru; unsigned xv, gv, xs0, gs0, ld0, ld1; };
#define K4_DMA(i, t, slot) do { if ((i) < 2) dma16c(D.ld0 + (slot) + (i) * 1024, D.xv, D.srx, D.xs0 + (t) * FRAG(0, 1, 32) + (i) * FRAG(1, 0, 32)); \
        else dma16(D.ld1 + (slot) + ((i) - 2) * 7168, D.gv, (i) == 2 ? D.srg : D.sru, D.gs0 + (t) * (32 * FF * 2)); } while (0)
template <int NJ, int J0, bool HASW>
__device__ __forceinline__ void k4_body(LAS char* lds, const K4Dma& D, char* ws, int w, int lane, int rt, int ct STAMP_ARGS) {
    const int wm = w & 3, g = lane >> 4, r = lane & 15;
    constexpr int NP = HASW ? 4 : 2;
    const int xrd = wm * 4096 + lane * 16;
    const int q4 = (lane >> 2) & 3, p4 = lane & 3;
    const int wrd = 16384 + (4 * g + q4) * 224 + J0 * 32 + (p4 >> 1) * 16 + (p4 & 1) * 8;
    f32x4 ag[4][NJ], au[4][NJ];
#pragma unroll
    for (int m = 0; m < 4; ++m)
#pragma unroll
        for (int j = 0; j < NJ; ++j) { ag[m][j] = (f32x4){0.f, 0.f, 0.f, 0.f}; au[m][j] = (f32x4){0.f, 0.f, 0.f, 0.f}; }
    bf16x8 xa[4], xb[4]; s16x4 wgf[2 * NJ], wuf[2 * NJ];
#define K4_RW(i, W, sl, o) W[i] = trd(lds + (sl) + wrd + (o) + ((i) >> 1) * 32 + ((i) & 1) * (16 * 224))
#define K4_RX(i, X, sl) X[i] = ldf(lds + (sl) + xrd + (i) * 1024)
#define K4_MM(i, A, X, W) do { const int j_ = (i) >> 2, m_ = (i) & 3; A[m_][j_] = MFMA16(cat8(W[2 * j_], W[2 * j_ + 1]), X[m_], A[m_][j_]); } while (0)
#define K4_RD1(i, XC, sl) do { if ((i) == 0) K4_RX(0, XC, sl); else if ((i) < 3) K4_RW((i) - 1, wgf, sl, 0); else if ((i) < 6) K4_RX((i) - 2, XC, sl); else if ((i) < 4 + 2 * NJ) K4_RW((i) - 4, wgf, sl, 0); } while (0)
#define K4_H1(XC, XP, DOMMA, DODMA, t, sl, fsl) do { \
        _Pragma("unroll") for (int i_ = 0; i_ < 4 * NJ; ++i_) { \
            if (DOMMA) K4_MM(i_, au, XP, wuf); \
            K4_RD1(i_, XC, sl); \
            if (DODMA) { if (i_ == (NJ == 4 ? 13 : 11)) K4_DMA(0, t, fsl); } \
            SCHED0(); } } while (0)
#define K4_H2(XC, sl, DODMA, t, fsl) do { \
        _Pragma("unroll") for (int i_ = 0; i_ < 4 * NJ; ++i_) { \
            K4_MM(i_, ag, XC, wgf); \
            if (i_ < 2 * NJ) K4_RW(i_, wuf, sl, 7168); \
            if (DODMA) { if (NJ == 4) { if (i_ == 8) K4_DMA(1, t, fsl); if (i_ == 11 && NP == 4) K4_DMA(2, t, fsl); if (i_ == 14 && NP == 4) K4_DMA(3, t, fsl); } \
                         else { if (i_ == 6) K4_DMA(1, t, fsl); if (i_ == 8 && NP == 4) K4_DMA(2, t, fsl); if (i_ == 10 && NP == 4) K4_DMA(3, t, fsl); } } \
            SCHED0(); } } while (0)
    constexpr int NT = DM / 32;
    constexpr int NX = HASW ? 4 : 8;
    WAITBAR(4 + NX);
    STAMP_AT(2);
    K4_H1(xa, xb, false, true, 3, 0, 3 * K4_SLOT);
    K4_H2(xa, 0, true, 3, 3 * K4_SLOT);
    int cur = K4_SLOT, fr = 0;
#define K4_ADV() do { fr = (fr == (K4_NS - 1) * K4_SLOT) ? 0 : fr + K4_SLOT; cur = (cur == (K4_NS - 1) * K4_SLOT) ? 0 : cur + K4_SLOT; } while (0)
    WAITBAR(2 + NX + NP); K4_H1(xb, xa, true, true, 4, cur, fr); K4_H2(xb, cur, true, 4, fr); K4_ADV();
    WAITBAR(NX + 2 * NP); K4_H1(xa, xb, true, true, 5, cur, fr); K4_H2(xa, cur, true, 5, fr); K4_ADV();
    WAITBAR(2 * NP);
    {
        const int tid = w * 64 + lane, row = tid >> 1, hf = tid & 1;
        float s = 0.f, q = 0.f;
#pragma unroll
        for (int i = 0; i < 4; ++i) { const f32x4 v = *(const LAS f32x4*)(lds + K4_STRAW + row * 128 + hf * 64 + i * 16); s += v[0] + v[2]; q += v[1] + v[3]; }
        s += __shfl_xor(s, 1); q += __shfl_xor(q, 1);
        const float mean = s * (1.0f / DM);
        const float var = fmaxf(q * (1.0f / DM) - mean * mean, 0.f);
        if (hf == 0) ((LAS f32x2*)(lds + K4_STAT))[row] = (f32x2){mean, 1.0f / sqrtf(var + LN_EPS)};
        if (tid < 224) { const LAS float* cr = (const LAS float*)(lds + K4_CSRAW) + tid; ((LAS float*)(lds + K4_CS))[tid] = (cr[0] + cr[256]) + (cr[512] + cr[768]); }
        SCHED0();
    }
    K4_H1(xb, xa, true, true, 6, cur, fr); K4_H2(xb, cur, true, 6, fr); K4_ADV();
    int t = 4;
#pragma unroll 1
    for (; t + 4 < NT; t += 2) {
        WAITBAR(2 * NP); K4_H1(xa, xb, true, true, t + 3, cur, fr); K4_H2(xa, cur, true, t + 3, fr); K4_ADV();
        WAITBAR(2 * NP); K4_H1(xb, xa, true, true, t + 4, cur, fr); K4_H2(xb, cur, true, t + 4, fr); K4_ADV();
    }
    WAITBAR(2 * NP); K4_H1(xa, xb, true, true, t + 3, cur, fr); K4_H2(xa, cur, true, t + 3, fr); K4_ADV();
    WAITBAR(2 * NP); K4_H1(xb, xa, true, false, 0, cur, fr); K4_H2(xb, cur, false, 0, fr); K4_ADV();
    WAITBAR(NP);     K4_H1(xa, xb, true, false, 0, cur, fr); K4_H2(xa, cur, false, 0, fr); K4_ADV();
    WAITBAR(0);      K4_H1(xb, xa, true, false, 0, cur, fr); K4_H2(xb, cur, false, 0, fr);
#pragma unroll
    for (int i = 0; i < 4 * NJ; ++i) K4_MM(i, au, xb, wuf);
    STAMP_AT(3);
    const LAS f32x2* st = (const LAS f32x2*)(lds + K4_STAT);
    const LAS float* cs = (const LAS float*)(lds + K4_CS);
    f32x2 ms[4];
#pragma unroll
    for (int m = 0; m < 4; ++m) ms[m] = st[wm * 64 + m * 16 + r];
    const srd_t sra = mk_srd(ws + WS_ACT, SEQ * FF * 2);
    const unsigned ap0 = FRAG(rt * 16 + wm * 4, 0, 112) + lane * 16;
    const int ft0 = ct * 7 + J0;
    constexpr int LONE = (NJ == 3) ? (J0 == 4 ? 2 : 0) : -1;
    u32x2 keep[4];
#pragma unroll
    for (int j = 0; j < NJ; ++j) {
        const f32x4 cg = *(const LAS f32x4*)(cs + (J0 + j) * 16 + 4 * g), cu = *(const LAS f32x4*)(cs + 112 + (J0 + j) * 16 + 4 * g);
        const bool second = (LONE == 0) ? (j == 2) : ((j & 1) == 1);
#pragma unroll
        for (int m = 0; m < 4; ++m) {
            const float c1 = -LOG2E * ms[m].y;
            const f32x4 gz = (ag[m][j] - cg * ms[m].x) * c1;
            const f32x4 uu = (au[m][j] - cu * ms[m].x) * ms[m].y;
            float a[4];
#pragma unroll
            for (int e = 0; e < 4; ++e) a[e] = uu[e] * __builtin_amdgcn_rcpf(1.0f + __builtin_amdgcn_exp2f(gz[e]));
            u32x2 ov; ov.x = pk_bf16(a[0], a[1]); ov.y = pk_bf16(a[2], a[3]);
            const int ft = ft0 + j;
            if (j == LONE) st8(sra, ap0 + FRAG(m, ft >> 1, 112) + (ft & 1) * 8, ov);
            else if (!second) keep[m] = ov;
            else st16(sra, ap0 + FRAG(m, ft >> 1, 112), (u32x4){keep[m].x, keep[m].y, ov.x, ov.y});
        }
        SCHED0();
    }
}

template <bool SYNC>
__device__ __forceinline__ void phase4(LAS char* lds, const Params& P, unsigned tag STAMP_ARGS) {
    const unsigned lds0 = (unsigned)(uintptr_t)lds;
    const int tid = threadIdx.x, lane = tid & 63, w = __builtin_amdgcn_readfirstlane(tid >> 6);
    const int b = blockIdx.x, xcd = b & 7, loc = b >> 3;
    const int rt = (xcd >> 2) * 4 + (loc & 3), ct = (xcd & 3) * 8 + (loc >> 2);
    char* ws = P.ws;
    K4Dma D;
    D.srx = mk_srd(ws + WS_H, SEQ * DM * 2); D.srg = mk_srd(P.wg, DM * FF * 2); D.sru = mk_srd(P.wu, DM * FF * 2);
    D.xv = lane * 16; D.xs0 = FRAG(rt * 16 + w * 2, 0, 32);
    { const int ci = (w < 7 ? w : 6) * 64 + lane; const int row = ci / 14, c = ci % 14; D.gv = (unsigned)row * (FF * 2) + c * 16; }
    D.gs0 = (unsigned)ct * 224u; D.ld0 = lds0 + w * 2048; D.ld1 = lds0 + 16384 + w * 1024;
    if (w < 7) {
#pragma unroll
        for (int t = 0; t < 3; ++t) { K4_DMA(2, t, t * K4_SLOT); K4_DMA(3, t, t * K4_SLOT); }
    }
    if (SYNC) {
        if (w == 7) {
            int idx = -1;
            if (lane < 32) idx = FL_3 + rt * 32 + lane;
            else if (lane < 48) { const int k = lane - 32, gsel = k & 3, rq = k >> 2;
                const int c0 = (ct * 112 + ((gsel & 1) ? 111 : 0)) >> 7, bb = rq * 56 + (gsel >> 1) * 28 + c0;
                idx = FL_2 + (bb >> 5) * 32 + (bb & 7) * 4 + ((bb >> 3) & 3); }
            flags_wait(ws, idx, tag);
        }
        LBAR();
    }
    STAMP_AT(1);
#pragma unroll
    for (int t = 0; t < 3; ++t) { K4_DMA(0, t, t * K4_SLOT); K4_DMA(1, t, t * K4_SLOT); }
    {
        const srd_t srs = mk_srd(ws + WS_ST, SEQ * 128);
#pragma unroll
        for (int i = 0; i < 4; ++i) dma16c(lds0 + K4_STRAW + w * 4096 + i * 1024, lane * 16, srs, (unsigned)(rt * 256 + w * 32) * 128u + i * 1024);
        if (w == 7) {
            const srd_t src = mk_srd(ws + WS_CS, 4 * 7168 * 4);
            const unsigned cv = lane < 28 ? (unsigned)(ct * 112 + 4 * lane) * 4u : lane < 56 ? (unsigned)(3584 + ct * 112 + 4 * (lane - 28)) * 4u : 0x7ff00000u;
#pragma unroll
            for (int i = 0; i < 4; ++i) dma16c(lds0 + K4_CSRAW + i * 1024, cv, src, i * 7168 * 4);
        }
    }
    if ((ct & 1) == 0) {
        if (w < 4) k4_body<4, 0, true>(lds, D, ws, w, lane, rt, ct STAMP_PASS);
        else if (w < 7) k4_body<3, 4, true>(lds, D, ws, w, lane, rt, ct STAMP_PASS);
        else k4_body<3, 4, false>(lds, D, ws, w, lane, rt, ct STAMP_PASS);
    } else {
        if (w < 4) k4_body<3, 0, true>(lds, D, ws, w, lane, rt, ct STAMP_PASS);
        else if (w < 7) k4_body<4, 3, true>(lds, D, ws, w, lane, rt, ct STAMP_PASS);
        else k4_body<4, 3, false>(lds, D, ws, w, lane, rt, ct STAMP_PASS);
    }
    STAMP_AT(4);
    WAITBAR(0);
    if (SYNC && tid == 0) flag_set(ws, FL_4 + rt * 32 + ct, tag);
}

constexpr int LDS_BYTES = 160 * 1024;
__global__ __launch_bounds__(512, 2) void mega(Params P) {
    extern __shared__ __attribute__((aligned(16))) char smem_g[];
    LAS char* lds = (LAS char*)smem_g;
    char* ws = P.ws;
    const unsigned tag = __hip_atomic_load((gu32*)(ws + WS_FLG) + FL_EPOCH, RLX_AGENT) + 1u;
    { STAMP_DECL STAMP_AT(0); phase1<true>(lds, P, tag STAMP_PASS); STAMP_OUT(1); }
    { STAMP_DECL STAMP_AT(0); phase2<true>(lds, P, tag STAMP_PASS); STAMP_OUT(2); }
    { STAMP_DECL STAMP_AT(0); phase35<0, true>(lds, P, tag STAMP_PASS); STAMP_OUT(3); }
    { STAMP_DECL STAMP_AT(0); phase4<true>(lds, P, tag STAMP_PASS); STAMP_OUT(4); }
    { STAMP_DECL STAMP_AT(0); phase5<true>(lds, P, tag STAMP_PASS); STAMP_OUT(5); }
    if (blockIdx.x == 0 && threadIdx.x == 0) __hip_atomic_store((gu32*)(ws + WS_FLG) + FL_EPOCH, tag, RLX_AGENT);
}

extern "C" void kernel_launch(void* const* d_in, const int* in_sizes, int n_in, void* d_out, int out_size, void* d_ws, size_t ws_size, hipStream_t stream) {
    Params P;
    P.x = (const u16*)d_in[0]; P.wqkv = (const u16*)d_in[1]; P.wo = (const u16*)d_in[2];
    P.wg = (const u16*)d_in[4]; P.wu = (const u16*)d_in[5]; P.wd = (const u16*)d_in[6];
    P.out = (u16*)d_out; P.ws = (char*)d_ws;
    (void)hipFuncSetAttribute((const void*)mega, hipFuncAttributeMaxDynamicSharedMemorySize, LDS_BYTES);
    mega<<<256, 512, LDS_BYTES, stream>>>(P);
}
```

```cpp
#include <hip/hip_runtime.h>
#include <hip/hip_bf16.h>
#include <stdint.h>

#define LAS __attribute__((address_space(3)))
typedef unsigned short u16;
typedef short bf16x8 __attribute__((ext_vector_type(8)));
typedef short s16x4 __attribute__((ext_vector_type(4)));
typedef float f32x2 __attribute__((ext_vector_type(2)));
typedef float f32x4 __attribute__((ext_vector_type(4)));
typedef float f32x16 __attribute__((ext_vector_type(16)));
typedef unsigned u32x2 __attribute__((ext_vector_type(2)));
typedef unsigned u32x4 __attribute__((ext_vector_type(4)));
typedef __bf16 bf16x2_t __attribute__((ext_vector_type(2)));
typedef __amdgpu_buffer_rsrc_t srd_t;

constexpr int SEQ = 2048, DM = 1024, NH = 32, FF = 3584;
constexpr float LN_EPS = 1e-5f;
constexpr float QSCALE = 0.17677669529663687f * 1.4426950408889634f;
constexpr float LOG2E = 1.4426950408889634f;

constexpr size_t WS_Q = 0;
constexpr size_t WS_K = WS_Q + (4u << 20);
constexpr size_t WS_V = WS_K + (4u << 20);
constexpr size_t WS_O = WS_V + (4u << 20);
constexpr size_t WS_H = WS_O + (4u << 20);
constexpr size_t WS_ACT = WS_H + (4u << 20);
constexpr size_t WS_ST = WS_ACT + (size_t)SEQ * FF * 2;
constexpr size_t WS_CS = WS_ST + (size_t)SEQ * 16 * 8;
constexpr size_t WS_S1 = WS_CS + 4 * 7168 * 4;
constexpr size_t WS_C1 = WS_S1 + SEQ * 8;
constexpr size_t WS_FLG = WS_C1 + 16 * 3072 * 4;
constexpr size_t WS_XCH = WS_FLG + 16384;
constexpr size_t WS_END = WS_XCH + (8u << 20);
constexpr int FL_EPOCH = 0, FL_ERR = 32, FL_0 = 64, FL_1 = FL_0 + 256, FL_2 = FL_1 + 256, FL_3 = FL_2 + 256, FL_4 = FL_3 + 256, FL_5 = FL_4 + 256;

#define FRAG(mt, kt, KT) (((unsigned)(kt) * 128u + (unsigned)(mt)) * 1024u)
#define ST_AUX 16

__device__ __forceinline__ unsigned pk_bf16(float lo, float hi) { f32x2 v = {lo, hi}; bf16x2_t b = __builtin_convertvector(v, bf16x2_t); return __builtin_bit_cast(unsigned, b); }
__device__ __forceinline__ float bf_lo(unsigned u) { return __uint_as_float(u << 16); }
__device__ __forceinline__ float bf_hi(unsigned u) { return __uint_as_float(u & 0xffff0000u); }
__device__ __forceinline__ s16x4 trd(LAS const char* p) { return __builtin_bit_cast(s16x4, __builtin_amdgcn_ds_read_tr16_b64_v4i16((LAS s16x4*)p)); }
__device__ __forceinline__ bf16x8 cat8(s16x4 a, s16x4 b) { return (bf16x8){a[0], a[1], a[2], a[3], b[0], b[1], b[2], b[3]}; }
__device__ __forceinline__ bf16x8 ldf(LAS const char* p) { return *(LAS const bf16x8*)p; }
__device__ __forceinline__ srd_t mk_srd(const void* p, unsigned bytes) { return __builtin_amdgcn_make_buffer_rsrc((void*)p, (short)0, (int)bytes, 0x00020000); }
__device__ __forceinline__ void dma16(unsigned ldsa, unsigned voff, srd_t srd, unsigned soff) {
    asm volatile("s_mov_b32 m0, %0\n\ts_nop 0\n\tbuffer_load_dwordx4 %1, %2, %3 offen lds" :: "s"(ldsa), "v"(voff), "s"(srd), "s"(soff));
}
__device__ __forceinline__ void dma16c(unsigned ldsa, unsigned voff, srd_t srd, unsigned soff) {
    asm volatile("s_mov_b32 m0, %0\n\ts_nop 0\n\tbuffer_load_dwordx4 %1, %2, %3 offen sc1 lds" :: "s"(ldsa), "v"(voff), "s"(srd), "s"(soff));
}
__device__ __forceinline__ void st8(srd_t srd, unsigned off, u32x2 v) { __builtin_amdgcn_raw_buffer_store_b64(v, srd, off, 0, ST_AUX); }
__device__ __forceinline__ void st16(srd_t srd, unsigned off, u32x4 v) { __builtin_amdgcn_raw_buffer_store_b128(v, srd, off, 0, ST_AUX); }
__device__ __forceinline__ u32x4 pk8(const f32x4 a, const f32x4 b) { u32x4 o; o.x = pk_bf16(a[0], a[1]); o.y = pk_bf16(a[2], a[3]); o.z = pk_bf16(b[0], b[1]); o.w = pk_bf16(b[2], b[3]); return o; }
#define MFMA16(a, b, c) __builtin_amdgcn_mfma_f32_16x16x32_bf16(a, b, c, 0, 0, 0)
#define MFMA32(a, b, c) __builtin_amdgcn_mfma_f32_32x32x16_bf16(a, b, c, 0, 0, 0)
#define WAITVL(n) asm volatile("s_waitcnt vmcnt(%0) lgkmcnt(0)" :: "n"(n) : "memory")
#define WAITBAR(n) asm volatile("s_waitcnt vmcnt(%0) lgkmcnt(0)\n\ts_barrier" :: "n"(n) : "memory")
#define LBAR() asm volatile("s_waitcnt lgkmcnt(0)\n\ts_barrier" ::: "memory")
#define SCHED0() __builtin_amdgcn_sched_barrier(0)
#define CODE_TOUCH(A, B, t0, nthr) do { unsigned long long pc_; int lo_, hi_; unsigned dm_ = 0u; \
        asm volatile("s_getpc_b64 %0\n.Lct%=:\n\ts_mov_b32 %1, " A "-.Lct%=\n\ts_mov_b32 %2, " B "-.Lct%=" : "=&s"(pc_), "=&s"(lo_), "=&s"(hi_)); \
        for (int o_ = lo_ + ((int)threadIdx.x - (t0)) * 128; o_ < hi_; o_ += (nthr) * 128) asm volatile("global_load_dword %0, %1, off" : "+v"(dm_) : "v"((const char*)pc_ + o_)); \
        asm volatile("s_waitcnt vmcnt(0)" : "+v"(dm_) :: "memory");        \
        } while (0)
#define CODE_TOUCH_ASYNC(A, B) do { unsigned long long pc_; int lo_, hi_; \
        asm volatile("s_getpc_b64 %0\n.Lct%=:\n\ts_mov_b32 %1, " A "-.Lct%=\n\ts_mov_b32 %2, " B "-.Lct%=" : "=&s"(pc_), "=&s"(lo_), "=&s"(hi_)); \
        const srd_t sc_ = mk_srd((const char*)pc_ + lo_, (unsigned)(hi_ - lo_)); \
        for (int o_ = 0; o_ < hi_ - lo_; o_ += 512 * 128) \
            asm volatile("s_mov_b32 m0, %0\n\ts_nop 0\n\tbuffer_load_dword %1, %2, %3 offen lds" :: "s"((unsigned)(uintptr_t)lds + 163584u), "v"(threadIdx.x * 128u), "s"(sc_), "s"(o_)); \
        } while (0)
#define WAITV(n) asm volatile("s_waitcnt vmcnt(%0)" :: "n"(n) : "memory")
#define RLX_AGENT __ATOMIC_RELAXED, __HIP_MEMORY_SCOPE_AGENT
typedef __attribute__((address_space(1))) unsigned gu32;
typedef __attribute__((address_space(1))) unsigned long long gu64;
struct Params { const u16 *x, *wqkv, *wo, *wg, *wu, *wd; u16* out; char* ws; };
__device__ __forceinline__ void flag_set(char* ws, int idx, unsigned tag) { __hip_atomic_store((gu32*)(ws + WS_FLG) + idx, tag, RLX_AGENT); }
__device__ __forceinline__ void flags_wait(char* ws, int idx, unsigned tag) {
    gu32* fl = (gu32*)(ws + WS_FLG);
    const unsigned long long t0 = __builtin_amdgcn_s_memrealtime();
    for (unsigned spins = 1;; ++spins) {
        const unsigned v = idx >= 0 ? __hip_atomic_load(fl + idx, RLX_AGENT) : tag;
        if (__all(v == tag)) break;
        if ((spins & 63u) == 0u) {
            if (__any(__hip_atomic_load(fl + FL_ERR, RLX_AGENT) == tag)) break;
            if (__builtin_amdgcn_s_memrealtime() - t0 > 2000000ull) { __hip_atomic_store(fl + FL_ERR, tag, RLX_AGENT); break; }
        }
        __builtin_amdgcn_s_sleep(1);
    }
    asm volatile("" ::: "memory");
}
template <int CTRL, int RM> __device__ __forceinline__ float dpp_add(float v) { return v + __int_as_float(__builtin_amdgcn_update_dpp(0, __float_as_int(v), CTRL, RM, 0xf, true)); }
__device__ __forceinline__ float sum32(float v) { v = dpp_add<0x111, 0xf>(v); v = dpp_add<0x112, 0xf>(v); v = dpp_add<0x114, 0xf>(v); v = dpp_add<0x118, 0xf>(v); return dpp_add<0x142, 0xa>(v); }
__device__ __forceinline__ void wait_mask(char* ws, int idx, unsigned tag, unsigned long long need, unsigned long long& rdy) {
    if ((rdy & need) == need) return;
    gu32* fl = (gu32*)(ws + WS_FLG);
    const unsigned long long t0 = __builtin_amdgcn_s_memrealtime();
    for (unsigned spins = 1;; ++spins) {
        const unsigned v = idx >= 0 ? __hip_atomic_load(fl + idx, RLX_AGENT) : ~tag;
        rdy = __ballot(v == tag);
        if ((rdy & need) == need) break;
        if ((spins & 63u) == 0u) {
            if (__any(__hip_atomic_load(fl + FL_ERR, RLX_AGENT) == tag)) break;
            if (__builtin_amdgcn_s_memrealtime() - t0 > 2000000ull) { __hip_atomic_store(fl + FL_ERR, tag, RLX_AGENT); break; }
        }
        __builtin_amdgcn_s_sleep(1);
    }
    asm volatile("" ::: "memory");
}
__device__ __forceinline__ u32x4 ldc16(srd_t srd, unsigned off) { return __builtin_amdgcn_raw_buffer_load_b128(srd, off, 0, 16); }
__device__ __forceinline__ float ldc4(srd_t srd, unsigned off) { return __uint_as_float(__builtin_amdgcn_raw_buffer_load_b32(srd, off, 0, 16)); }

constexpr size_t WS_LITE = (size_t)80 << 20;
#define LITE_BEGIN()
#define LITE_END()
#define STAMP_DECL
#define STAMP_AT(i)
#define STAMP_OUT(kid)
#define STAMP_ARGS
#define STAMP_PASS

constexpr int K1_SLOT = 40960, K1_NS = 4, K1_AUX = 0;
template <bool SYNC>
__device__ __forceinline__ void phase1(LAS char* lds, const Params& P, unsigned tag STAMP_ARGS) {
    const unsigned lds0 = (unsigned)(uintptr_t)lds;
    const int tid = threadIdx.x, lane = tid & 63, w = __builtin_amdgcn_readfirstlane(tid >> 6), g = lane >> 4, r = lane & 15;
    const int b = blockIdx.x, xcd = b & 7, loc = b >> 3;
        const int p = xcd * 2 + (loc & 1), rt = loc >> 1;
    char* ws = P.ws;
    constexpr int NT = DM / 64;
    LAS f32x2* stat = (LAS f32x2*)(lds + K1_AUX);
    LAS float* csl = (LAS float*)(lds + K1_AUX + 1024);
    if (__builtin_expect(w >= 4, 1)) {
        const srd_t srx = mk_srd(P.x, SEQ * DM * 2), srw = mk_srd(P.wqkv, DM * 3 * DM * 2);
        const int j = w - 4, rr = lane >> 3, c8 = lane & 7;
        unsigned xv[2], wv[2];
#pragma unroll
        for (int i = 0; i < 2; ++i) {
            xv[i] = rr * 2048 + ((c8 ^ (4 * i + (rr >> 1))) * 16);
            const int f = ((rr >> 1) & 1) | (i << 1);
            wv[i] = rr * 6144 + (((c8 >> 1) ^ f) * 32) + (c8 & 1) * 16;
        }
        const unsigned xs0 = (unsigned)(rt * 128 + 32 * j) * 2048u, ws0 = (unsigned)p * 128u + (unsigned)(16 * j) * 6144u;
        const unsigned xl0 = lds0 + j * 4096, wl0 = lds0 + 16384 + j * 2048;
#define K1_DMA(i, t, slot) do { if ((i) < 4) dma16(xl0 + (slot) + (i) * 1024, xv[(i) & 1], srx, xs0 + (i) * (8 * 2048) + (((t) + rt) & 15) * 128); \
        else dma16(wl0 + (slot) + (((i) - 4) >> 1) * 8192 + (((i) - 4) & 1) * 1024, wv[((i) - 4) & 1], srw, ws0 + (((i) - 4) >> 1) * 2048 + (((i) - 4) & 1) * (8 * 6144) + (((t) + rt) & 15) * (64 * 6144)); } while (0)
#define K1_STAGE(t, slot) do { _Pragma("unroll") for (int i_ = 0; i_ < 10; ++i_) K1_DMA(i_, t, slot); } while (0)
        const int srow = 8 * b + 2 * j + (lane >> 5);
        u32x4 xr[4];
        { const unsigned xo = (unsigned)srow * 2048u + (lane & 31) * 16;
#pragma unroll
          for (int k = 0; k < 4; ++k) asm volatile("buffer_load_dwordx4 %0, %1, %2, 0 offen offset:%3" : "=v"(xr[k]) : "v"(xo), "s"(srx), "n"(k * 512)); }
        K1_STAGE(0, 0); K1_STAGE(1, K1_SLOT); K1_STAGE(2, 2 * K1_SLOT);
        WAITBAR(20);
        asm volatile("" : "+v"(xr[0]), "+v"(xr[1]), "+v"(xr[2]), "+v"(xr[3]));
        K1_STAGE(3, 3 * K1_SLOT);
        {
            const unsigned x00 = xr[0][0];
            const float pv = bf_lo(lane < 32 ? __builtin_amdgcn_readlane(x00, 0) : __builtin_amdgcn_readlane(x00, 32));
            float s = 0.f, q = 0.f;
#pragma unroll
            for (int k = 0; k < 4; ++k)
#pragma unroll
                for (int e = 0; e < 4; ++e) { const unsigned d = xr[k][e]; const float u = bf_lo(d) - pv, v = bf_hi(d) - pv; s += u + v; q += u * u + v * v; }
            s = sum32(s); q = sum32(q);
            const float dm = s * (1.0f / DM), var = fmaxf(q * (1.0f / DM) - dm * dm, 0.f);
            const u32x2 sv = {__float_as_uint(pv + dm), __float_as_uint(1.0f / sqrtf(var + LN_EPS))};
            __builtin_amdgcn_raw_buffer_store_b64(sv, mk_srd(ws + WS_S1, SEQ * 8), (lane & 31) == 31 ? (unsigned)srow * 8u : 0x7ff00000u, 0, 16);
        }
        WAITBAR(21); K1_STAGE(4, 0);
        WAITBAR(21); K1_STAGE(5, K1_SLOT);
        WAITBAR(21); K1_STAGE(6, 2 * K1_SLOT);
        WAITBAR(20);
        if (SYNC && tid == 256) flag_set(ws, FL_0 + b, tag);
        K1_STAGE(7, 3 * K1_SLOT);
        WAITBAR(20); K1_STAGE(8, 0);
        WAITBAR(20); K1_STAGE(9, K1_SLOT);
        WAITBAR(20); K1_STAGE(10, 2 * K1_SLOT);
        WAITBAR(20);
        const int fidx = lane < 16 ? FL_0 + 16 * rt + lane : lane < 32 ? FL_0 + ((lane - 16) * 2 + (p & 1)) * 8 + (p >> 1) : -1;
        unsigned fseen;
        asm volatile("global_load_dword %0, %1, off sc1" : "=v"(fseen) : "v"((const char*)(ws + WS_FLG) + (fidx >= 0 ? fidx : FL_0 + b) * 4));
        K1_STAGE(11, 3 * K1_SLOT);
        WAITBAR(21); K1_STAGE(12, 0);
        WAITBAR(21); K1_STAGE(13, K1_SLOT);
        WAITBAR(20);
        asm volatile("" : "+v"(fseen));
        K1_STAGE(14, 2 * K1_SLOT);
        if (SYNC && !__all(fseen == tag)) flags_wait(ws, fidx, tag);
        u32x4 gs, gp[16];
        const int tc = 48 * j + 4 * lane;
        { const srd_t srs = mk_srd(ws + WS_S1, SEQ * 8), src1 = mk_srd(ws + WS_C1, 16 * 3072 * 4);
          const unsigned so = j == 0 ? (unsigned)(rt * 128) * 8u + lane * 16 : 0x7ff00000u, po = lane < 12 ? (unsigned)((tc >> 6) * 1024 + p * 64 + (tc & 63)) * 4u : 0x7ff00000u;
          asm volatile("buffer_load_dwordx4 %0, %1, %2, 0 offen sc1" : "=v"(gs) : "v"(so), "s"(srs));
#pragma unroll
          for (int i = 0; i < 16; ++i) asm volatile("buffer_load_dwordx4 %0, %1, %2, %3 offen sc1" : "=v"(gp[i]) : "v"(po), "s"(src1), "s"(i * 3072 * 4)); }
        WAITBAR(37); K1_STAGE(15, 3 * K1_SLOT);
        WAITBAR(37);
        WAITBAR(10);
        asm volatile("" : "+v"(gs), "+v"(gp[0]), "+v"(gp[1]), "+v"(gp[2]), "+v"(gp[3]), "+v"(gp[4]), "+v"(gp[5]), "+v"(gp[6]), "+v"(gp[7]));
        asm volatile("" : "+v"(gp[8]), "+v"(gp[9]), "+v"(gp[10]), "+v"(gp[11]), "+v"(gp[12]), "+v"(gp[13]), "+v"(gp[14]), "+v"(gp[15]));
        {
            f32x4 v = {0.f, 0.f, 0.f, 0.f};
#pragma unroll
            for (int i = 0; i < 16; ++i) v += __builtin_bit_cast(f32x4, gp[i]);
            if (lane < 12) *(LAS f32x4*)(csl + tc) = v;
            if (j == 0) *(LAS u32x4*)(lds + K1_AUX + lane * 16) = gs;
        }
        WAITBAR(0);
    } else {
        const int wr = w >> 1, wc = w & 1;
#define CWV 2
        CODE_TOUCH(".Lcw_p0", ".Lcw_p2", 0, 256); CODE_TOUCH(".Lcw_p5", ".Lcw_end", 0, 256);
        int xrd[2], wrd[2];
        { const int q4 = (lane >> 2) & 3, p4 = lane & 3, trow = 8 * g + q4, f = ((trow >> 1) & 1) | (((trow >> 3) & 1) << 1);
#pragma unroll
          for (int s = 0; s < 2; ++s) { xrd[s] = (64 * wr + r) * 128 + (((4 * s + g) ^ ((r >> 1) & 7)) * 16); wrd[s] = 16384 + trow * 128 + (((2 * wc + s) ^ f) * 32) + (p4 >> 1) * 16 + (p4 & 1) * 8; } }
        f32x4 acc[4][6];
#pragma unroll
        for (int m = 0; m < 4; ++m)
#pragma unroll
            for (int n = 0; n < 6; ++n) acc[m][n] = (f32x4){0.f, 0.f, 0.f, 0.f};
        bf16x8 xa[4], xb[4]; s16x4 wa[12], wb[12];
#define K1_RW(i, W, sl, s) W[i] = trd(lds + (sl) + wrd[((i) >> 1) & 1] + ((i) >> 2) * 8192 + (s) * 4096 + ((i) & 1) * 512)
#define K1_RX(i, X, sl, s) X[i] = ldf(lds + (sl) + xrd[s] + (i) * 2048)
#define K1_RD(i, X, W, sl, s) do { if ((i) == 2) K1_RX(0, X, sl, s); else if ((i) == 7) K1_RX(1, X, sl, s); else if ((i) == 12) K1_RX(2, X, sl, s); else if ((i) == 15) K1_RX(3, X, sl, s); \
        else K1_RW((i) < 2 ? (i) : (i) < 7 ? (i) - 1 : (i) < 12 ? (i) - 2 : (i) - 3, W, sl, s); } while (0)
#define K1_MM(i, X, W) do { const int m_ = (i) / 6, n_ = (i) % 6; const bf16x8 wf_ = cat8(W[2 * n_], W[2 * n_ + 1]); \
        acc[m_][n_] = (n_ < 4) ? MFMA16(wf_, X[m_], acc[m_][n_]) : MFMA16(X[m_], wf_, acc[m_][n_]); } while (0)
#define K1_HALF(XM, WM, XR, WR, sl, s, DOMMA) do { \
        _Pragma("unroll") for (int i_ = 0; i_ < 24; ++i_) { \
            if (DOMMA) K1_MM(i_, XM, WM); \
            if (i_ < 16) K1_RD(i_, XR, WR, sl, s); \
            SCHED0(); } } while (0)
        LBAR();
        STAMP_AT(2);
        K1_HALF(xb, wb, xa, wa, 0, 0, false);
        K1_HALF(xa, wa, xb, wb, 0, 1, true);
        if (wr == 0) {
            bf16x8 one8;
#pragma unroll
            for (int e = 0; e < 8; ++e) one8[e] = (short)0x3f80;
            const srd_t src1 = mk_srd(ws + WS_C1, 16 * 3072 * 4);
#pragma unroll
            for (int n = 0; n < 6; ++n) {
                f32x4 c = MFMA16(cat8(wa[2 * n], wa[2 * n + 1]), one8, ((f32x4){0.f, 0.f, 0.f, 0.f}));
                c = MFMA16(cat8(wb[2 * n], wb[2 * n + 1]), one8, c);
                __builtin_amdgcn_raw_buffer_store_b128(__builtin_bit_cast(u32x4, c), src1, r == 0 ? (unsigned)(rt * 3072 + (n >> 1) * 1024 + p * 64 + wc * 32 + (n & 1) * 16 + 4 * g) * 4u : 0x7ff00000u, 0, 16);
            }
        }
        int cur = K1_SLOT;
        LBAR();
        K1_HALF(xb, wb, xa, wa, cur, 0, true);
        K1_HALF(xa, wa, xb, wb, cur, 1, true);
        cur = 2 * K1_SLOT;
        LBAR();
        K1_HALF(xb, wb, xa, wa, cur, 0, true);
        K1_HALF(xa, wa, xb, wb, cur, 1, true);
        cur = 3 * K1_SLOT;
        WAITBAR(0);
        K1_HALF(xb, wb, xa, wa, cur, 0, true);
        K1_HALF(xa, wa, xb, wb, cur, 1, true);
        cur = 0;
#pragma unroll 1
        for (int t = 4; t < NT; ++t) {
            LBAR();
            K1_HALF(xb, wb, xa, wa, cur, 0, true);
            K1_HALF(xa, wa, xb, wb, cur, 1, true);
            cur = (cur == 3 * K1_SLOT) ? 0 : cur + K1_SLOT;
        }
#pragma unroll
        for (int i = 0; i < 24; ++i) K1_MM(i, xb, wb);
        STAMP_AT(3);
        const int head = 2 * p + wc;
        const srd_t srq = mk_srd(ws + WS_Q, 12u << 20);
        f32x4 cq[4]; float cv[2];
#pragma unroll
        for (int n = 0; n < 4; ++n) cq[n] = *(LAS const f32x4*)(csl + (n >> 1) * 64 + wc * 32 + (n & 1) * 16 + 4 * g);
#pragma unroll
        for (int n = 0; n < 2; ++n) cv[n] = csl[128 + wc * 32 + n * 16 + r];
        const int rho = 16 * (r >> 3) + 4 * ((r >> 2) & 1) + (r & 3);
        f32x4 vv[2][2];
#pragma unroll
        for (int m = 0; m < 4; ++m) {
            const int lrow = wr * 64 + m * 16, row0 = rt * 128 + lrow;
            const unsigned blk = ((unsigned)head * 64 + (row0 >> 5)) * 2048u + (g >> 1) * 1024 + (g & 1) * 512;
            const f32x2 st = stat[lrow + r];
            const float qs = st.y * QSCALE;
            st16(srq, (4u << 20) + blk + (rho + (m & 1) * 8) * 16, pk8((acc[m][2] - st.x * cq[2]) * st.y, (acc[m][3] - st.x * cq[3]) * st.y));
            const f32x4 s01 = *(LAS const f32x4*)(stat + lrow + 4 * g), s23 = *(LAS const f32x4*)(stat + lrow + 4 * g + 2);
            const f32x4 mean4 = {s01[0], s01[2], s23[0], s23[2]}, rstd4 = {s01[1], s01[3], s23[1], s23[3]};
#pragma unroll
            for (int n = 0; n < 2; ++n) vv[m & 1][n] = (acc[m][4 + n] - mean4 * cv[n]) * rstd4;
            if (m & 1) {
#pragma unroll
                for (int n = 0; n < 2; ++n) st16(srq, (8u << 20) + blk + (16 * n + r) * 16, pk8(vv[0][n], vv[1][n]));
            }
            st16(srq, blk + ((m & 1) * 16 + r) * 16, pk8((acc[m][0] - st.x * cq[0]) * qs, (acc[m][1] - st.x * cq[1]) * qs));
        }
        asm volatile(".Lcw_end:");
    }
    STAMP_AT(4);
    WAITBAR(0);
    if (SYNC && tid == 0) flag_set(ws, FL_1 + p * 16 + rt, tag);
    CODE_TOUCH_ASYNC(".Lcw_p1", ".Lcw_p2");
}

constexpr int K2_SLOT = 32768, K2_SIDE = 3 * K2_SLOT;
struct K2Side { srd_t src, srp, sro; unsigned cv, cs0, ps0; };
template <int KH>
__device__ __forceinline__ void attn_fast(LAS char* lds, srd_t srk, srd_t srv, unsigned hbase, const K2Side& S, int w, int lane, const bf16x8 (&qf)[2][2], f32x16 (&o)[2], f32x4 (&lacc)[2], f32x4& cs) {
    const int tl = w & 3;
    const unsigned lds0 = (unsigned)(uintptr_t)lds;
    const unsigned lv = lane * 16;
    const unsigned s0 = hbase + (unsigned)(KH * 32 + tl) * 2048u, d0 = lds0 + KH * 16384 + tl * 2048;
    const int rd0 = KH * 16384 + lane * 16;
    bf16x8 ones, sel;
    { const short v = ((lane & 15) == ((lane >> 4) & 1)) ? (short)0x3f80 : (short)0;
#pragma unroll
      for (int j = 0; j < 8; ++j) { ones[j] = v; sel[j] = ((lane & 15) == j) ? (short)0x3f80 : (short)0; } }
    const unsigned sd = lds0 + K2_SIDE + w * 1024;
#define K2_DMA(k, rd, fr) do { \
        if ((k) < 4) dma16c(d0 + (fr) + ((k) >> 1) * 8192 + ((k) & 1) * 1024, lv, (k) < 2 ? srk : srv, s0 + ((rd) + 2) * 8192 + ((k) & 1) * 1024); \
        else if ((k) == 4) dma16(sd + sc, S.cv, S.src, S.cs0 + (rd) * (4 * FF * 2)); \
        else dma16(sd + 24576, lv, (rd) < 4 ? S.srp : S.sro, (rd) < 4 ? S.ps0 + (rd) * 1024 : (rd) == 4 ? (S.ps0 >> 2) : 0x7ff00000u); } while (0)
#define K2_HOOK(gi, rd, fr) do { if (((gi) >> 1) < 6 && ((gi) & 1) == KH) K2_DMA((gi) >> 1, rd, fr); } while (0)
    f32x16 sa[2], sb[2];
    const f32x16 zero16 = {};
    bf16x8 k0 = ldf(lds + rd0), k1 = ldf(lds + rd0 + 1024), v0, v1;
#pragma unroll
    for (int qt = 0; qt < 2; ++qt) { sa[qt] = MFMA32(k0, qf[qt][0], zero16); sa[qt] = MFMA32(k1, qf[qt][1], sa[qt]); }
    k0 = ldf(lds + rd0 + 2048);
    u32x4 pa = {0u, 0u, 0u, 0u}, pb = {0u, 0u, 0u, 0u}; float e6 = 0.f, e7 = 0.f;
    v1 = k1;
    bf16x8 csd = {};
#define EX2(x) __builtin_amdgcn_exp2f(x)
#define K2_GROUP(S_, B, PC, PP, QK, VP, QP, RD, gi) do { \
        QK; PP[3] = pk_bf16(e6, e7); SCHED0(); \
        const float x0 = EX2(S_[B + 0]), x1 = EX2(S_[B + 1]), x2 = EX2(S_[B + 2]); RD; SCHED0(); \
        o[QP] = MFMA32(VP, __builtin_bit_cast(bf16x8, PP), o[QP]); SCHED0(); \
        const float x3 = EX2(S_[B + 3]), x4 = EX2(S_[B + 4]), x5 = EX2(S_[B + 5]); PC[0] = pk_bf16(x0, x1); SCHED0(); \
        lacc[QP] = MFMA16(ones, __builtin_bit_cast(bf16x8, PP), lacc[QP]); K2_HOOK(gi, rd, fr); SCHED0(); \
        e6 = EX2(S_[B + 6]); e7 = EX2(S_[B + 7]); PC[1] = pk_bf16(x2, x3); PC[2] = pk_bf16(x4, x5); SCHED0(); } while (0)
#define K2_TILE(SC, SN, kn1, kn2, va, ti) do { \
        K2_GROUP(SC[0], 0, pa, pb, SN[0] = MFMA32(k0, qf[0][0], zero16), v1, 1, v0 = ldf(va), 4 * (ti)); \
        K2_GROUP(SC[1], 0, pb, pa, SN[1] = MFMA32(k0, qf[1][0], zero16), v0, 0, k1 = ldf((kn1) + 1024), 4 * (ti) + 1); \
        K2_GROUP(SC[0], 8, pa, pb, SN[0] = MFMA32(k1, qf[0][1], SN[0]), v0, 1, v1 = ldf((va) + 1024), 4 * (ti) + 2); \
        K2_GROUP(SC[1], 8, pb, pa, SN[1] = MFMA32(k1, qf[1][1], SN[1]), v1, 0, k0 = ldf(kn2), 4 * (ti) + 3); } while (0)
    int cur = 0, fr = 2 * K2_SLOT, sc = 0;
#pragma unroll 1
    for (int rd = 0; rd < 8; ++rd) {
        const int nx = (cur == 2 * K2_SLOT) ? 0 : cur + K2_SLOT;
        const int sn = (sc == 16384) ? 0 : sc + 8192;
        if (rd > 0) WAITBAR(2);
        LAS const char* pc = lds + cur + rd0; LAS const char* pn = lds + nx + rd0;
        K2_TILE(sa, sb, pc + 2048, pc + 4096, pc + 8192, 0);
        csd = ldf(lds + K2_SIDE + sn + w * 1024 + lane * 16);
        if (rd < 2) csd = (bf16x8){};
        K2_TILE(sb, sa, pc + 4096, pc + 6144, pc + 8192 + 2048, 1);
        cs = MFMA16(sel, csd, cs);
        K2_TILE(sa, sb, pc + 6144, pn, pc + 8192 + 4096, 2);
        K2_TILE(sb, sa, pn, pn + 2048, pc + 8192 + 6144, 3);
        fr = cur; cur = nx; sc = sn;
    }
    pb[3] = pk_bf16(e6, e7);
    o[1] = MFMA32(v1, __builtin_bit_cast(bf16x8, pb), o[1]);
    lacc[1] = MFMA16(ones, __builtin_bit_cast(bf16x8, pb), lacc[1]);
    WAITBAR(0);
    cs = MFMA16(sel, ldf(lds + K2_SIDE + w * 1024 + lane * 16), cs);
    cs = MFMA16(sel, ldf(lds + K2_SIDE + 8192 + w * 1024 + lane * 16), cs);
#undef K2_GROUP
#undef K2_TILE
#undef K2_DMA
#undef K2_HOOK
}
#define K2_STAGE(rd, slot) do { dma16c(d0 + (slot), lv, srk, s0 + (rd) * 8192); dma16c(d0 + (slot) + 1024, lv, srk, s0 + (rd) * 8192 + 1024); \
        dma16c(d0 + (slot) + 8192, lv, srv, s0 + (rd) * 8192); dma16c(d0 + (slot) + 9216, lv, srv, s0 + (rd) * 8192 + 1024); } while (0)
__device__ __forceinline__ void attn_safe(LAS char* lds, srd_t srk, srd_t srv, unsigned hbase, const bf16x8 (&qf)[2][2], int w, int lane, f32x16 (&o)[2], float (&mrun)[2], float (&lrun)[2]) {
    const int kh = w >> 2, tl = w & 3;
    const unsigned lds0 = (unsigned)(uintptr_t)lds;
    const unsigned lv = lane * 16;
    const unsigned s0 = hbase + (unsigned)(kh * 32 + tl) * 2048u, d0 = lds0 + kh * 16384 + tl * 2048;
    const int rd0 = kh * 16384 + lane * 16;
    const f32x16 zero16 = {};
#pragma unroll 1
    for (int rd = 0; rd < 8; ++rd) {
        K2_STAGE(rd, 0);
        WAITBAR(0);
#pragma unroll 1
        for (int i = 0; i < 4; ++i) {
            LAS const char* pk = lds + rd0 + i * 2048;
            const bf16x8 k0 = ldf(pk), k1 = ldf(pk + 1024), v0 = ldf(pk + 8192), v1 = ldf(pk + 9216);
#pragma unroll
            for (int qt = 0; qt < 2; ++qt) {
                f32x16 sc = MFMA32(k0, qf[qt][0], zero16); sc = MFMA32(k1, qf[qt][1], sc);
                float mx = sc[0];
#pragma unroll
                for (int j = 1; j < 16; ++j) mx = fmaxf(mx, sc[j]);
                mx = fmaxf(mx, __shfl_xor(mx, 32));
                const float mn = fmaxf(mrun[qt], mx);
                const float ms = (mn == -INFINITY) ? 0.f : mn;
                const float al = __builtin_amdgcn_exp2f(mrun[qt] - ms);
                mrun[qt] = mn;
                u32x4 p0, p1;
#pragma unroll
                for (int j = 0; j < 4; ++j) { p0[j] = pk_bf16(__builtin_amdgcn_exp2f(sc[2 * j] - ms), __builtin_amdgcn_exp2f(sc[2 * j + 1] - ms));
                                              p1[j] = pk_bf16(__builtin_amdgcn_exp2f(sc[8 + 2 * j] - ms), __builtin_amdgcn_exp2f(sc[9 + 2 * j] - ms)); }
                float s_ = 0.f;
#pragma unroll
                for (int j = 0; j < 4; ++j) { const unsigned a = p0[j], c = p1[j]; s_ += bf_lo(a) + bf_hi(a); s_ += bf_lo(c) + bf_hi(c); }
                lrun[qt] = lrun[qt] * al + s_;
#pragma unroll
                for (int j = 0; j < 16; ++j) o[qt][j] *= al;
                o[qt] = MFMA32(v0, __builtin_bit_cast(bf16x8, p0), o[qt]); o[qt] = MFMA32(v1, __builtin_bit_cast(bf16x8, p1), o[qt]);
            }
        }
        WAITBAR(0);
    }
}

template <bool SYNC>
__device__ __forceinline__ void phase2(LAS char* lds, const Params& P, unsigned tag STAMP_ARGS) {
    const int tid = threadIdx.x, lane = tid & 63, w = __builtin_amdgcn_readfirstlane(tid >> 6);
    const int b = blockIdx.x, xcd = b & 7, loc = b >> 3;
    const int head = xcd * 4 + (loc & 3), qb = loc >> 2;
    char* ws = P.ws;
    K2Side S;
    const int cgrp = b % 56, rq = b / 56;
    const bool do_cs = b < 224;
    S.src = mk_srd(cgrp < 28 ? P.wg : P.wu, DM * FF * 2);
    S.cv = (unsigned)(lane >> 4) * (FF * 2) + (lane & 15) * 16;
    S.cs0 = do_cs ? (unsigned)(rq * 256 + w * 32) * (FF * 2) + (unsigned)(cgrp < 28 ? cgrp : cgrp - 28) * 256u : 0x7ff00000u;
    S.srp = mk_srd(P.wd, FF * DM * 2); S.sro = mk_srd(P.wo, DM * DM * 2);
    S.ps0 = (unsigned)(b * 8 + w) * 4096u;
    const srd_t srk = mk_srd(ws + WS_K, SEQ * DM * 2), srv = mk_srd(ws + WS_V, SEQ * DM * 2);
    const unsigned hbase = (unsigned)head * 64u * 2048u;
    const int kh = w >> 2;
    if (SYNC) { if (w == 0) flags_wait(ws, lane < 16 ? FL_1 + (head >> 1) * 16 + lane : -1, tag); LBAR(); }
    STAMP_AT(1);
    bf16x8 qf[2][2];
    {
        const unsigned lds0 = (unsigned)(uintptr_t)lds, lv = lane * 16;
        const unsigned s0 = hbase + (unsigned)(kh * 32 + (w & 3)) * 2048u, d0 = lds0 + kh * 16384 + (w & 3) * 2048;
        K2_STAGE(0, 0);
        const srd_t srq = mk_srd(ws + WS_Q, SEQ * DM * 2);
        const unsigned qo = ((unsigned)head * 64 + qb * 8 + (w & 3) * 2) * 2048u + lane * 16;
#pragma unroll
        for (int qt = 0; qt < 2; ++qt)
#pragma unroll
            for (int s = 0; s < 2; ++s) qf[qt][s] = __builtin_bit_cast(bf16x8, ldc16(srq, qo + qt * 2048 + s * 1024));
        K2_STAGE(1, K2_SLOT);
    }
    WAITBAR(0);
    STAMP_AT(2);
    f32x16 o[2] = {}; f32x4 lacc[2] = {}; f32x4 cs = {0.f, 0.f, 0.f, 0.f};
    if (kh == 0) attn_fast<0>(lds, srk, srv, hbase, S, w, lane, qf, o, lacc, cs);
    else attn_fast<1>(lds, srk, srv, hbase, S, w, lane, qf, o, lacc, cs);
    STAMP_AT(3);
    LAS float* ob = (LAS float*)lds;
    LAS float* lb = (LAS float*)(lds + 32768);
    LAS unsigned* flag = (LAS unsigned*)(lds + 32768 + 4096);
    LAS float* cb = (LAS float*)(lds + 40960);
    const int hi = lane >> 5, q32 = lane & 31;
    const srd_t sro = mk_srd(ws + WS_O, SEQ * DM * 2);
    const unsigned dst = FRAG(qb * 16 + (w & 3) * 4 + kh * 2 + (q32 >> 4), head, 32) + (hi * 16 + (q32 & 15)) * 16;
    bool bad;
    {
        if (tid == 0) flag[0] = 0u;
        if (lane < 16) { lb[w * 64 + lane] = lacc[0][0]; lb[w * 64 + 16 + lane] = lacc[0][1]; lb[w * 64 + 32 + lane] = lacc[1][0]; lb[w * 64 + 48 + lane] = lacc[1][1]; }
#pragma unroll
        for (int j = 0; j < 4; ++j) { const f32x4 v = kh ? (f32x4){o[0][4 * j], o[0][4 * j + 1], o[0][4 * j + 2], o[0][4 * j + 3]} : (f32x4){o[1][4 * j], o[1][4 * j + 1], o[1][4 * j + 2], o[1][4 * j + 3]};
            *(LAS f32x4*)(ob + ((w * 4 + j) * 64 + lane) * 4) = v; }
        if (lane < 32) *(LAS f32x4*)(cb + w * 128 + (lane & 15) * 8 + (lane >> 4) * 4) = cs;
        LBAR();
        const int pw = w ^ 4;
        f32x16 of;
#pragma unroll
        for (int j = 0; j < 4; ++j) { const f32x4 v = *(LAS const f32x4*)(ob + ((pw * 4 + j) * 64 + lane) * 4);
#pragma unroll
            for (int e = 0; e < 4; ++e) of[4 * j + e] = (kh ? o[1][4 * j + e] : o[0][4 * j + e]) + v[e]; }
        const float l = lb[w * 64 + kh * 32 + q32] + lb[pw * 64 + kh * 32 + q32];
        bool ok = (l >= 0x1p-60f) && (l <= 0x1p+80f);
#pragma unroll
        for (int j = 0; j < 16; ++j) ok = ok && (fabsf(of[j]) <= 0x1p+120f);
        if (!__all(ok)) { if (lane == 0) flag[0] = 1u; }
        if (do_cs && tid < 128) {
            float s = 0.f;
#pragma unroll
            for (int i = 0; i < 8; ++i) s += cb[i * 128 + tid];
            __hip_atomic_store((gu32*)(ws + WS_CS) + (rq * 7168 + cgrp * 128 + tid), __float_as_uint(s), RLX_AGENT);
        }
        LBAR();
        bad = flag[0] != 0u;
        if (!bad) {
            const float rl = 1.0f / l;
#pragma unroll
            for (int c = 0; c < 2; ++c) {
                u32x4 ov; ov.x = pk_bf16(of[8 * c + 0] * rl, of[8 * c + 1] * rl); ov.y = pk_bf16(of[8 * c + 2] * rl, of[8 * c + 3] * rl);
                ov.z = pk_bf16(of[8 * c + 4] * rl, of[8 * c + 5] * rl); ov.w = pk_bf16(of[8 * c + 6] * rl, of[8 * c + 7] * rl);
                st16(sro, dst + c * 512, ov);
            }
        }
    }
    if (bad) {
        WAITBAR(0);
        o[0] = (f32x16){}; o[1] = (f32x16){}; float mrun[2] = {-INFINITY, -INFINITY}, lrun[2] = {0.f, 0.f};
        attn_safe(lds, srk, srv, hbase, qf, w, lane, o, mrun, lrun);
        LAS float* mb = lb + 512;
#pragma unroll
        for (int qt = 0; qt < 2; ++qt) { const float lt = lrun[qt] + __shfl_xor(lrun[qt], 32); if (lane < 32) { lb[w * 64 + qt * 32 + lane] = lt; mb[w * 64 + qt * 32 + lane] = mrun[qt]; } }
#pragma unroll
        for (int j = 0; j < 16; ++j) ob[(w * 16 + j) * 64 + lane] = kh ? o[0][j] : o[1][j];
        WAITBAR(0);
        const int pw = w ^ 4;
        const float ma = mb[w * 64 + kh * 32 + q32], mp = mb[pw * 64 + kh * 32 + q32];
        const float mn = fmaxf(ma, mp), ms = (mn == -INFINITY) ? 0.f : mn;
        const float fa = __builtin_amdgcn_exp2f(ma - ms), fp = __builtin_amdgcn_exp2f(mp - ms);
        const float l = lb[w * 64 + kh * 32 + q32] * fa + lb[pw * 64 + kh * 32 + q32] * fp;
        const float rl = 1.0f / l;
        float of[16];
#pragma unroll
        for (int j = 0; j < 16; ++j) of[j] = ((kh ? o[1][j] : o[0][j]) * fa + ob[(pw * 16 + j) * 64 + lane] * fp) * rl;
#pragma unroll
        for (int c = 0; c < 2; ++c) {
            u32x4 ov; ov.x = pk_bf16(of[8 * c + 0], of[8 * c + 1]); ov.y = pk_bf16(of[8 * c + 2], of[8 * c + 3]);
            ov.z = pk_bf16(of[8 * c + 4], of[8 * c + 5]); ov.w = pk_bf16(of[8 * c + 6], of[8 * c + 7]);
            st16(sro, dst + c * 512, ov);
        }
    }
    STAMP_AT(4);
    WAITBAR(0);
    if (SYNC && tid == 0) flag_set(ws, FL_2 + qb * 32 + head, tag);
    CODE_TOUCH_ASYNC(".Lcw_p2", ".Lcw_p3");
}
#undef K2_STAGE

constexpr int K3_SLOT = 24576, K3_NS = 6, K3_AUX = K3_NS * K3_SLOT;
template <int MODE, bool SYNC>
__device__ __forceinline__ void phase35(LAS char* lds, const Params& P, unsigned tag STAMP_ARGS) {
    constexpr int KD = MODE == 0 ? DM : FF, KT = KD / 32, NT = KD / 64;
    const unsigned lds0 = (unsigned)(uintptr_t)lds;
    const int tid = threadIdx.x, lane = tid & 63, w = __builtin_amdgcn_readfirstlane(tid >> 6), g = lane >> 4, r = lane & 15;
    const int b = blockIdx.x, xcd = b & 7, loc = b >> 3;
    const int rt = (xcd >> 1) * 4 + (loc & 3), ct = (xcd & 1) * 8 + (loc >> 2);
    char* ws = P.ws;
    if (w >= 4) {
        const int j = w - 4;
        const srd_t srx = mk_srd(ws + (MODE == 0 ? WS_O : WS_ACT), SEQ * KD * 2), srw = mk_srd(MODE == 0 ? P.wo : P.wd, KD * DM * 2);
        const unsigned xv = lane * 16, xs0 = FRAG(rt * 8 + 2 * j, 0, KT);
        constexpr unsigned XM = FRAG(1, 0, KT), XK = FRAG(0, 1, KT);
        const int wrow = lane >> 3, wch = (lane & 7) ^ (((wrow >> 1) & 3) << 1);
        const unsigned wv = (unsigned)wrow * 2048u + wch * 16, ws0 = (unsigned)ct * 128u + (unsigned)(16 * j) * 2048u;
        const unsigned ld0 = lds0 + j * 4096, ld1 = lds0 + 16384 + j * 2048;
        const int rot = 2 * xcd, fidx = lane < 32 ? FL_2 + (rt >> 1) * 32 + lane : -1;
        unsigned long long rdy = SYNC ? 0ull : ~0ull;
#define K3_T(t) (((t) + rot) & 15)
#define K3_W(t, slot) do { dma16(ld1 + (slot), wv, srw, ws0 + K3_T(t) * (64 * 2048)); dma16(ld1 + (slot) + 1024, wv, srw, ws0 + 8 * 2048 + K3_T(t) * (64 * 2048)); } while (0)
#define K3_X(t, slot) do { wait_mask(ws, fidx, tag, 3ull << (2 * K3_T(t)), rdy); \
        dma16c(ld0 + (slot), xv, srx, xs0 + K3_T(t) * (2 * XK)); dma16c(ld0 + (slot) + 1024, xv, srx, xs0 + K3_T(t) * (2 * XK) + XK); \
        dma16c(ld0 + (slot) + 2048, xv, srx, xs0 + XM + K3_T(t) * (2 * XK)); dma16c(ld0 + (slot) + 3072, xv, srx, xs0 + XM + K3_T(t) * (2 * XK) + XK); } while (0)
        K3_W(0, 0); K3_W(1, K3_SLOT); K3_W(2, 2 * K3_SLOT); K3_W(3, 3 * K3_SLOT); K3_W(4, 4 * K3_SLOT);
        K3_X(0, 0); K3_X(1, K3_SLOT); K3_X(2, 2 * K3_SLOT);
        WAITBAR(8);  K3_X(3, 3 * K3_SLOT); K3_X(4, 4 * K3_SLOT); K3_X(5, 5 * K3_SLOT); K3_W(5, 5 * K3_SLOT);
        WAITBAR(18); K3_X(6, 0); K3_W(6, 0);
        WAITBAR(20); K3_X(7, K3_SLOT); K3_W(7, K3_SLOT);
        WAITBAR(22); K3_X(8, 2 * K3_SLOT); K3_W(8, 2 * K3_SLOT);
        int fr = 3 * K3_SLOT;
#pragma unroll 1
        for (int t = 4; t + K3_NS - 1 < NT; ++t) {
            WAITBAR(6 * (K3_NS - 2));
            K3_X(t + K3_NS - 1, fr); K3_W(t + K3_NS - 1, fr);
            fr = (fr == (K3_NS - 1) * K3_SLOT) ? 0 : fr + K3_SLOT;
        }
        WAITBAR(24); WAITBAR(18); WAITBAR(12); WAITBAR(6); WAITBAR(0);
        if (MODE == 0) LBAR();
    } else {
        const int wm = w >> 1, wn = w & 1;
        const int col0 = ct * 64 + wn * 32 + 4 * g;
        STAMP_AT(1);
        u32x4 res[4];
#pragma unroll
        for (int m = 0; m < 4; ++m) {
            const int mrow = rt * 128 + wm * 64 + m * 16;
            if (MODE == 0) { const u32x2 a = *(const u32x2*)(P.x + (size_t)(mrow + r) * DM + col0), c = *(const u32x2*)(P.x + (size_t)(mrow + r) * DM + col0 + 16); res[m] = (u32x4){a.x, a.y, c.x, c.y}; }
            else res[m] = ldc16(mk_srd(ws + WS_H, SEQ * DM * 2), FRAG(mrow >> 4, ct * 2 + wn, 32) + lane * 16);
        }
        const int xrd = wm * 8192 + lane * 16;
        const int q4 = (lane >> 2) & 3, p4 = lane & 3;
        const int trow = (MODE == 0) ? (4 * (g & 1) + 16 * (g >> 1) + q4) : (4 * g + q4);
        constexpr int TR2 = (MODE == 0) ? 8 * 128 : 16 * 128;
        int wrd[2];
#pragma unroll
        for (int n = 0; n < 2; ++n) wrd[n] = 16384 + trow * 128 + (((wn * 2 + n) ^ ((trow >> 1) & 3)) * 32) + (p4 >> 1) * 16 + (p4 & 1) * 8;
        f32x4 acc[4][2];
#pragma unroll
        for (int m = 0; m < 4; ++m) { acc[m][0] = (f32x4){0.f, 0.f, 0.f, 0.f}; acc[m][1] = (f32x4){0.f, 0.f, 0.f, 0.f}; }
        bf16x8 xa[4], xb[4]; s16x4 wa[4], wb[4];
#define K3_RD(i, X, W, sl, s) do { if ((i) == 2) X[0] = ldf(lds + (sl) + xrd + (s) * 1024); else if ((i) >= 5) X[(i) - 4] = ldf(lds + (sl) + xrd + ((i) - 4) * 2048 + (s) * 1024); \
        else { const int k_ = (i) < 2 ? (i) : (i) - 1; W[k_] = trd(lds + (sl) + wrd[k_ >> 1] + (k_ & 1) * TR2 + (s) * 4096); } } while (0)
#define K3_MM(i, X, W) do { const int m_ = (i) >> 1, n_ = (i) & 1; acc[m_][n_] = MFMA16(cat8(W[2 * n_], W[2 * n_ + 1]), X[m_], acc[m_][n_]); } while (0)
#define K3_HALF(XM, WM, XR, WR, sl, s, DOMMA) do { _Pragma("unroll") for (int i_ = 0; i_ < 8; ++i_) { if (DOMMA) K3_MM(i_, XM, WM); K3_RD(i_, XR, WR, sl, s); SCHED0(); } } while (0)
        LBAR();
        STAMP_AT(2);
        K3_HALF(xb, wb, xa, wa, 0, 0, false);
        K3_HALF(xa, wa, xb, wb, 0, 1, true);
        int cur = K3_SLOT;
#pragma unroll 1
        for (int t = 1; t < NT; ++t) {
            LBAR();
            K3_HALF(xb, wb, xa, wa, cur, 0, true);
            K3_HALF(xa, wa, xb, wb, cur, 1, true);
            cur = (cur == (K3_NS - 1) * K3_SLOT) ? 0 : cur + K3_SLOT;
        }
#pragma unroll
        for (int i = 0; i < 8; ++i) K3_MM(i, xb, wb);
        STAMP_AT(3);
        if (MODE == 0) {
            LAS f32x2* sb = (LAS f32x2*)(lds + K3_AUX);
            const srd_t srh = mk_srd(ws + WS_H, SEQ * DM * 2);
#pragma unroll
            for (int m = 0; m < 4; ++m) {
                const int mrow = rt * 128 + wm * 64 + m * 16;
                const f32x4 a = {bf_lo(res[m].x) + acc[m][0][0], bf_hi(res[m].x) + acc[m][0][1], bf_lo(res[m].y) + acc[m][0][2], bf_hi(res[m].y) + acc[m][0][3]};
                const f32x4 c = {bf_lo(res[m].z) + acc[m][1][0], bf_hi(res[m].z) + acc[m][1][1], bf_lo(res[m].w) + acc[m][1][2], bf_hi(res[m].w) + acc[m][1][3]};
                const u32x4 hv = pk8(a, c);
                st16(srh, FRAG(mrow >> 4, ct * 2 + wn, 32) + lane * 16, hv);
                float s = 0.f, q = 0.f;
#pragma unroll
                for (int j = 0; j < 4; ++j) { const unsigned d = hv[j]; const float u = bf_lo(d), v = bf_hi(d); s += u + v; q += u * u + v * v; }
                s += __shfl_xor(s, 16); s += __shfl_xor(s, 32); q += __shfl_xor(q, 16); q += __shfl_xor(q, 32);
                if (g == 0) sb[(wm * 64 + m * 16 + r) * 2 + wn] = (f32x2){s, q};
            }
            LBAR();
            if (tid < 128) { const f32x2 a = sb[tid * 2] + sb[tid * 2 + 1];
                __hip_atomic_store((gu64*)(ws + WS_ST) + ((size_t)(rt * 128 + tid) * 16 + ct), ((unsigned long long)__float_as_uint(a[1]) << 32) | __float_as_uint(a[0]), RLX_AGENT); }
        } else {
#pragma unroll
            for (int m = 0; m < 4; ++m) {
                const int mrow = rt * 128 + wm * 64 + m * 16;
                unsigned a0 = pk_bf16(bf_lo(res[m].x) + acc[m][0][0], bf_hi(res[m].x) + acc[m][0][1]), a1 = pk_bf16(bf_lo(res[m].y) + acc[m][0][2], bf_hi(res[m].y) + acc[m][0][3]);
                unsigned b0 = pk_bf16(bf_lo(res[m].z) + acc[m][1][0], bf_hi(res[m].z) + acc[m][1][1]), b1 = pk_bf16(bf_lo(res[m].w) + acc[m][1][2], bf_hi(res[m].w) + acc[m][1][3]);
                const auto s0 = __builtin_amdgcn_permlane16_swap(b0, a0, false, false), s1 = __builtin_amdgcn_permlane16_swap(b1, a1, false, false);
                const u32x4 ov = {s0[0], s1[0], s0[1], s1[1]};
                const int c8 = ((g & 1) ? 0 : 16) + (g >> 1) * 8;
                *(u32x4*)(P.out + (size_t)(mrow + r) * DM + ct * 64 + wn * 32 + c8) = ov;
            }
        }
    }
    STAMP_AT(4);
    if (MODE == 0) {
        WAITBAR(0);
        if (SYNC && tid == 0) flag_set(ws, FL_3 + rt * 16 + ct, tag);
        CODE_TOUCH_ASYNC(".Lcw_p3", ".Lcw_p4");
    }
}

constexpr int K5_SLOT = 32768, K5_NS = 4;
template <int KH>
__device__ __forceinline__ void p5_finish(const Params& P, const f32x4 (&acc)[4][4], unsigned tag, int b, int w, int lane, int rt, int ct STAMP_ARGS) {
    char* ws = P.ws;
    const int g = lane >> 4, r = lane & 15, wm = w >> 1, wn = w & 1;
    const srd_t sre = mk_srd(ws + WS_XCH, 8u << 20);
    const unsigned eo = (unsigned)(w * 8192) + lane * 16;
#pragma unroll
    for (int m = 0; m < 4; ++m)
#pragma unroll
        for (int n = 0; n < 2; ++n) st16(sre, (unsigned)b * 32768u + eo + (m * 2 + n) * 1024, __builtin_bit_cast(u32x4, acc[m][2 * (1 - KH) + n]));
    u32x4 res[4];
#pragma unroll
    for (int m = 0; m < 4; ++m) res[m] = ldc16(mk_srd(ws + WS_H, SEQ * DM * 2), FRAG(rt * 8 + wm * 4 + m, ct * 4 + wn * 2 + KH, 32) + lane * 16);
    WAITV(0);
    if (lane == 0) flag_set(ws, FL_5 + b * 4 + w, tag);
    flags_wait(ws, lane == 0 ? FL_5 + (b ^ 2) * 4 + w : -1, tag);
    STAMP_AT(4);
    u32x4 pv[4][2];
#pragma unroll
    for (int m = 0; m < 4; ++m)
#pragma unroll
        for (int n = 0; n < 2; ++n) pv[m][n] = ldc16(sre, (unsigned)(b ^ 2) * 32768u + eo + (m * 2 + n) * 1024);
#pragma unroll
    for (int m = 0; m < 4; ++m) {
        const int mrow = rt * 128 + wm * 64 + m * 16;
        const f32x4 t0 = acc[m][2 * KH] + __builtin_bit_cast(f32x4, pv[m][0]), t1 = acc[m][2 * KH + 1] + __builtin_bit_cast(f32x4, pv[m][1]);
        unsigned a0 = pk_bf16(bf_lo(res[m].x) + t0[0], bf_hi(res[m].x) + t0[1]), a1 = pk_bf16(bf_lo(res[m].y) + t0[2], bf_hi(res[m].y) + t0[3]);
        unsigned b0 = pk_bf16(bf_lo(res[m].z) + t1[0], bf_hi(res[m].z) + t1[1]), b1 = pk_bf16(bf_lo(res[m].w) + t1[2], bf_hi(res[m].w) + t1[3]);
        const auto s0 = __builtin_amdgcn_permlane16_swap(b0, a0, false, false), s1 = __builtin_amdgcn_permlane16_swap(b1, a1, false, false);
        const u32x4 ov = {s0[0], s1[0], s0[1], s1[1]};
        const int c8 = ((g & 1) ? 0 : 16) + (g >> 1) * 8;
        *(u32x4*)(P.out + (size_t)(mrow + r) * DM + ct * 128 + wn * 64 + KH * 32 + c8) = ov;
    }
}
template <bool SYNC>
__device__ __forceinline__ void phase5(LAS char* lds, const Params& P, unsigned tag STAMP_ARGS) {
    constexpr int KT = FF / 32, NT = FF / 128;
    const unsigned lds0 = (unsigned)(uintptr_t)lds;
    const int tid = threadIdx.x, lane = tid & 63, w = __builtin_amdgcn_readfirstlane(tid >> 6), g = lane >> 4, r = lane & 15;
    const int b = blockIdx.x, xcd = b & 7, loc = b >> 3;
    const int kh = (xcd >> 1) & 1, rt = (xcd >> 2) * 8 + (xcd & 1) * 4 + (loc & 3), ct = loc >> 2;
    char* ws = P.ws;
    if (w >= 4) {
        const int j = w - 4;
        const srd_t srx = mk_srd(ws + WS_ACT, SEQ * FF * 2), srw = mk_srd(P.wd, FF * DM * 2);
        constexpr unsigned XM = FRAG(1, 0, KT), XK = FRAG(0, 1, KT);
        const unsigned xv = lane * 16, xs0 = FRAG(rt * 8 + 2 * j, kh * (KT / 2), KT);
        const int wrow = lane >> 3, wch = (lane & 7) ^ (((wrow >> 1) & 3) << 1);
        const unsigned wv = (unsigned)wrow * 2048u + wch * 16, ws0 = (unsigned)ct * 256u + (unsigned)(kh * (FF / 2) + 16 * j) * 2048u;
        const unsigned ld0 = lds0 + j * 4096, ld1 = lds0 + 16384 + j * 2048;
        const int rot = 14 * (xcd & 1), fidx = lane < 16 ? FL_4 + (rt >> 1) * 32 + kh * 16 + lane : -1;
        unsigned long long rdy = SYNC ? 0ull : ~0ull;
#define K5_T(t) ((t) + rot >= NT ? (t) + rot - NT : (t) + rot)
#define K5_W(t, slot) do { const unsigned o_ = ws0 + K5_T(t) * (64 * 2048); dma16(ld1 + (slot), wv, srw, o_); dma16(ld1 + (slot) + 1024, wv, srw, o_ + 8 * 2048); \
        dma16(ld1 + (slot) + 8192, wv, srw, o_ + 128); dma16(ld1 + (slot) + 9216, wv, srw, o_ + 128 + 8 * 2048); } while (0)
#define K5_X(t, slot) do { const int tt_ = K5_T(t); wait_mask(ws, fidx, tag, (1ull << ((4 * tt_) / 7)) | (1ull << ((4 * tt_ + 3) / 7)), rdy); const unsigned o_ = xs0 + tt_ * (2 * XK); \
        dma16c(ld0 + (slot), xv, srx, o_); dma16c(ld0 + (slot) + 1024, xv, srx, o_ + XK); dma16c(ld0 + (slot) + 2048, xv, srx, o_ + XM); dma16c(ld0 + (slot) + 3072, xv, srx, o_ + XM + XK); } while (0)
        K5_W(0, 0); K5_W(1, K5_SLOT); K5_W(2, 2 * K5_SLOT); K5_W(3, 3 * K5_SLOT);
        K5_X(0, 0); K5_X(1, K5_SLOT); K5_X(2, 2 * K5_SLOT); K5_X(3, 3 * K5_SLOT);
        WAITBAR(12);
        WAITBAR(8);  K5_X(4, 0); K5_W(4, 0);
        WAITBAR(12); K5_X(5, K5_SLOT); K5_W(5, K5_SLOT);
        WAITBAR(16); K5_X(6, 2 * K5_SLOT); K5_W(6, 2 * K5_SLOT);
        int fr = 3 * K5_SLOT;
#pragma unroll 1
        for (int t = 4; t + 3 < NT; ++t) {
            WAITBAR(16);
            K5_X(t + 3, fr); K5_W(t + 3, fr);
            fr = (fr == 3 * K5_SLOT) ? 0 : fr + K5_SLOT;
        }
        WAITBAR(16); WAITBAR(8); WAITBAR(0);
    } else {
        const int wm = w >> 1, wn = w & 1;
        STAMP_AT(1);
        const int xrd = wm * 8192 + lane * 16;
        const int q4 = (lane >> 2) & 3, p4 = lane & 3, trow = 4 * g + q4;
        int wrd[4];
#pragma unroll
        for (int n = 0; n < 4; ++n) wrd[n] = 16384 + wn * 8192 + trow * 128 + ((n ^ ((trow >> 1) & 3)) * 32) + (p4 >> 1) * 16 + (p4 & 1) * 8;
        f32x4 acc[4][4];
#pragma unroll
        for (int m = 0; m < 4; ++m)
#pragma unroll
            for (int n = 0; n < 4; ++n) acc[m][n] = (f32x4){0.f, 0.f, 0.f, 0.f};
        bf16x8 xa[4], xb[4]; s16x4 wa[8], wb[8];
#define K5_RD(i, X, W, sl, s) do { if ((i) == 2) X[0] = ldf(lds + (sl) + xrd + (s) * 1024); else if ((i) >= 9) X[(i) - 8] = ldf(lds + (sl) + xrd + ((i) - 8) * 2048 + (s) * 1024); \
        else { const int k_ = (i) < 2 ? (i) : (i) - 1; W[k_] = trd(lds + (sl) + wrd[k_ >> 1] + (k_ & 1) * 2048 + (s) * 4096); } } while (0)
#define K5_MM(i, X, W) do { const int m_ = (i) >> 2, n_ = (i) & 3; acc[m_][n_] = MFMA16(cat8(W[2 * n_], W[2 * n_ + 1]), X[m_], acc[m_][n_]); } while (0)
#define K5_HALF(XM_, WM_, XR, WR, sl, s, DOMMA) do { _Pragma("unroll") for (int i_ = 0; i_ < 16; ++i_) { if (DOMMA) K5_MM(i_, XM_, WM_); if (i_ < 12) K5_RD(i_, XR, WR, sl, s); SCHED0(); } } while (0)
        LBAR();
        STAMP_AT(2);
        K5_HALF(xb, wb, xa, wa, 0, 0, false);
        K5_HALF(xa, wa, xb, wb, 0, 1, true);
        int cur = K5_SLOT;
#pragma unroll 1
        for (int t = 1; t < NT; ++t) {
            LBAR();
            K5_HALF(xb, wb, xa, wa, cur, 0, true);
            K5_HALF(xa, wa, xb, wb, cur, 1, true);
            cur = (cur == 3 * K5_SLOT) ? 0 : cur + K5_SLOT;
        }
#pragma unroll
        for (int i = 0; i < 16; ++i) K5_MM(i, xb, wb);
        STAMP_AT(3);
        if (kh) p5_finish<1>(P, acc, tag, b, w, lane, rt, ct STAMP_PASS); else p5_finish<0>(P, acc, tag, b, w, lane, rt, ct STAMP_PASS);
    }
}

constexpr int K4_SLOT = 30720, K4_NS = 4, K4_STRAW = K4_NS * K4_SLOT, K4_STAT = K4_STRAW + 32768, K4_CS = K4_STAT + 2048, K4_CSRAW = K4_CS + 1024;
struct K4Dma { srd_t srx, srg, sru; unsigned xv, gv, xs0, gs0, ld0, ld1; };
#define K4_DMA(i, t, slot) do { if ((i) < 2) dma16c(D.ld0 + (slot) + (i) * 1024, D.xv, D.srx, D.xs0 + (t) * FRAG(0, 1, 32) + (i) * FRAG(1, 0, 32)); \
        else dma16(D.ld1 + (slot) + ((i) - 2) * 7168, D.gv, (i) == 2 ? D.srg : D.sru, D.gs0 + (t) * (32 * FF * 2)); } while (0)
template <int NJ, int J0, bool HASW>
__device__ __forceinline__ void k4_body(LAS char* lds, const K4Dma& D, char* ws, int w, int lane, int rt, int ct STAMP_ARGS) {
    const int wm = w & 3, g = lane >> 4, r = lane & 15;
    constexpr int NP = HASW ? 4 : 2;
    const int xrd = wm * 4096 + lane * 16;
    const int q4 = (lane >> 2) & 3, p4 = lane & 3;
    const int wrd = 16384 + (4 * g + q4) * 224 + J0 * 32 + (p4 >> 1) * 16 + (p4 & 1) * 8;
    f32x4 ag[4][NJ], au[4][NJ];
#pragma unroll
    for (int m = 0; m < 4; ++m)
#pragma unroll
        for (int j = 0; j < NJ; ++j) { ag[m][j] = (f32x4){0.f, 0.f, 0.f, 0.f}; au[m][j] = (f32x4){0.f, 0.f, 0.f, 0.f}; }
    bf16x8 xa[4], xb[4]; s16x4 wgf[2 * NJ], wuf[2 * NJ];
#define K4_RW(i, W, sl, o) W[i] = trd(lds + (sl) + wrd + (o) + ((i) >> 1) * 32 + ((i) & 1) * (16 * 224))
#define K4_RX(i, X, sl) X[i] = ldf(lds + (sl) + xrd + (i) * 1024)
#define K4_MM(i, A, X, W) do { const int j_ = (i) >> 2, m_ = (i) & 3; A[m_][j_] = MFMA16(cat8(W[2 * j_], W[2 * j_ + 1]), X[m_], A[m_][j_]); } while (0)
#define K4_RD1(i, XC, sl) do { if ((i) == 0) K4_RX(0, XC, sl); else if ((i) < 3) K4_RW((i) - 1, wgf, sl, 0); else if ((i) < 6) K4_RX((i) - 2, XC, sl); else if ((i) < 4 + 2 * NJ) K4_RW((i) - 4, wgf, sl, 0); } while (0)
#define K4_H1(XC, XP, DOMMA, DODMA, t, sl, fsl) do { \
        _Pragma("unroll") for (int i_ = 0; i_ < 4 * NJ; ++i_) { \
            if (DOMMA) K4_MM(i_, au, XP, wuf); \
            K4_RD1(i_, XC, sl); \
            if (DODMA) { if (i_ == (NJ == 4 ? 13 : 11)) K4_DMA(0, t, fsl); } \
            SCHED0(); } } while (0)
#define K4_H2(XC, sl, DODMA, t, fsl) do { \
        _Pragma("unroll") for (int i_ = 0; i_ < 4 * NJ; ++i_) { \
            K4_MM(i_, ag, XC, wgf); \
            if (i_ < 2 * NJ) K4_RW(i_, wuf, sl, 7168); \
            if (DODMA) { if (NJ == 4) { if (i_ == 8) K4_DMA(1, t, fsl); if (i_ == 11 && NP == 4) K4_DMA(2, t, fsl); if (i_ == 14 && NP == 4) K4_DMA(3, t, fsl); } \
                         else { if (i_ == 6) K4_DMA(1, t, fsl); if (i_ == 8 && NP == 4) K4_DMA(2, t, fsl); if (i_ == 10 && NP == 4) K4_DMA(3, t, fsl); } } \
            SCHED0(); } } while (0)
    constexpr int NT = DM / 32;
    constexpr int NX = HASW ? 4 : 8;
    WAITBAR(4 + NX);
    STAMP_AT(2);
    K4_H1(xa, xb, false, true, 3, 0, 3 * K4_SLOT);
    K4_H2(xa, 0, true, 3, 3 * K4_SLOT);
    int cur = K4_SLOT, fr = 0;
#define K4_ADV() do { fr = (fr == (K4_NS - 1) * K4_SLOT) ? 0 : fr + K4_SLOT; cur = (cur == (K4_NS - 1) * K4_SLOT) ? 0 : cur + K4_SLOT; } while (0)
    WAITBAR(2 + NX + NP); K4_H1(xb, xa, true, true, 4, cur, fr); K4_H2(xb, cur, true, 4, fr); K4_ADV();
    WAITBAR(NX + 2 * NP); K4_H1(xa, xb, true, true, 5, cur, fr); K4_H2(xa, cur, true, 5, fr); K4_ADV();
    WAITBAR(2 * NP);
    {
        const int tid = w * 64 + lane, row = tid >> 1, hf = tid & 1;
        float s = 0.f, q = 0.f;
#pragma unroll
        for (int i = 0; i < 4; ++i) { const f32x4 v = *(const LAS f32x4*)(lds + K4_STRAW + row * 128 + hf * 64 + i * 16); s += v[0] + v[2]; q += v[1] + v[3]; }
        s += __shfl_xor(s, 1); q += __shfl_xor(q, 1);
        const float mean = s * (1.0f / DM);
        const float var = fmaxf(q * (1.0f / DM) - mean * mean, 0.f);
        if (hf == 0) ((LAS f32x2*)(lds + K4_STAT))[row] = (f32x2){mean, 1.0f / sqrtf(var + LN_EPS)};
        if (tid < 224) { const LAS float* cr = (const LAS float*)(lds + K4_CSRAW) + tid; ((LAS float*)(lds + K4_CS))[tid] = (cr[0] + cr[256]) + (cr[512] + cr[768]); }
        SCHED0();
    }
    K4_H1(xb, xa, true, true, 6, cur, fr); K4_H2(xb, cur, true, 6, fr); K4_ADV();
    int t = 4;
#pragma unroll 1
    for (; t + 4 < NT; t += 2) {
        WAITBAR(2 * NP); K4_H1(xa, xb, true, true, t + 3, cur, fr); K4_H2(xa, cur, true, t + 3, fr); K4_ADV();
        WAITBAR(2 * NP); K4_H1(xb, xa, true, true, t + 4, cur, fr); K4_H2(xb, cur, true, t + 4, fr); K4_ADV();
    }
    WAITBAR(2 * NP); K4_H1(xa, xb, true, true, t + 3, cur, fr); K4_H2(xa, cur, true, t + 3, fr); K4_ADV();
    WAITBAR(2 * NP); K4_H1(xb, xa, true, false, 0, cur, fr); K4_H2(xb, cur, false, 0, fr); K4_ADV();
    WAITBAR(NP);     K4_H1(xa, xb, true, false, 0, cur, fr); K4_H2(xa, cur, false, 0, fr); K4_ADV();
    WAITBAR(0);      K4_H1(xb, xa, true, false, 0, cur, fr); K4_H2(xb, cur, false, 0, fr);
#pragma unroll
    for (int i = 0; i < 4 * NJ; ++i) K4_MM(i, au, xb, wuf);
    STAMP_AT(3);
    const LAS f32x2* st = (const LAS f32x2*)(lds + K4_STAT);
    const LAS float* cs = (const LAS float*)(lds + K4_CS);
    f32x2 ms[4];
#pragma unroll
    for (int m = 0; m < 4; ++m) ms[m] = st[wm * 64 + m * 16 + r];
    const srd_t sra = mk_srd(ws + WS_ACT, SEQ * FF * 2);
    const unsigned ap0 = FRAG(rt * 16 + wm * 4, 0, 112) + lane * 16;
    const int ft0 = ct * 7 + J0;
    constexpr int LONE = (NJ == 3) ? (J0 == 4 ? 2 : 0) : -1;
    u32x2 keep[4];
#pragma unroll
    for (int j = 0; j < NJ; ++j) {
        const f32x4 cg = *(const LAS f32x4*)(cs + (J0 + j) * 16 + 4 * g), cu = *(const LAS f32x4*)(cs + 112 + (J0 + j) * 16 + 4 * g);
        const bool second = (LONE == 0) ? (j == 2) : ((j & 1) == 1);
#pragma unroll
        for (int m = 0; m < 4; ++m) {
            const float c1 = -LOG2E * ms[m].y;
            const f32x4 gz = (ag[m][j] - cg * ms[m].x) * c1;
            const f32x4 uu = (au[m][j] - cu * ms[m].x) * ms[m].y;
            float a[4];
#pragma unroll
            for (int e = 0; e < 4; ++e) a[e] = uu[e] * __builtin_amdgcn_rcpf(1.0f + __builtin_amdgcn_exp2f(gz[e]));
            u32x2 ov; ov.x = pk_bf16(a[0], a[1]); ov.y = pk_bf16(a[2], a[3]);
            const int ft = ft0 + j;
            if (j == LONE) st8(sra, ap0 + FRAG(m, ft >> 1, 112) + (ft & 1) * 8, ov);
            else if (!second) keep[m] = ov;
            else st16(sra, ap0 + FRAG(m, ft >> 1, 112), (u32x4){keep[m].x, keep[m].y, ov.x, ov.y});
        }
        SCHED0();
    }
}

template <bool SYNC>
__device__ __forceinline__ void phase4(LAS char* lds, const Params& P, unsigned tag STAMP_ARGS) {
    const unsigned lds0 = (unsigned)(uintptr_t)lds;
    const int tid = threadIdx.x, lane = tid & 63, w = __builtin_amdgcn_readfirstlane(tid >> 6);
    const int b = blockIdx.x, xcd = b & 7, loc = b >> 3;
    const int rt = (xcd >> 2) * 4 + (loc & 3), ct = (xcd & 3) * 8 + (loc >> 2);
    char* ws = P.ws;
    K4Dma D;
    D.srx = mk_srd(ws + WS_H, SEQ * DM * 2); D.srg = mk_srd(P.wg, DM * FF * 2); D.sru = mk_srd(P.wu, DM * FF * 2);
    D.xv = lane * 16; D.xs0 = FRAG(rt * 16 + w * 2, 0, 32);
    { const int ci = (w < 7 ? w : 6) * 64 + lane; const int row = ci / 14, c = ci % 14; D.gv = (unsigned)row * (FF * 2) + c * 16; }
    D.gs0 = (unsigned)ct * 224u; D.ld0 = lds0 + w * 2048; D.ld1 = lds0 + 16384 + w * 1024;
    if (w < 7) {
#pragma unroll
        for (int t = 0; t < 3; ++t) { K4_DMA(2, t, t * K4_SLOT); K4_DMA(3, t, t * K4_SLOT); }
    }
    if (SYNC) {
        if (w == 7) {
            int idx = -1;
            if (lane < 32) idx = FL_3 + rt * 32 + lane;
            else if (lane < 48) { const int k = lane - 32, gsel = k & 3, rq = k >> 2;
                const int c0 = (ct * 112 + ((gsel & 1) ? 111 : 0)) >> 7, bb = rq * 56 + (gsel >> 1) * 28 + c0;
                idx = FL_2 + (bb >> 5) * 32 + (bb & 7) * 4 + ((bb >> 3) & 3); }
            flags_wait(ws, idx, tag);
        }
        LBAR();
    }
    STAMP_AT(1);
#pragma unroll
    for (int t = 0; t < 3; ++t) { K4_DMA(0, t, t * K4_SLOT); K4_DMA(1, t, t * K4_SLOT); }
    {
        const srd_t srs = mk_srd(ws + WS_ST, SEQ * 128);
#pragma unroll
        for (int i = 0; i < 4; ++i) dma16c(lds0 + K4_STRAW + w * 4096 + i * 1024, lane * 16, srs, (unsigned)(rt * 256 + w * 32) * 128u + i * 1024);
        if (w == 7) {
            const srd_t src = mk_srd(ws + WS_CS, 4 * 7168 * 4);
            const unsigned cv = lane < 28 ? (unsigned)(ct * 112 + 4 * lane) * 4u : lane < 56 ? (unsigned)(3584 + ct * 112 + 4 * (lane - 28)) * 4u : 0x7ff00000u;
#pragma unroll
            for (int i = 0; i < 4; ++i) dma16c(lds0 + K4_CSRAW + i * 1024, cv, src, i * 7168 * 4);
        }
    }
    if ((ct & 1) == 0) {
        if (w < 4) k4_body<4, 0, true>(lds, D, ws, w, lane, rt, ct STAMP_PASS);
        else if (w < 7) k4_body<3, 4, true>(lds, D, ws, w, lane, rt, ct STAMP_PASS);
        else k4_body<3, 4, false>(lds, D, ws, w, lane, rt, ct STAMP_PASS);
    } else {
        if (w < 4) k4_body<3, 0, true>(lds, D, ws, w, lane, rt, ct STAMP_PASS);
        else if (w < 7) k4_body<4, 3, true>(lds, D, ws, w, lane, rt, ct STAMP_PASS);
        else k4_body<4, 3, false>(lds, D, ws, w, lane, rt, ct STAMP_PASS);
    }
    STAMP_AT(4);
    WAITBAR(0);
    if (SYNC && tid == 0) flag_set(ws, FL_4 + rt * 32 + ct, tag);
    CODE_TOUCH_ASYNC(".Lcw_p4", ".Lcw_p5");
}

constexpr int LDS_BYTES = 160 * 1024;
__global__ __launch_bounds__(512, 2) void mega(Params P) {
    extern __shared__ __attribute__((aligned(16))) char smem_g[];
    LAS char* lds = (LAS char*)smem_g;
    char* ws = P.ws;
    const unsigned tag = __hip_atomic_load((gu32*)(ws + WS_FLG) + FL_EPOCH, RLX_AGENT) + 1u;
    LITE_BEGIN()
    asm volatile(".Lcw_p0:");
    { STAMP_DECL STAMP_AT(0); phase1<true>(lds, P, tag STAMP_PASS); STAMP_OUT(1); }
    asm volatile(".Lcw_p1:");
    { STAMP_DECL STAMP_AT(0); phase2<true>(lds, P, tag STAMP_PASS); STAMP_OUT(2); }
    asm volatile(".Lcw_p2:");
    { STAMP_DECL STAMP_AT(0); phase35<0, true>(lds, P, tag STAMP_PASS); STAMP_OUT(3); }
    asm volatile(".Lcw_p3:");
    { STAMP_DECL STAMP_AT(0); phase4<true>(lds, P, tag STAMP_PASS); STAMP_OUT(4); }
    asm volatile(".Lcw_p4:");
    { STAMP_DECL STAMP_AT(0); phase5<true>(lds, P, tag STAMP_PASS); STAMP_OUT(5); }
    asm volatile(".Lcw_p5:");
    LITE_END();
    if (blockIdx.x == 0 && threadIdx.x == 0) __hip_atomic_store((gu32*)(ws + WS_FLG) + FL_EPOCH, tag, RLX_AGENT);
}

extern "C" void kernel_launch(void* const* d_in, const int* in_sizes, int n_in, void* d_out, int out_size, void* d_ws, size_t ws_size, hipStream_t stream) {
    Params P;
    P.x = (const u16*)d_in[0]; P.wqkv = (const u16*)d_in[1]; P.wo = (const u16*)d_in[2];
    P.wg = (const u16*)d_in[4]; P.wu = (const u16*)d_in[5]; P.wd = (const u16*)d_in[6];
    P.out = (u16*)d_out; P.ws = (char*)d_ws;
    (void)hipFuncSetAttribute((const void*)mega, hipFuncAttributeMaxDynamicSharedMemorySize, LDS_BYTES);
    mega<<<256, 512, LDS_BYTES, stream>>>(P);
}
```

```cpp
#include <hip/hip_runtime.h>
#include <hip/hip_bf16.h>
#include <stdint.h>

#define LAS __attribute__((address_space(3)))
typedef unsigned short u16;
typedef short bf16x8 __attribute__((ext_vector_type(8)));
typedef short s16x4 __attribute__((ext_vector_type(4)));
typedef float f32x2 __attribute__((ext_vector_type(2)));
typedef float f32x4 __attribute__((ext_vector_type(4)));
typedef float f32x16 __attribute__((ext_vector_type(16)));
typedef unsigned u32x2 __attribute__((ext_vector_type(2)));
typedef unsigned u32x4 __attribute__((ext_vector_type(4)));
typedef __bf16 bf16x2_t __attribute__((ext_vector_type(2)));
typedef __amdgpu_buffer_rsrc_t srd_t;

constexpr int SEQ = 2048, DM = 1024, NH = 32, FF = 3584;
constexpr float LN_EPS = 1e-5f;
constexpr float QSCALE = 0.17677669529663687f * 1.4426950408889634f;
constexpr float LOG2E = 1.4426950408889634f;

constexpr size_t WS_Q = 0;
constexpr size_t WS_K = WS_Q + (4u << 20);
constexpr size_t WS_V = WS_K + (4u << 20);
constexpr size_t WS_O = WS_V + (4u << 20);
constexpr size_t WS_H = WS_O + (4u << 20);
constexpr size_t WS_ACT = WS_H + (4u << 20);
constexpr size_t WS_ST = WS_ACT + (size_t)SEQ * FF * 2;
constexpr size_t WS_CS = WS_ST + (size_t)SEQ * 16 * 8;
constexpr size_t WS_S1 = WS_CS + 4 * 7168 * 4;
constexpr size_t WS_C1 = WS_S1 + SEQ * 8;
constexpr size_t WS_FLG = WS_C1 + 16 * 3072 * 4;
constexpr size_t WS_XCH = WS_FLG + 16384;
constexpr size_t WS_END = WS_XCH + (8u << 20);
constexpr int FL_EPOCH = 0, FL_ERR = 32, FL_0 = 64, FL_1 = FL_0 + 256, FL_2 = FL_1 + 256, FL_3 = FL_2 + 256, FL_4 = FL_3 + 256, FL_5 = FL_4 + 256;

#define FRAG(mt, kt, KT) (((unsigned)(kt) * 128u + (unsigned)(mt)) * 1024u)
#define ST_AUX 16

__device__ __forceinline__ unsigned pk_bf16(float lo, float hi) { f32x2 v = {lo, hi}; bf16x2_t b = __builtin_convertvector(v, bf16x2_t); return __builtin_bit_cast(unsigned, b); }
__device__ __forceinline__ float bf_lo(unsigned u) { return __uint_as_float(u << 16); }
__device__ __forceinline__ float bf_hi(unsigned u) { return __uint_as_float(u & 0xffff0000u); }
__device__ __forceinline__ s16x4 trd(LAS const char* p) { return __builtin_bit_cast(s16x4, __builtin_amdgcn_ds_read_tr16_b64_v4i16((LAS s16x4*)p)); }
__device__ __forceinline__ bf16x8 cat8(s16x4 a, s16x4 b) { return (bf16x8){a[0], a[1], a[2], a[3], b[0], b[1], b[2], b[3]}; }
__device__ __forceinline__ bf16x8 ldf(LAS const char* p) { return *(LAS const bf16x8*)p; }
__device__ __forceinline__ srd_t mk_srd(const void* p, unsigned bytes) { return __builtin_amdgcn_make_buffer_rsrc((void*)p, (short)0, (int)bytes, 0x00020000); }
__device__ __forceinline__ void dma16(unsigned ldsa, unsigned voff, srd_t srd, unsigned soff) {
    asm volatile("s_mov_b32 m0, %0\n\ts_nop 0\n\tbuffer_load_dwordx4 %1, %2, %3 offen lds" :: "s"(ldsa), "v"(voff), "s"(srd), "s"(soff));
}
__device__ __forceinline__ void dma16c(unsigned ldsa, unsigned voff, srd_t srd, unsigned soff) {
    asm volatile("s_mov_b32 m0, %0\n\ts_nop 0\n\tbuffer_load_dwordx4 %1, %2, %3 offen sc1 lds" :: "s"(ldsa), "v"(voff), "s"(srd), "s"(soff));
}
__device__ __forceinline__ void st8(srd_t srd, unsigned off, u32x2 v) { __builtin_amdgcn_raw_buffer_store_b64(v, srd, off, 0, ST_AUX); }
__device__ __forceinline__ void st16(srd_t srd, unsigned off, u32x4 v) { __builtin_amdgcn_raw_buffer_store_b128(v, srd, off, 0, ST_AUX); }
__device__ __forceinline__ u32x4 pk8(const f32x4 a, const f32x4 b) { u32x4 o; o.x = pk_bf16(a[0], a[1]); o.y = pk_bf16(a[2], a[3]); o.z = pk_bf16(b[0], b[1]); o.w = pk_bf16(b[2], b[3]); return o; }
#define MFMA16(a, b, c) __builtin_amdgcn_mfma_f32_16x16x32_bf16(a, b, c, 0, 0, 0)
#define MFMA32(a, b, c) __builtin_amdgcn_mfma_f32_32x32x16_bf16(a, b, c, 0, 0, 0)
#define WAITVL(n) asm volatile("s_waitcnt vmcnt(%0) lgkmcnt(0)" :: "n"(n) : "memory")
#define WAITBAR(n) asm volatile("s_waitcnt vmcnt(%0) lgkmcnt(0)\n\ts_barrier" :: "n"(n) : "memory")
#define LBAR() asm volatile("s_waitcnt lgkmcnt(0)\n\ts_barrier" ::: "memory")
#define SCHED0() __builtin_amdgcn_sched_barrier(0)
#define CODE_TOUCH(A, B, t0, nthr) do { unsigned long long pc_; int lo_, hi_; unsigned dm_ = 0u; \
        asm volatile("s_getpc_b64 %0\n.Lct%=:\n\ts_mov_b32 %1, " A "-.Lct%=\n\ts_mov_b32 %2, " B "-.Lct%=" : "=&s"(pc_), "=&s"(lo_), "=&s"(hi_)); \
        for (int o_ = lo_ + ((int)threadIdx.x - (t0)) * 128; o_ < hi_; o_ += (nthr) * 128) asm volatile("global_load_dword %0, %1, off" : "+v"(dm_) : "v"((const char*)pc_ + o_)); \
        asm volatile("s_waitcnt vmcnt(0)" : "+v"(dm_) :: "memory");        \
        } while (0)
#define WAITV(n) asm volatile("s_waitcnt vmcnt(%0)" :: "n"(n) : "memory")
#define RLX_AGENT __ATOMIC_RELAXED, __HIP_MEMORY_SCOPE_AGENT
typedef __attribute__((address_space(1))) unsigned gu32;
typedef __attribute__((address_space(1))) unsigned long long gu64;
struct Params { const u16 *x, *wqkv, *wo, *wg, *wu, *wd; u16* out; char* ws; };
__device__ __forceinline__ void flag_set(char* ws, int idx, unsigned tag) { __hip_atomic_store((gu32*)(ws + WS_FLG) + idx, tag, RLX_AGENT); }
__device__ __forceinline__ void flags_wait(char* ws, int idx, unsigned tag) {
    gu32* fl = (gu32*)(ws + WS_FLG);
    const unsigned long long t0 = __builtin_amdgcn_s_memrealtime();
    for (unsigned spins = 1;; ++spins) {
        const unsigned v = idx >= 0 ? __hip_atomic_load(fl + idx, RLX_AGENT) : tag;
        if (__all(v == tag)) break;
        if ((spins & 63u) == 0u) {
            if (__any(__hip_atomic_load(fl + FL_ERR, RLX_AGENT) == tag)) break;
            if (__builtin_amdgcn_s_memrealtime() - t0 > 2000000ull) { __hip_atomic_store(fl + FL_ERR, tag, RLX_AGENT); break; }
        }
        __builtin_amdgcn_s_sleep(1);
    }
    asm volatile("" ::: "memory");
}
template <int CTRL, int RM> __device__ __forceinline__ float dpp_add(float v) { return v + __int_as_float(__builtin_amdgcn_update_dpp(0, __float_as_int(v), CTRL, RM, 0xf, true)); }
__device__ __forceinline__ float sum32(float v) { v = dpp_add<0x111, 0xf>(v); v = dpp_add<0x112, 0xf>(v); v = dpp_add<0x114, 0xf>(v); v = dpp_add<0x118, 0xf>(v); return dpp_add<0x142, 0xa>(v); }
__device__ __forceinline__ void wait_mask(char* ws, int idx, unsigned tag, unsigned long long need, unsigned long long& rdy) {
    if ((rdy & need) == need) return;
    gu32* fl = (gu32*)(ws + WS_FLG);
    const unsigned long long t0 = __builtin_amdgcn_s_memrealtime();
    for (unsigned spins = 1;; ++spins) {
        const unsigned v = idx >= 0 ? __hip_atomic_load(fl + idx, RLX_AGENT) : ~tag;
        rdy = __ballot(v == tag);
        if ((rdy & need) == need) break;
        if ((spins & 63u) == 0u) {
            if (__any(__hip_atomic_load(fl + FL_ERR, RLX_AGENT) == tag)) break;
            if (__builtin_amdgcn_s_memrealtime() - t0 > 2000000ull) { __hip_atomic_store(fl + FL_ERR, tag, RLX_AGENT); break; }
        }
        __builtin_amdgcn_s_sleep(1);
    }
    asm volatile("" ::: "memory");
}
__device__ __forceinline__ u32x4 ldc16(srd_t srd, unsigned off) { return __builtin_amdgcn_raw_buffer_load_b128(srd, off, 0, 16); }
__device__ __forceinline__ float ldc4(srd_t srd, unsigned off) { return __uint_as_float(__builtin_amdgcn_raw_buffer_load_b32(srd, off, 0, 16)); }

constexpr size_t WS_LITE = (size_t)80 << 20;
#define LITE_BEGIN()
#define LITE_END()
#define STAMP_DECL
#define STAMP_AT(i)
#define STAMP_OUT(kid)
#define STAMP_ARGS
#define STAMP_PASS

constexpr int K1_SLOT = 40960, K1_NS = 4, K1_AUX = 0;
template <bool SYNC>
__device__ __forceinline__ void phase1(LAS char* lds, const Params& P, unsigned tag STAMP_ARGS) {
    const unsigned lds0 = (unsigned)(uintptr_t)lds;
    const int tid = threadIdx.x, lane = tid & 63, w = __builtin_amdgcn_readfirstlane(tid >> 6), g = lane >> 4, r = lane & 15;
    const int b = blockIdx.x, xcd = b & 7, loc = b >> 3;
        const int p = (xcd & 3) * 4 + (loc & 3), rt = (xcd >> 2) * 8 + (loc >> 2);
    char* ws = P.ws;
    constexpr int NT = DM / 64;
    LAS f32x2* stat = (LAS f32x2*)(lds + K1_AUX);
    LAS float* csl = (LAS float*)(lds + K1_AUX + 1024);
    if (__builtin_expect(w >= 4, 1)) {
        const srd_t srx = mk_srd(P.x, SEQ * DM * 2), srw = mk_srd(P.wqkv, DM * 3 * DM * 2);
        const int j = w - 4, rr = lane >> 3, c8 = lane & 7;
        unsigned xv[2], wv[2];
#pragma unroll
        for (int i = 0; i < 2; ++i) {
            xv[i] = rr * 2048 + ((c8 ^ (4 * i + (rr >> 1))) * 16);
            const int f = ((rr >> 1) & 1) | (i << 1);
            wv[i] = rr * 6144 + (((c8 >> 1) ^ f) * 32) + (c8 & 1) * 16;
        }
        const unsigned xs0 = (unsigned)(rt * 128 + 32 * j) * 2048u, ws0 = (unsigned)p * 128u + (unsigned)(16 * j) * 6144u;
        const unsigned xl0 = lds0 + j * 4096, wl0 = lds0 + 16384 + j * 2048;
#define K1_DMA(i, t, slot) do { if ((i) < 4) dma16(xl0 + (slot) + (i) * 1024, xv[(i) & 1], srx, xs0 + (i) * (8 * 2048) + (((t) + rt) & 15) * 128); \
        else dma16(wl0 + (slot) + (((i) - 4) >> 1) * 8192 + (((i) - 4) & 1) * 1024, wv[((i) - 4) & 1], srw, ws0 + (((i) - 4) >> 1) * 2048 + (((i) - 4) & 1) * (8 * 6144) + (((t) + rt) & 15) * (64 * 6144)); } while (0)
#define K1_STAGE(t, slot) do { _Pragma("unroll") for (int i_ = 0; i_ < 10; ++i_) K1_DMA(i_, t, slot); } while (0)
        const int srow = 8 * b + 2 * j + (lane >> 5);
        u32x4 xr[4];
        { const unsigned xo = (unsigned)srow * 2048u + (lane & 31) * 16;
#pragma unroll
          for (int k = 0; k < 4; ++k) asm volatile("buffer_load_dwordx4 %0, %1, %2, 0 offen offset:%3" : "=v"(xr[k]) : "v"(xo), "s"(srx), "n"(k * 512)); }
        K1_STAGE(0, 0); K1_STAGE(1, K1_SLOT); K1_STAGE(2, 2 * K1_SLOT);
        WAITBAR(20);
        asm volatile("" : "+v"(xr[0]), "+v"(xr[1]), "+v"(xr[2]), "+v"(xr[3]));
        K1_STAGE(3, 3 * K1_SLOT);
        {
            const unsigned x00 = xr[0][0];
            const float pv = bf_lo(lane < 32 ? __builtin_amdgcn_readlane(x00, 0) : __builtin_amdgcn_readlane(x00, 32));
            float s = 0.f, q = 0.f;
#pragma unroll
            for (int k = 0; k < 4; ++k)
#pragma unroll
                for (int e = 0; e < 4; ++e) { const unsigned d = xr[k][e]; const float u = bf_lo(d) - pv, v = bf_hi(d) - pv; s += u + v; q += u * u + v * v; }
            s = sum32(s); q = sum32(q);
            const float dm = s * (1.0f / DM), var = fmaxf(q * (1.0f / DM) - dm * dm, 0.f);
            const u32x2 sv = {__float_as_uint(pv + dm), __float_as_uint(1.0f / sqrtf(var + LN_EPS))};
            __builtin_amdgcn_raw_buffer_store_b64(sv, mk_srd(ws + WS_S1, SEQ * 8), (lane & 31) == 31 ? (unsigned)srow * 8u : 0x7ff00000u, 0, 16);
        }
        WAITBAR(21); K1_STAGE(4, 0);
        WAITBAR(21); K1_STAGE(5, K1_SLOT);
        WAITBAR(21); K1_STAGE(6, 2 * K1_SLOT);
        WAITBAR(20);
        if (SYNC && tid == 256) flag_set(ws, FL_0 + b, tag);
        K1_STAGE(7, 3 * K1_SLOT);
        WAITBAR(20); K1_STAGE(8, 0);
        WAITBAR(20); K1_STAGE(9, K1_SLOT);
        WAITBAR(20); K1_STAGE(10, 2 * K1_SLOT);
        WAITBAR(20);
        const int fidx = lane < 16 ? FL_0 + 16 * rt + lane : lane < 32 ? FL_0 + (((lane - 16) & 7) * 4 + (p & 3)) * 8 + ((lane - 16) >> 3) * 4 + (p >> 2) : -1;
        unsigned fseen;
        asm volatile("global_load_dword %0, %1, off sc1" : "=v"(fseen) : "v"((const char*)(ws + WS_FLG) + (fidx >= 0 ? fidx : FL_0 + b) * 4));
        K1_STAGE(11, 3 * K1_SLOT);
        WAITBAR(21); K1_STAGE(12, 0);
        WAITBAR(21); K1_STAGE(13, K1_SLOT);
        WAITBAR(20);
        asm volatile("" : "+v"(fseen));
        K1_STAGE(14, 2 * K1_SLOT);
        if (SYNC && !__all(fseen == tag)) flags_wait(ws, fidx, tag);
        u32x4 gs, gp[16];
        const int tc = 48 * j + 4 * lane;
        { const srd_t srs = mk_srd(ws + WS_S1, SEQ * 8), src1 = mk_srd(ws + WS_C1, 16 * 3072 * 4);
          const unsigned so = j == 0 ? (unsigned)(rt * 128) * 8u + lane * 16 : 0x7ff00000u, po = lane < 12 ? (unsigned)((tc >> 6) * 1024 + p * 64 + (tc & 63)) * 4u : 0x7ff00000u;
          asm volatile("buffer_load_dwordx4 %0, %1, %2, 0 offen sc1" : "=v"(gs) : "v"(so), "s"(srs));
#pragma unroll
          for (int i = 0; i < 16; ++i) asm volatile("buffer_load_dwordx4 %0, %1, %2, %3 offen sc1" : "=v"(gp[i]) : "v"(po), "s"(src1), "s"(i * 3072 * 4)); }
        WAITBAR(37); K1_STAGE(15, 3 * K1_SLOT);
        WAITBAR(37);
        WAITBAR(10);
        asm volatile("" : "+v"(gs), "+v"(gp[0]), "+v"(gp[1]), "+v"(gp[2]), "+v"(gp[3]), "+v"(gp[4]), "+v"(gp[5]), "+v"(gp[6]), "+v"(gp[7]));
        asm volatile("" : "+v"(gp[8]), "+v"(gp[9]), "+v"(gp[10]), "+v"(gp[11]), "+v"(gp[12]), "+v"(gp[13]), "+v"(gp[14]), "+v"(gp[15]));
        {
            f32x4 v = {0.f, 0.f, 0.f, 0.f};
#pragma unroll
            for (int i = 0; i < 16; ++i) v += __builtin_bit_cast(f32x4, gp[i]);
            if (lane < 12) *(LAS f32x4*)(csl + tc) = v;
            if (j == 0) *(LAS u32x4*)(lds + K1_AUX + lane * 16) = gs;
        }
        WAITBAR(0);
    } else {
        const int wr = w >> 1, wc = w & 1;
#define CWV 2
        CODE_TOUCH(".Lcw_p0", ".Lcw_p2", 0, 256); CODE_TOUCH(".Lcw_p5", ".Lcw_end", 0, 256);
        int xrd[2], wrd[2];
        { const int q4 = (lane >> 2) & 3, p4 = lane & 3, trow = 8 * g + q4, f = ((trow >> 1) & 1) | (((trow >> 3) & 1) << 1);
#pragma unroll
          for (int s = 0; s < 2; ++s) { xrd[s] = (64 * wr + r) * 128 + (((4 * s + g) ^ ((r >> 1) & 7)) * 16); wrd[s] = 16384 + trow * 128 + (((2 * wc + s) ^ f) * 32) + (p4 >> 1) * 16 + (p4 & 1) * 8; } }
        f32x4 acc[4][6];
#pragma unroll
        for (int m = 0; m < 4; ++m)
#pragma unroll
            for (int n = 0; n < 6; ++n) acc[m][n] = (f32x4){0.f, 0.f, 0.f, 0.f};
        bf16x8 xa[4], xb[4]; s16x4 wa[12], wb[12];
#define K1_RW(i, W, sl, s) W[i] = trd(lds + (sl) + wrd[((i) >> 1) & 1] + ((i) >> 2) * 8192 + (s) * 4096 + ((i) & 1) * 512)
#define K1_RX(i, X, sl, s) X[i] = ldf(lds + (sl) + xrd[s] + (i) * 2048)
#define K1_RD(i, X, W, sl, s) do { if ((i) == 2) K1_RX(0, X, sl, s); else if ((i) == 7) K1_RX(1, X, sl, s); else if ((i) == 12) K1_RX(2, X, sl, s); else if ((i) == 15) K1_RX(3, X, sl, s); \
        else K1_RW((i) < 2 ? (i) : (i) < 7 ? (i) - 1 : (i) < 12 ? (i) - 2 : (i) - 3, W, sl, s); } while (0)
#define K1_MM(i, X, W) do { const int m_ = (i) / 6, n_ = (i) % 6; const bf16x8 wf_ = cat8(W[2 * n_], W[2 * n_ + 1]); \
        acc[m_][n_] = (n_ < 4) ? MFMA16(wf_, X[m_], acc[m_][n_]) : MFMA16(X[m_], wf_, acc[m_][n_]); } while (0)
#define K1_HALF(XM, WM, XR, WR, sl, s, DOMMA) do { \
        _Pragma("unroll") for (int i_ = 0; i_ < 24; ++i_) { \
            if (DOMMA) K1_MM(i_, XM, WM); \
            if (i_ < 16) K1_RD(i_, XR, WR, sl, s); \
            SCHED0(); } } while (0)
        LBAR();
        STAMP_AT(2);
        K1_HALF(xb, wb, xa, wa, 0, 0, false);
        K1_HALF(xa, wa, xb, wb, 0, 1, true);
        if (wr == 0) {
            bf16x8 one8;
#pragma unroll
            for (int e = 0; e < 8; ++e) one8[e] = (short)0x3f80;
            const srd_t src1 = mk_srd(ws + WS_C1, 16 * 3072 * 4);
#pragma unroll
            for (int n = 0; n < 6; ++n) {
                f32x4 c = MFMA16(cat8(wa[2 * n], wa[2 * n + 1]), one8, ((f32x4){0.f, 0.f, 0.f, 0.f}));
                c = MFMA16(cat8(wb[2 * n], wb[2 * n + 1]), one8, c);
                __builtin_amdgcn_raw_buffer_store_b128(__builtin_bit_cast(u32x4, c), src1, r == 0 ? (unsigned)(rt * 3072 + (n >> 1) * 1024 + p * 64 + wc * 32 + (n & 1) * 16 + 4 * g) * 4u : 0x7ff00000u, 0, 16);
            }
        }
        int cur = K1_SLOT;
        LBAR();
        K1_HALF(xb, wb, xa, wa, cur, 0, true);
        K1_HALF(xa, wa, xb, wb, cur, 1, true);
        cur = 2 * K1_SLOT;
        LBAR();
        K1_HALF(xb, wb, xa, wa, cur, 0, true);
        K1_HALF(xa, wa, xb, wb, cur, 1, true);
        cur = 3 * K1_SLOT;
        WAITBAR(0);
        K1_HALF(xb, wb, xa, wa, cur, 0, true);
        K1_HALF(xa, wa, xb, wb, cur, 1, true);
        cur = 0;
#pragma unroll 1
        for (int t = 4; t < NT; ++t) {
            LBAR();
            K1_HALF(xb, wb, xa, wa, cur, 0, true);
            K1_HALF(xa, wa, xb, wb, cur, 1, true);
            cur = (cur == 3 * K1_SLOT) ? 0 : cur + K1_SLOT;
        }
#pragma unroll
        for (int i = 0; i < 24; ++i) K1_MM(i, xb, wb);
        STAMP_AT(3);
        const int head = 2 * p + wc;
        const srd_t srq = mk_srd(ws + WS_Q, 12u << 20);
        f32x4 cq[4]; float cv[2];
#pragma unroll
        for (int n = 0; n < 4; ++n) cq[n] = *(LAS const f32x4*)(csl + (n >> 1) * 64 + wc * 32 + (n & 1) * 16 + 4 * g);
#pragma unroll
        for (int n = 0; n < 2; ++n) cv[n] = csl[128 + wc * 32 + n * 16 + r];
        const int rho = 16 * (r >> 3) + 4 * ((r >> 2) & 1) + (r & 3);
        f32x4 vv[2][2];
#pragma unroll
        for (int m = 0; m < 4; ++m) {
            const int lrow = wr * 64 + m * 16, row0 = rt * 128 + lrow;
            const unsigned blk = ((unsigned)head * 64 + (row0 >> 5)) * 2048u + (g >> 1) * 1024 + (g & 1) * 512;
            const f32x2 st = stat[lrow + r];
            const float qs = st.y * QSCALE;
            st16(srq, (4u << 20) + blk + (rho + (m & 1) * 8) * 16, pk8((acc[m][2] - st.x * cq[2]) * st.y, (acc[m][3] - st.x * cq[3]) * st.y));
            const f32x4 s01 = *(LAS const f32x4*)(stat + lrow + 4 * g), s23 = *(LAS const f32x4*)(stat + lrow + 4 * g + 2);
            const f32x4 mean4 = {s01[0], s01[2], s23[0], s23[2]}, rstd4 = {s01[1], s01[3], s23[1], s23[3]};
#pragma unroll
            for (int n = 0; n < 2; ++n) vv[m & 1][n] = (acc[m][4 + n] - mean4 * cv[n]) * rstd4;
            if (m & 1) {
#pragma unroll
                for (int n = 0; n < 2; ++n) st16(srq, (8u << 20) + blk + (16 * n + r) * 16, pk8(vv[0][n], vv[1][n]));
            }
            st16(srq, blk + ((m & 1) * 16 + r) * 16, pk8((acc[m][0] - st.x * cq[0]) * qs, (acc[m][1] - st.x * cq[1]) * qs));
        }
        asm volatile(".Lcw_end:");
    }
    CODE_TOUCH(".Lcw_p1", ".Lcw_p2", 0, 512);
    STAMP_AT(4);
    WAITBAR(0);
    if (SYNC && tid == 0) flag_set(ws, FL_1 + p * 16 + rt, tag);
}

constexpr int K2_SLOT = 32768, K2_SIDE = 3 * K2_SLOT;
struct K2Side { srd_t src, srp, sro; unsigned cv, cs0, ps0; };
template <int KH>
__device__ __forceinline__ void attn_fast(LAS char* lds, srd_t srk, srd_t srv, unsigned hbase, const K2Side& S, int w, int lane, const bf16x8 (&qf)[2][2], f32x16 (&o)[2], f32x4 (&lacc)[2], f32x4& cs) {
    const int tl = w & 3;
    const unsigned lds0 = (unsigned)(uintptr_t)lds;
    const unsigned lv = lane * 16;
    const unsigned s0 = hbase + (unsigned)(KH * 32 + tl) * 2048u, d0 = lds0 + KH * 16384 + tl * 2048;
    const int rd0 = KH * 16384 + lane * 16;
    bf16x8 ones, sel;
    { const short v = ((lane & 15) == ((lane >> 4) & 1)) ? (short)0x3f80 : (short)0;
#pragma unroll
      for (int j = 0; j < 8; ++j) { ones[j] = v; sel[j] = ((lane & 15) == j) ? (short)0x3f80 : (short)0; } }
    const unsigned sd = lds0 + K2_SIDE + w * 1024;
#define K2_DMA(k, rd, fr) do { \
        if ((k) < 4) dma16c(d0 + (fr) + ((k) >> 1) * 8192 + ((k) & 1) * 1024, lv, (k) < 2 ? srk : srv, s0 + ((rd) + 2) * 8192 + ((k) & 1) * 1024); \
        else if ((k) == 4) dma16(sd + sc, S.cv, S.src, S.cs0 + (rd) * (4 * FF * 2)); \
        else dma16(sd + 24576, lv, (rd) < 4 ? S.srp : S.sro, (rd) < 4 ? S.ps0 + (rd) * 1024 : (rd) == 4 ? (S.ps0 >> 2) : 0x7ff00000u); } while (0)
#define K2_HOOK(gi, rd, fr) do { if (((gi) >> 1) < 6 && ((gi) & 1) == KH) K2_DMA((gi) >> 1, rd, fr); } while (0)
    f32x16 sa[2], sb[2];
    const f32x16 zero16 = {};
    bf16x8 k0 = ldf(lds + rd0), k1 = ldf(lds + rd0 + 1024), v0, v1;
#pragma unroll
    for (int qt = 0; qt < 2; ++qt) { sa[qt] = MFMA32(k0, qf[qt][0], zero16); sa[qt] = MFMA32(k1, qf[qt][1], sa[qt]); }
    k0 = ldf(lds + rd0 + 2048);
    u32x4 pa = {0u, 0u, 0u, 0u}, pb = {0u, 0u, 0u, 0u}; float e6 = 0.f, e7 = 0.f;
    v1 = k1;
    bf16x8 csd = {};
#define EX2(x) __builtin_amdgcn_exp2f(x)
#define K2_GROUP(S_, B, PC, PP, QK, VP, QP, RD, gi) do { \
        QK; PP[3] = pk_bf16(e6, e7); SCHED0(); \
        const float x0 = EX2(S_[B + 0]), x1 = EX2(S_[B + 1]), x2 = EX2(S_[B + 2]); RD; SCHED0(); \
        o[QP] = MFMA32(VP, __builtin_bit_cast(bf16x8, PP), o[QP]); SCHED0(); \
        const float x3 = EX2(S_[B + 3]), x4 = EX2(S_[B + 4]), x5 = EX2(S_[B + 5]); PC[0] = pk_bf16(x0, x1); SCHED0(); \
        lacc[QP] = MFMA16(ones, __builtin_bit_cast(bf16x8, PP), lacc[QP]); K2_HOOK(gi, rd, fr); SCHED0(); \
        e6 = EX2(S_[B + 6]); e7 = EX2(S_[B + 7]); PC[1] = pk_bf16(x2, x3); PC[2] = pk_bf16(x4, x5); SCHED0(); } while (0)
#define K2_TILE(SC, SN, kn1, kn2, va, ti) do { \
        K2_GROUP(SC[0], 0, pa, pb, SN[0] = MFMA32(k0, qf[0][0], zero16), v1, 1, v0 = ldf(va), 4 * (ti)); \
        K2_GROUP(SC[1], 0, pb, pa, SN[1] = MFMA32(k0, qf[1][0], zero16), v0, 0, k1 = ldf((kn1) + 1024), 4 * (ti) + 1); \
        K2_GROUP(SC[0], 8, pa, pb, SN[0] = MFMA32(k1, qf[0][1], SN[0]), v0, 1, v1 = ldf((va) + 1024), 4 * (ti) + 2); \
        K2_GROUP(SC[1], 8, pb, pa, SN[1] = MFMA32(k1, qf[1][1], SN[1]), v1, 0, k0 = ldf(kn2), 4 * (ti) + 3); } while (0)
    int cur = 0, fr = 2 * K2_SLOT, sc = 0;
#pragma unroll 1
    for (int rd = 0; rd < 8; ++rd) {
        const int nx = (cur == 2 * K2_SLOT) ? 0 : cur + K2_SLOT;
        const int sn = (sc == 16384) ? 0 : sc + 8192;
        if (rd > 0) WAITBAR(2);
        LAS const char* pc = lds + cur + rd0; LAS const char* pn = lds + nx + rd0;
        K2_TILE(sa, sb, pc + 2048, pc + 4096, pc + 8192, 0);
        csd = ldf(lds + K2_SIDE + sn + w * 1024 + lane * 16);
        if (rd < 2) csd = (bf16x8){};
        K2_TILE(sb, sa, pc + 4096, pc + 6144, pc + 8192 + 2048, 1);
        cs = MFMA16(sel, csd, cs);
        K2_TILE(sa, sb, pc + 6144, pn, pc + 8192 + 4096, 2);
        K2_TILE(sb, sa, pn, pn + 2048, pc + 8192 + 6144, 3);
        fr = cur; cur = nx; sc = sn;
    }
    pb[3] = pk_bf16(e6, e7);
    o[1] = MFMA32(v1, __builtin_bit_cast(bf16x8, pb), o[1]);
    lacc[1] = MFMA16(ones, __builtin_bit_cast(bf16x8, pb), lacc[1]);
    WAITBAR(0);
    cs = MFMA16(sel, ldf(lds + K2_SIDE + w * 1024 + lane * 16), cs);
    cs = MFMA16(sel, ldf(lds + K2_SIDE + 8192 + w * 1024 + lane * 16), cs);
#undef K2_GROUP
#undef K2_TILE
#undef K2_DMA
#undef K2_HOOK
}
#define K2_STAGE(rd, slot) do { dma16c(d0 + (slot), lv, srk, s0 + (rd) * 8192); dma16c(d0 + (slot) + 1024, lv, srk, s0 + (rd) * 8192 + 1024); \
        dma16c(d0 + (slot) + 8192, lv, srv, s0 + (rd) * 8192); dma16c(d0 + (slot) + 9216, lv, srv, s0 + (rd) * 8192 + 1024); } while (0)
__device__ __forceinline__ void attn_safe(LAS char* lds, srd_t srk, srd_t srv, unsigned hbase, const bf16x8 (&qf)[2][2], int w, int lane, f32x16 (&o)[2], float (&mrun)[2], float (&lrun)[2]) {
    const int kh = w >> 2, tl = w & 3;
    const unsigned lds0 = (unsigned)(uintptr_t)lds;
    const unsigned lv = lane * 16;
    const unsigned s0 = hbase + (unsigned)(kh * 32 + tl) * 2048u, d0 = lds0 + kh * 16384 + tl * 2048;
    const int rd0 = kh * 16384 + lane * 16;
    const f32x16 zero16 = {};
#pragma unroll 1
    for (int rd = 0; rd < 8; ++rd) {
        K2_STAGE(rd, 0);
        WAITBAR(0);
#pragma unroll 1
        for (int i = 0; i < 4; ++i) {
            LAS const char* pk = lds + rd0 + i * 2048;
            const bf16x8 k0 = ldf(pk), k1 = ldf(pk + 1024), v0 = ldf(pk + 8192), v1 = ldf(pk + 9216);
#pragma unroll
            for (int qt = 0; qt < 2; ++qt) {
                f32x16 sc = MFMA32(k0, qf[qt][0], zero16); sc = MFMA32(k1, qf[qt][1], sc);
                float mx = sc[0];
#pragma unroll
                for (int j = 1; j < 16; ++j) mx = fmaxf(mx, sc[j]);
                mx = fmaxf(mx, __shfl_xor(mx, 32));
                const float mn = fmaxf(mrun[qt], mx);
                const float ms = (mn == -INFINITY) ? 0.f : mn;
                const float al = __builtin_amdgcn_exp2f(mrun[qt] - ms);
                mrun[qt] = mn;
                u32x4 p0, p1;
#pragma unroll
                for (int j = 0; j < 4; ++j) { p0[j] = pk_bf16(__builtin_amdgcn_exp2f(sc[2 * j] - ms), __builtin_amdgcn_exp2f(sc[2 * j + 1] - ms));
                                              p1[j] = pk_bf16(__builtin_amdgcn_exp2f(sc[8 + 2 * j] - ms), __builtin_amdgcn_exp2f(sc[9 + 2 * j] - ms)); }
                float s_ = 0.f;
#pragma unroll
                for (int j = 0; j < 4; ++j) { const unsigned a = p0[j], c = p1[j]; s_ += bf_lo(a) + bf_hi(a); s_ += bf_lo(c) + bf_hi(c); }
                lrun[qt] = lrun[qt] * al + s_;
#pragma unroll
                for (int j = 0; j < 16; ++j) o[qt][j] *= al;
                o[qt] = MFMA32(v0, __builtin_bit_cast(bf16x8, p0), o[qt]); o[qt] = MFMA32(v1, __builtin_bit_cast(bf16x8, p1), o[qt]);
            }
        }
        WAITBAR(0);
    }
}

template <bool SYNC>
__device__ __forceinline__ void phase2(LAS char* lds, const Params& P, unsigned tag STAMP_ARGS) {
    const int tid = threadIdx.x, lane = tid & 63, w = __builtin_amdgcn_readfirstlane(tid >> 6);
    const int b = blockIdx.x, xcd = b & 7, loc = b >> 3;
    const int head = xcd * 4 + (loc & 3), qb = loc >> 2;
    char* ws = P.ws;
    K2Side S;
    const int cgrp = b % 56, rq = b / 56;
    const bool do_cs = b < 224;
    S.src = mk_srd(cgrp < 28 ? P.wg : P.wu, DM * FF * 2);
    S.cv = (unsigned)(lane >> 4) * (FF * 2) + (lane & 15) * 16;
    S.cs0 = do_cs ? (unsigned)(rq * 256 + w * 32) * (FF * 2) + (unsigned)(cgrp < 28 ? cgrp : cgrp - 28) * 256u : 0x7ff00000u;
    S.srp = mk_srd(P.wd, FF * DM * 2); S.sro = mk_srd(P.wo, DM * DM * 2);
    S.ps0 = (unsigned)(b * 8 + w) * 4096u;
    const srd_t srk = mk_srd(ws + WS_K, SEQ * DM * 2), srv = mk_srd(ws + WS_V, SEQ * DM * 2);
    const unsigned hbase = (unsigned)head * 64u * 2048u;
    const int kh = w >> 2;
    if (SYNC) { if (w == 0) flags_wait(ws, lane < 16 ? FL_1 + (head >> 1) * 16 + lane : -1, tag); LBAR(); }
    STAMP_AT(1);
    bf16x8 qf[2][2];
    {
        const unsigned lds0 = (unsigned)(uintptr_t)lds, lv = lane * 16;
        const unsigned s0 = hbase + (unsigned)(kh * 32 + (w & 3)) * 2048u, d0 = lds0 + kh * 16384 + (w & 3) * 2048;
        K2_STAGE(0, 0);
        const srd_t srq = mk_srd(ws + WS_Q, SEQ * DM * 2);
        const unsigned qo = ((unsigned)head * 64 + qb * 8 + (w & 3) * 2) * 2048u + lane * 16;
#pragma unroll
        for (int qt = 0; qt < 2; ++qt)
#pragma unroll
            for (int s = 0; s < 2; ++s) qf[qt][s] = __builtin_bit_cast(bf16x8, ldc16(srq, qo + qt * 2048 + s * 1024));
        K2_STAGE(1, K2_SLOT);
    }
    WAITBAR(0);
    STAMP_AT(2);
    f32x16 o[2] = {}; f32x4 lacc[2] = {}; f32x4 cs = {0.f, 0.f, 0.f, 0.f};
    if (kh == 0) attn_fast<0>(lds, srk, srv, hbase, S, w, lane, qf, o, lacc, cs);
    else attn_fast<1>(lds, srk, srv, hbase, S, w, lane, qf, o, lacc, cs);
    STAMP_AT(3);
    LAS float* ob = (LAS float*)lds;
    LAS float* lb = (LAS float*)(lds + 32768);
    LAS unsigned* flag = (LAS unsigned*)(lds + 32768 + 4096);
    LAS float* cb = (LAS float*)(lds + 40960);
    const int hi = lane >> 5, q32 = lane & 31;
    const srd_t sro = mk_srd(ws + WS_O, SEQ * DM * 2);
    const unsigned dst = FRAG(qb * 16 + (w & 3) * 4 + kh * 2 + (q32 >> 4), head, 32) + (hi * 16 + (q32 & 15)) * 16;
    bool bad;
    {
        if (tid == 0) flag[0] = 0u;
        if (lane < 16) { lb[w * 64 + lane] = lacc[0][0]; lb[w * 64 + 16 + lane] = lacc[0][1]; lb[w * 64 + 32 + lane] = lacc[1][0]; lb[w * 64 + 48 + lane] = lacc[1][1]; }
#pragma unroll
        for (int j = 0; j < 4; ++j) { const f32x4 v = kh ? (f32x4){o[0][4 * j], o[0][4 * j + 1], o[0][4 * j + 2], o[0][4 * j + 3]} : (f32x4){o[1][4 * j], o[1][4 * j + 1], o[1][4 * j + 2], o[1][4 * j + 3]};
            *(LAS f32x4*)(ob + ((w * 4 + j) * 64 + lane) * 4) = v; }
        if (lane < 32) *(LAS f32x4*)(cb + w * 128 + (lane & 15) * 8 + (lane >> 4) * 4) = cs;
        LBAR();
        const int pw = w ^ 4;
        f32x16 of;
#pragma unroll
        for (int j = 0; j < 4; ++j) { const f32x4 v = *(LAS const f32x4*)(ob + ((pw * 4 + j) * 64 + lane) * 4);
#pragma unroll
            for (int e = 0; e < 4; ++e) of[4 * j + e] = (kh ? o[1][4 * j + e] : o[0][4 * j + e]) + v[e]; }
        const float l = lb[w * 64 + kh * 32 + q32] + lb[pw * 64 + kh * 32 + q32];
        bool ok = (l >= 0x1p-60f) && (l <= 0x1p+80f);
#pragma unroll
        for (int j = 0; j < 16; ++j) ok = ok && (fabsf(of[j]) <= 0x1p+120f);
        if (!__all(ok)) { if (lane == 0) flag[0] = 1u; }
        if (do_cs && tid < 128) {
            float s = 0.f;
#pragma unroll
            for (int i = 0; i < 8; ++i) s += cb[i * 128 + tid];
            __hip_atomic_store((gu32*)(ws + WS_CS) + (rq * 7168 + cgrp * 128 + tid), __float_as_uint(s), RLX_AGENT);
        }
        LBAR();
        bad = flag[0] != 0u;
        if (!bad) {
            const float rl = 1.0f / l;
#pragma unroll
            for (int c = 0; c < 2; ++c) {
                u32x4 ov; ov.x = pk_bf16(of[8 * c + 0] * rl, of[8 * c + 1] * rl); ov.y = pk_bf16(of[8 * c + 2] * rl, of[8 * c + 3] * rl);
                ov.z = pk_bf16(of[8 * c + 4] * rl, of[8 * c + 5] * rl); ov.w = pk_bf16(of[8 * c + 6] * rl, of[8 * c + 7] * rl);
                st16(sro, dst + c * 512, ov);
            }
        }
    }
    if (bad) {
        WAITBAR(0);
        o[0] = (f32x16){}; o[1] = (f32x16){}; float mrun[2] = {-INFINITY, -INFINITY}, lrun[2] = {0.f, 0.f};
        attn_safe(lds, srk, srv, hbase, qf, w, lane, o, mrun, lrun);
        LAS float* mb = lb + 512;
#pragma unroll
        for (int qt = 0; qt < 2; ++qt) { const float lt = lrun[qt] + __shfl_xor(lrun[qt], 32); if (lane < 32) { lb[w * 64 + qt * 32 + lane] = lt; mb[w * 64 + qt * 32 + lane] = mrun[qt]; } }
#pragma unroll
        for (int j = 0; j < 16; ++j) ob[(w * 16 + j) * 64 + lane] = kh ? o[0][j] : o[1][j];
        WAITBAR(0);
        const int pw = w ^ 4;
        const float ma = mb[w * 64 + kh * 32 + q32], mp = mb[pw * 64 + kh * 32 + q32];
        const float mn = fmaxf(ma, mp), ms = (mn == -INFINITY) ? 0.f : mn;
        const float fa = __builtin_amdgcn_exp2f(ma - ms), fp = __builtin_amdgcn_exp2f(mp - ms);
        const float l = lb[w * 64 + kh * 32 + q32] * fa + lb[pw * 64 + kh * 32 + q32] * fp;
        const float rl = 1.0f / l;
        float of[16];
#pragma unroll
        for (int j = 0; j < 16; ++j) of[j] = ((kh ? o[1][j] : o[0][j]) * fa + ob[(pw * 16 + j) * 64 + lane] * fp) * rl;
#pragma unroll
        for (int c = 0; c < 2; ++c) {
            u32x4 ov; ov.x = pk_bf16(of[8 * c + 0], of[8 * c + 1]); ov.y = pk_bf16(of[8 * c + 2], of[8 * c + 3]);
            ov.z = pk_bf16(of[8 * c + 4], of[8 * c + 5]); ov.w = pk_bf16(of[8 * c + 6], of[8 * c + 7]);
            st16(sro, dst + c * 512, ov);
        }
    }
    CODE_TOUCH(".Lcw_p2", ".Lcw_p3", 0, 512);
    STAMP_AT(4);
    WAITBAR(0);
    if (SYNC && tid == 0) flag_set(ws, FL_2 + qb * 32 + head, tag);
}
#undef K2_STAGE

constexpr int K3_SLOT = 24576, K3_NS = 6, K3_AUX = K3_NS * K3_SLOT;
template <int MODE, bool SYNC>
__device__ __forceinline__ void phase35(LAS char* lds, const Params& P, unsigned tag STAMP_ARGS) {
    constexpr int KD = MODE == 0 ? DM : FF, KT = KD / 32, NT = KD / 64;
    const unsigned lds0 = (unsigned)(uintptr_t)lds;
    const int tid = threadIdx.x, lane = tid & 63, w = __builtin_amdgcn_readfirstlane(tid >> 6), g = lane >> 4, r = lane & 15;
    const int b = blockIdx.x, xcd = b & 7, loc = b >> 3;
    const int rt = (xcd >> 1) * 4 + (loc & 3), ct = (xcd & 1) * 8 + (loc >> 2);
    char* ws = P.ws;
    if (w >= 4) {
        const int j = w - 4;
        const srd_t srx = mk_srd(ws + (MODE == 0 ? WS_O : WS_ACT), SEQ * KD * 2), srw = mk_srd(MODE == 0 ? P.wo : P.wd, KD * DM * 2);
        const unsigned xv = lane * 16, xs0 = FRAG(rt * 8 + 2 * j, 0, KT);
        constexpr unsigned XM = FRAG(1, 0, KT), XK = FRAG(0, 1, KT);
        const int wrow = lane >> 3, wch = (lane & 7) ^ (((wrow >> 1) & 3) << 1);
        const unsigned wv = (unsigned)wrow * 2048u + wch * 16, ws0 = (unsigned)ct * 128u + (unsigned)(16 * j) * 2048u;
        const unsigned ld0 = lds0 + j * 4096, ld1 = lds0 + 16384 + j * 2048;
        const int rot = 2 * xcd, fidx = lane < 32 ? FL_2 + (rt >> 1) * 32 + lane : -1;
        unsigned long long rdy = SYNC ? 0ull : ~0ull;
#define K3_T(t) (((t) + rot) & 15)
#define K3_W(t, slot) do { dma16(ld1 + (slot), wv, srw, ws0 + K3_T(t) * (64 * 2048)); dma16(ld1 + (slot) + 1024, wv, srw, ws0 + 8 * 2048 + K3_T(t) * (64 * 2048)); } while (0)
#define K3_X(t, slot) do { wait_mask(ws, fidx, tag, 3ull << (2 * K3_T(t)), rdy); \
        dma16c(ld0 + (slot), xv, srx, xs0 + K3_T(t) * (2 * XK)); dma16c(ld0 + (slot) + 1024, xv, srx, xs0 + K3_T(t) * (2 * XK) + XK); \
        dma16c(ld0 + (slot) + 2048, xv, srx, xs0 + XM + K3_T(t) * (2 * XK)); dma16c(ld0 + (slot) + 3072, xv, srx, xs0 + XM + K3_T(t) * (2 * XK) + XK); } while (0)
        K3_W(0, 0); K3_W(1, K3_SLOT); K3_W(2, 2 * K3_SLOT); K3_W(3, 3 * K3_SLOT); K3_W(4, 4 * K3_SLOT);
        K3_X(0, 0); K3_X(1, K3_SLOT); K3_X(2, 2 * K3_SLOT);
        WAITBAR(8);  K3_X(3, 3 * K3_SLOT); K3_X(4, 4 * K3_SLOT); K3_X(5, 5 * K3_SLOT); K3_W(5, 5 * K3_SLOT);
        WAITBAR(18); K3_X(6, 0); K3_W(6, 0);
        WAITBAR(20); K3_X(7, K3_SLOT); K3_W(7, K3_SLOT);
        WAITBAR(22); K3_X(8, 2 * K3_SLOT); K3_W(8, 2 * K3_SLOT);
        int fr = 3 * K3_SLOT;
#pragma unroll 1
        for (int t = 4; t + K3_NS - 1 < NT; ++t) {
            WAITBAR(6 * (K3_NS - 2));
            K3_X(t + K3_NS - 1, fr); K3_W(t + K3_NS - 1, fr);
            fr = (fr == (K3_NS - 1) * K3_SLOT) ? 0 : fr + K3_SLOT;
        }
        WAITBAR(24); WAITBAR(18); WAITBAR(12); WAITBAR(6); WAITBAR(0);
        if (MODE == 0) LBAR();
    } else {
        const int wm = w >> 1, wn = w & 1;
        const int col0 = ct * 64 + wn * 32 + 4 * g;
        STAMP_AT(1);
        u32x4 res[4];
#pragma unroll
        for (int m = 0; m < 4; ++m) {
            const int mrow = rt * 128 + wm * 64 + m * 16;
            if (MODE == 0) { const u32x2 a = *(const u32x2*)(P.x + (size_t)(mrow + r) * DM + col0), c = *(const u32x2*)(P.x + (size_t)(mrow + r) * DM + col0 + 16); res[m] = (u32x4){a.x, a.y, c.x, c.y}; }
            else res[m] = ldc16(mk_srd(ws + WS_H, SEQ * DM * 2), FRAG(mrow >> 4, ct * 2 + wn, 32) + lane * 16);
        }
        const int xrd = wm * 8192 + lane * 16;
        const int q4 = (lane >> 2) & 3, p4 = lane & 3;
        const int trow = (MODE == 0) ? (4 * (g & 1) + 16 * (g >> 1) + q4) : (4 * g + q4);
        constexpr int TR2 = (MODE == 0) ? 8 * 128 : 16 * 128;
        int wrd[2];
#pragma unroll
        for (int n = 0; n < 2; ++n) wrd[n] = 16384 + trow * 128 + (((wn * 2 + n) ^ ((trow >> 1) & 3)) * 32) + (p4 >> 1) * 16 + (p4 & 1) * 8;
        f32x4 acc[4][2];
#pragma unroll
        for (int m = 0; m < 4; ++m) { acc[m][0] = (f32x4){0.f, 0.f, 0.f, 0.f}; acc[m][1] = (f32x4){0.f, 0.f, 0.f, 0.f}; }
        bf16x8 xa[4], xb[4]; s16x4 wa[4], wb[4];
#define K3_RD(i, X, W, sl, s) do { if ((i) == 2) X[0] = ldf(lds + (sl) + xrd + (s) * 1024); else if ((i) >= 5) X[(i) - 4] = ldf(lds + (sl) + xrd + ((i) - 4) * 2048 + (s) * 1024); \
        else { const int k_ = (i) < 2 ? (i) : (i) - 1; W[k_] = trd(lds + (sl) + wrd[k_ >> 1] + (k_ & 1) * TR2 + (s) * 4096); } } while (0)
#define K3_MM(i, X, W) do { const int m_ = (i) >> 1, n_ = (i) & 1; acc[m_][n_] = MFMA16(cat8(W[2 * n_], W[2 * n_ + 1]), X[m_], acc[m_][n_]); } while (0)
#define K3_HALF(XM, WM, XR, WR, sl, s, DOMMA) do { _Pragma("unroll") for (int i_ = 0; i_ < 8; ++i_) { if (DOMMA) K3_MM(i_, XM, WM); K3_RD(i_, XR, WR, sl, s); SCHED0(); } } while (0)
        LBAR();
        STAMP_AT(2);
        K3_HALF(xb, wb, xa, wa, 0, 0, false);
        K3_HALF(xa, wa, xb, wb, 0, 1, true);
        int cur = K3_SLOT;
#pragma unroll 1
        for (int t = 1; t < NT; ++t) {
            LBAR();
            K3_HALF(xb, wb, xa, wa, cur, 0, true);
            K3_HALF(xa, wa, xb, wb, cur, 1, true);
            cur = (cur == (K3_NS - 1) * K3_SLOT) ? 0 : cur + K3_SLOT;
        }
#pragma unroll
        for (int i = 0; i < 8; ++i) K3_MM(i, xb, wb);
        STAMP_AT(3);
        if (MODE == 0) {
            LAS f32x2* sb = (LAS f32x2*)(lds + K3_AUX);
            const srd_t srh = mk_srd(ws + WS_H, SEQ * DM * 2);
#pragma unroll
            for (int m = 0; m < 4; ++m) {
                const int mrow = rt * 128 + wm * 64 + m * 16;
                const f32x4 a = {bf_lo(res[m].x) + acc[m][0][0], bf_hi(res[m].x) + acc[m][0][1], bf_lo(res[m].y) + acc[m][0][2], bf_hi(res[m].y) + acc[m][0][3]};
                const f32x4 c = {bf_lo(res[m].z) + acc[m][1][0], bf_hi(res[m].z) + acc[m][1][1], bf_lo(res[m].w) + acc[m][1][2], bf_hi(res[m].w) + acc[m][1][3]};
                const u32x4 hv = pk8(a, c);
                st16(srh, FRAG(mrow >> 4, ct * 2 + wn, 32) + lane * 16, hv);
                float s = 0.f, q = 0.f;
#pragma unroll
                for (int j = 0; j < 4; ++j) { const unsigned d = hv[j]; const float u = bf_lo(d), v = bf_hi(d); s += u + v; q += u * u + v * v; }
                s += __shfl_xor(s, 16); s += __shfl_xor(s, 32); q += __shfl_xor(q, 16); q += __shfl_xor(q, 32);
                if (g == 0) sb[(wm * 64 + m * 16 + r) * 2 + wn] = (f32x2){s, q};
            }
            LBAR();
            if (tid < 128) { const f32x2 a = sb[tid * 2] + sb[tid * 2 + 1];
                __hip_atomic_store((gu64*)(ws + WS_ST) + ((size_t)(rt * 128 + tid) * 16 + ct), ((unsigned long long)__float_as_uint(a[1]) << 32) | __float_as_uint(a[0]), RLX_AGENT); }
        } else {
#pragma unroll
            for (int m = 0; m < 4; ++m) {
                const int mrow = rt * 128 + wm * 64 + m * 16;
                unsigned a0 = pk_bf16(bf_lo(res[m].x) + acc[m][0][0], bf_hi(res[m].x) + acc[m][0][1]), a1 = pk_bf16(bf_lo(res[m].y) + acc[m][0][2], bf_hi(res[m].y) + acc[m][0][3]);
                unsigned b0 = pk_bf16(bf_lo(res[m].z) + acc[m][1][0], bf_hi(res[m].z) + acc[m][1][1]), b1 = pk_bf16(bf_lo(res[m].w) + acc[m][1][2], bf_hi(res[m].w) + acc[m][1][3]);
                const auto s0 = __builtin_amdgcn_permlane16_swap(b0, a0, false, false), s1 = __builtin_amdgcn_permlane16_swap(b1, a1, false, false);
                const u32x4 ov = {s0[0], s1[0], s0[1], s1[1]};
                const int c8 = ((g & 1) ? 0 : 16) + (g >> 1) * 8;
                *(u32x4*)(P.out + (size_t)(mrow + r) * DM + ct * 64 + wn * 32 + c8) = ov;
            }
        }
    }
    if (MODE == 0) CODE_TOUCH(".Lcw_p3", ".Lcw_p4", 0, 512);
    STAMP_AT(4);
    if (MODE == 0) {
        WAITBAR(0);
        if (SYNC && tid == 0) flag_set(ws, FL_3 + rt * 16 + ct, tag);
    }
}

constexpr int K5_SLOT = 32768, K5_NS = 4;
template <int KH>
__device__ __forceinline__ void p5_finish(const Params& P, const f32x4 (&acc)[4][4], unsigned tag, int b, int w, int lane, int rt, int ct STAMP_ARGS) {
    char* ws = P.ws;
    const int g = lane >> 4, r = lane & 15, wm = w >> 1, wn = w & 1;
    const srd_t sre = mk_srd(ws + WS_XCH, 8u << 20);
    const unsigned eo = (unsigned)(w * 8192) + lane * 16;
#pragma unroll
    for (int m = 0; m < 4; ++m)
#pragma unroll
        for (int n = 0; n < 2; ++n) st16(sre, (unsigned)b * 32768u + eo + (m * 2 + n) * 1024, __builtin_bit_cast(u32x4, acc[m][2 * (1 - KH) + n]));
    u32x4 res[4];
#pragma unroll
    for (int m = 0; m < 4; ++m) res[m] = ldc16(mk_srd(ws + WS_H, SEQ * DM * 2), FRAG(rt * 8 + wm * 4 + m, ct * 4 + wn * 2 + KH, 32) + lane * 16);
    WAITV(0);
    if (lane == 0) flag_set(ws, FL_5 + b * 4 + w, tag);
    flags_wait(ws, lane == 0 ? FL_5 + (b ^ 2) * 4 + w : -1, tag);
    STAMP_AT(4);
    u32x4 pv[4][2];
#pragma unroll
    for (int m = 0; m < 4; ++m)
#pragma unroll
        for (int n = 0; n < 2; ++n) pv[m][n] = ldc16(sre, (unsigned)(b ^ 2) * 32768u + eo + (m * 2 + n) * 1024);
#pragma unroll
    for (int m = 0; m < 4; ++m) {
        const int mrow = rt * 128 + wm * 64 + m * 16;
        const f32x4 t0 = acc[m][2 * KH] + __builtin_bit_cast(f32x4, pv[m][0]), t1 = acc[m][2 * KH + 1] + __builtin_bit_cast(f32x4, pv[m][1]);
        unsigned a0 = pk_bf16(bf_lo(res[m].x) + t0[0], bf_hi(res[m].x) + t0[1]), a1 = pk_bf16(bf_lo(res[m].y) + t0[2], bf_hi(res[m].y) + t0[3]);
        unsigned b0 = pk_bf16(bf_lo(res[m].z) + t1[0], bf_hi(res[m].z) + t1[1]), b1 = pk_bf16(bf_lo(res[m].w) + t1[2], bf_hi(res[m].w) + t1[3]);
        const auto s0 = __builtin_amdgcn_permlane16_swap(b0, a0, false, false), s1 = __builtin_amdgcn_permlane16_swap(b1, a1, false, false);
        const u32x4 ov = {s0[0], s1[0], s0[1], s1[1]};
        const int c8 = ((g & 1) ? 0 : 16) + (g >> 1) * 8;
        *(u32x4*)(P.out + (size_t)(mrow + r) * DM + ct * 128 + wn * 64 + KH * 32 + c8) = ov;
    }
}
template <bool SYNC>
__device__ __forceinline__ void phase5(LAS char* lds, const Params& P, unsigned tag STAMP_ARGS) {
    constexpr int KT = FF / 32, NT = FF / 128;
    const unsigned lds0 = (unsigned)(uintptr_t)lds;
    const int tid = threadIdx.x, lane = tid & 63, w = __builtin_amdgcn_readfirstlane(tid >> 6), g = lane >> 4, r = lane & 15;
    const int b = blockIdx.x, xcd = b & 7, loc = b >> 3;
    const int kh = (xcd >> 1) & 1, rt = (xcd >> 2) * 8 + (xcd & 1) * 4 + (loc & 3), ct = loc >> 2;
    char* ws = P.ws;
    if (w >= 4) {
        const int j = w - 4;
        const srd_t srx = mk_srd(ws + WS_ACT, SEQ * FF * 2), srw = mk_srd(P.wd, FF * DM * 2);
        constexpr unsigned XM = FRAG(1, 0, KT), XK = FRAG(0, 1, KT);
        const unsigned xv = lane * 16, xs0 = FRAG(rt * 8 + 2 * j, kh * (KT / 2), KT);
        const int wrow = lane >> 3, wch = (lane & 7) ^ (((wrow >> 1) & 3) << 1);
        const unsigned wv = (unsigned)wrow * 2048u + wch * 16, ws0 = (unsigned)ct * 256u + (unsigned)(kh * (FF / 2) + 16 * j) * 2048u;
        const unsigned ld0 = lds0 + j * 4096, ld1 = lds0 + 16384 + j * 2048;
        const int rot = 14 * (xcd & 1), fidx = lane < 16 ? FL_4 + (rt >> 1) * 32 + kh * 16 + lane : -1;
        unsigned long long rdy = SYNC ? 0ull : ~0ull;
#define K5_T(t) ((t) + rot >= NT ? (t) + rot - NT : (t) + rot)
#define K5_W(t, slot) do { const unsigned o_ = ws0 + K5_T(t) * (64 * 2048); dma16(ld1 + (slot), wv, srw, o_); dma16(ld1 + (slot) + 1024, wv, srw, o_ + 8 * 2048); \
        dma16(ld1 + (slot) + 8192, wv, srw, o_ + 128); dma16(ld1 + (slot) + 9216, wv, srw, o_ + 128 + 8 * 2048); } while (0)
#define K5_X(t, slot) do { const int tt_ = K5_T(t); wait_mask(ws, fidx, tag, (1ull << ((4 * tt_) / 7)) | (1ull << ((4 * tt_ + 3) / 7)), rdy); const unsigned o_ = xs0 + tt_ * (2 * XK); \
        dma16c(ld0 + (slot), xv, srx, o_); dma16c(ld0 + (slot) + 1024, xv, srx, o_ + XK); dma16c(ld0 + (slot) + 2048, xv, srx, o_ + XM); dma16c(ld0 + (slot) + 3072, xv, srx, o_ + XM + XK); } while (0)
        K5_W(0, 0); K5_W(1, K5_SLOT); K5_W(2, 2 * K5_SLOT); K5_W(3, 3 * K5_SLOT);
        K5_X(0, 0); K5_X(1, K5_SLOT); K5_X(2, 2 * K5_SLOT); K5_X(3, 3 * K5_SLOT);
        WAITBAR(12);
        WAITBAR(8);  K5_X(4, 0); K5_W(4, 0);
        WAITBAR(12); K5_X(5, K5_SLOT); K5_W(5, K5_SLOT);
        WAITBAR(16); K5_X(6, 2 * K5_SLOT); K5_W(6, 2 * K5_SLOT);
        int fr = 3 * K5_SLOT;
#pragma unroll 1
        for (int t = 4; t + 3 < NT; ++t) {
            WAITBAR(16);
            K5_X(t + 3, fr); K5_W(t + 3, fr);
            fr = (fr == 3 * K5_SLOT) ? 0 : fr + K5_SLOT;
        }
        WAITBAR(16); WAITBAR(8); WAITBAR(0);
    } else {
        const int wm = w >> 1, wn = w & 1;
        STAMP_AT(1);
        const int xrd = wm * 8192 + lane * 16;
        const int q4 = (lane >> 2) & 3, p4 = lane & 3, trow = 4 * g + q4;
        int wrd[4];
#pragma unroll
        for (int n = 0; n < 4; ++n) wrd[n] = 16384 + wn * 8192 + trow * 128 + ((n ^ ((trow >> 1) & 3)) * 32) + (p4 >> 1) * 16 + (p4 & 1) * 8;
        f32x4 acc[4][4];
#pragma unroll
        for (int m = 0; m < 4; ++m)
#pragma unroll
            for (int n = 0; n < 4; ++n) acc[m][n] = (f32x4){0.f, 0.f, 0.f, 0.f};
        bf16x8 xa[4], xb[4]; s16x4 wa[8], wb[8];
#define K5_RD(i, X, W, sl, s) do { if ((i) == 2) X[0] = ldf(lds + (sl) + xrd + (s) * 1024); else if ((i) >= 9) X[(i) - 8] = ldf(lds + (sl) + xrd + ((i) - 8) * 2048 + (s) * 1024); \
        else { const int k_ = (i) < 2 ? (i) : (i) - 1; W[k_] = trd(lds + (sl) + wrd[k_ >> 1] + (k_ & 1) * 2048 + (s) * 4096); } } while (0)
#define K5_MM(i, X, W) do { const int m_ = (i) >> 2, n_ = (i) & 3; acc[m_][n_] = MFMA16(cat8(W[2 * n_], W[2 * n_ + 1]), X[m_], acc[m_][n_]); } while (0)
#define K5_HALF(XM_, WM_, XR, WR, sl, s, DOMMA) do { _Pragma("unroll") for (int i_ = 0; i_ < 16; ++i_) { if (DOMMA) K5_MM(i_, XM_, WM_); if (i_ < 12) K5_RD(i_, XR, WR, sl, s); SCHED0(); } } while (0)
        LBAR();
        STAMP_AT(2);
        K5_HALF(xb, wb, xa, wa, 0, 0, false);
        K5_HALF(xa, wa, xb, wb, 0, 1, true);
        int cur = K5_SLOT;
#pragma unroll 1
        for (int t = 1; t < NT; ++t) {
            LBAR();
            K5_HALF(xb, wb, xa, wa, cur, 0, true);
            K5_HALF(xa, wa, xb, wb, cur, 1, true);
            cur = (cur == 3 * K5_SLOT) ? 0 : cur + K5_SLOT;
        }
#pragma unroll
        for (int i = 0; i < 16; ++i) K5_MM(i, xb, wb);
        STAMP_AT(3);
        if (kh) p5_finish<1>(P, acc, tag, b, w, lane, rt, ct STAMP_PASS); else p5_finish<0>(P, acc, tag, b, w, lane, rt, ct STAMP_PASS);
    }
}

constexpr int K4_SLOT = 30720, K4_NS = 4, K4_STRAW = K4_NS * K4_SLOT, K4_STAT = K4_STRAW + 32768, K4_CS = K4_STAT + 2048, K4_CSRAW = K4_CS + 1024;
struct K4Dma { srd_t srx, srg, sru; unsigned xv, gv, xs0, gs0, ld0, ld1; };
#define K4_DMA(i, t, slot) do { if ((i) < 2) dma16c(D.ld0 + (slot) + (i) * 1024, D.xv, D.srx, D.xs0 + (t) * FRAG(0, 1, 32) + (i) * FRAG(1, 0, 32)); \
        else dma16(D.ld1 + (slot) + ((i) - 2) * 7168, D.gv, (i) == 2 ? D.srg : D.sru, D.gs0 + (t) * (32 * FF * 2)); } while (0)
template <int NJ, int J0, bool HASW>
__device__ __forceinline__ void k4_body(LAS char* lds, const K4Dma& D, char* ws, int w, int lane, int rt, int ct STAMP_ARGS) {
    const int wm = w & 3, g = lane >> 4, r = lane & 15;
    constexpr int NP = HASW ? 4 : 2;
    const int xrd = wm * 4096 + lane * 16;
    const int q4 = (lane >> 2) & 3, p4 = lane & 3;
    const int wrd = 16384 + (4 * g + q4) * 224 + J0 * 32 + (p4 >> 1) * 16 + (p4 & 1) * 8;
    f32x4 ag[4][NJ], au[4][NJ];
#pragma unroll
    for (int m = 0; m < 4; ++m)
#pragma unroll
        for (int j = 0; j < NJ; ++j) { ag[m][j] = (f32x4){0.f, 0.f, 0.f, 0.f}; au[m][j] = (f32x4){0.f, 0.f, 0.f, 0.f}; }
    bf16x8 xa[4], xb[4]; s16x4 wgf[2 * NJ], wuf[2 * NJ];
#define K4_RW(i, W, sl, o) W[i] = trd(lds + (sl) + wrd + (o) + ((i) >> 1) * 32 + ((i) & 1) * (16 * 224))
#define K4_RX(i, X, sl) X[i] = ldf(lds + (sl) + xrd + (i) * 1024)
#define K4_MM(i, A, X, W) do { const int j_ = (i) >> 2, m_ = (i) & 3; A[m_][j_] = MFMA16(cat8(W[2 * j_], W[2 * j_ + 1]), X[m_], A[m_][j_]); } while (0)
#define K4_RD1(i, XC, sl) do { if ((i) == 0) K4_RX(0, XC, sl); else if ((i) < 3) K4_RW((i) - 1, wgf, sl, 0); else if ((i) < 6) K4_RX((i) - 2, XC, sl); else if ((i) < 4 + 2 * NJ) K4_RW((i) - 4, wgf, sl, 0); } while (0)
#define K4_H1(XC, XP, DOMMA, DODMA, t, sl, fsl) do { \
        _Pragma("unroll") for (int i_ = 0; i_ < 4 * NJ; ++i_) { \
            if (DOMMA) K4_MM(i_, au, XP, wuf); \
            K4_RD1(i_, XC, sl); \
            if (DODMA) { if (i_ == (NJ == 4 ? 13 : 11)) K4_DMA(0, t, fsl); } \
            SCHED0(); } } while (0)
#define K4_H2(XC, sl, DODMA, t, fsl) do { \
        _Pragma("unroll") for (int i_ = 0; i_ < 4 * NJ; ++i_) { \
            K4_MM(i_, ag, XC, wgf); \
            if (i_ < 2 * NJ) K4_RW(i_, wuf, sl, 7168); \
            if (DODMA) { if (NJ == 4) { if (i_ == 8) K4_DMA(1, t, fsl); if (i_ == 11 && NP == 4) K4_DMA(2, t, fsl); if (i_ == 14 && NP == 4) K4_DMA(3, t, fsl); } \
                         else { if (i_ == 6) K4_DMA(1, t, fsl); if (i_ == 8 && NP == 4) K4_DMA(2, t, fsl); if (i_ == 10 && NP == 4) K4_DMA(3, t, fsl); } } \
            SCHED0(); } } while (0)
    constexpr int NT = DM / 32;
    constexpr int NX = HASW ? 4 : 8;
    WAITBAR(4 + NX);
    STAMP_AT(2);
    K4_H1(xa, xb, false, true, 3, 0, 3 * K4_SLOT);
    K4_H2(xa, 0, true, 3, 3 * K4_SLOT);
    int cur = K4_SLOT, fr = 0;
#define K4_ADV() do { fr = (fr == (K4_NS - 1) * K4_SLOT) ? 0 : fr + K4_SLOT; cur = (cur == (K4_NS - 1) * K4_SLOT) ? 0 : cur + K4_SLOT; } while (0)
    WAITBAR(2 + NX + NP); K4_H1(xb, xa, true, true, 4, cur, fr); K4_H2(xb, cur, true, 4, fr); K4_ADV();
    WAITBAR(NX + 2 * NP); K4_H1(xa, xb, true, true, 5, cur, fr); K4_H2(xa, cur, true, 5, fr); K4_ADV();
    WAITBAR(2 * NP);
    {
        const int tid = w * 64 + lane, row = tid >> 1, hf = tid & 1;
        float s = 0.f, q = 0.f;
#pragma unroll
        for (int i = 0; i < 4; ++i) { const f32x4 v = *(const LAS f32x4*)(lds + K4_STRAW + row * 128 + hf * 64 + i * 16); s += v[0] + v[2]; q += v[1] + v[3]; }
        s += __shfl_xor(s, 1); q += __shfl_xor(q, 1);
        const float mean = s * (1.0f / DM);
        const float var = fmaxf(q * (1.0f / DM) - mean * mean, 0.f);
        if (hf == 0) ((LAS f32x2*)(lds + K4_STAT))[row] = (f32x2){mean, 1.0f / sqrtf(var + LN_EPS)};
        if (tid < 224) { const LAS float* cr = (const LAS float*)(lds + K4_CSRAW) + tid; ((LAS float*)(lds + K4_CS))[tid] = (cr[0] + cr[256]) + (cr[512] + cr[768]); }
        SCHED0();
    }
    K4_H1(xb, xa, true, true, 6, cur, fr); K4_H2(xb, cur, true, 6, fr); K4_ADV();
    int t = 4;
#pragma unroll 1
    for (; t + 4 < NT; t += 2) {
        WAITBAR(2 * NP); K4_H1(xa, xb, true, true, t + 3, cur, fr); K4_H2(xa, cur, true, t + 3, fr); K4_ADV();
        WAITBAR(2 * NP); K4_H1(xb, xa, true, true, t + 4, cur, fr); K4_H2(xb, cur, true, t + 4, fr); K4_ADV();
    }
    WAITBAR(2 * NP); K4_H1(xa, xb, true, true, t + 3, cur, fr); K4_H2(xa, cur, true, t + 3, fr); K4_ADV();
    WAITBAR(2 * NP); K4_H1(xb, xa, true, false, 0, cur, fr); K4_H2(xb, cur, false, 0, fr); K4_ADV();
    WAITBAR(NP);     K4_H1(xa, xb, true, false, 0, cur, fr); K4_H2(xa, cur, false, 0, fr); K4_ADV();
    WAITBAR(0);      K4_H1(xb, xa, true, false, 0, cur, fr); K4_H2(xb, cur, false, 0, fr);
#pragma unroll
    for (int i = 0; i < 4 * NJ; ++i) K4_MM(i, au, xb, wuf);
    STAMP_AT(3);
    const LAS f32x2* st = (const LAS f32x2*)(lds + K4_STAT);
    const LAS float* cs = (const LAS float*)(lds + K4_CS);
    f32x2 ms[4];
#pragma unroll
    for (int m = 0; m < 4; ++m) ms[m] = st[wm * 64 + m * 16 + r];
    const srd_t sra = mk_srd(ws + WS_ACT, SEQ * FF * 2);
    const unsigned ap0 = FRAG(rt * 16 + wm * 4, 0, 112) + lane * 16;
    const int ft0 = ct * 7 + J0;
    constexpr int LONE = (NJ == 3) ? (J0 == 4 ? 2 : 0) : -1;
    u32x2 keep[4];
#pragma unroll
    for (int j = 0; j < NJ; ++j) {
        const f32x4 cg = *(const LAS f32x4*)(cs + (J0 + j) * 16 + 4 * g), cu = *(const LAS f32x4*)(cs + 112 + (J0 + j) * 16 + 4 * g);
        const bool second = (LONE == 0) ? (j == 2) : ((j & 1) == 1);
#pragma unroll
        for (int m = 0; m < 4; ++m) {
            const float c1 = -LOG2E * ms[m].y;
            const f32x4 gz = (ag[m][j] - cg * ms[m].x) * c1;
            const f32x4 uu = (au[m][j] - cu * ms[m].x) * ms[m].y;
            float a[4];
#pragma unroll
            for (int e = 0; e < 4; ++e) a[e] = uu[e] * __builtin_amdgcn_rcpf(1.0f + __builtin_amdgcn_exp2f(gz[e]));
            u32x2 ov; ov.x = pk_bf16(a[0], a[1]); ov.y = pk_bf16(a[2], a[3]);
            const int ft = ft0 + j;
            if (j == LONE) st8(sra, ap0 + FRAG(m, ft >> 1, 112) + (ft & 1) * 8, ov);
            else if (!second) keep[m] = ov;
            else st16(sra, ap0 + FRAG(m, ft >> 1, 112), (u32x4){keep[m].x, keep[m].y, ov.x, ov.y});
        }
        SCHED0();
    }
}

template <bool SYNC>
__device__ __forceinline__ void phase4(LAS char* lds, const Params& P, unsigned tag STAMP_ARGS) {
    const unsigned lds0 = (unsigned)(uintptr_t)lds;
    const int tid = threadIdx.x, lane = tid & 63, w = __builtin_amdgcn_readfirstlane(tid >> 6);
    const int b = blockIdx.x, xcd = b & 7, loc = b >> 3;
    const int rt = (xcd >> 2) * 4 + (loc & 3), ct = (xcd & 3) * 8 + (loc >> 2);
    char* ws = P.ws;
    K4Dma D;
    D.srx = mk_srd(ws + WS_H, SEQ * DM * 2); D.srg = mk_srd(P.wg, DM * FF * 2); D.sru = mk_srd(P.wu, DM * FF * 2);
    D.xv = lane * 16; D.xs0 = FRAG(rt * 16 + w * 2, 0, 32);
    { const int ci = (w < 7 ? w : 6) * 64 + lane; const int row = ci / 14, c = ci % 14; D.gv = (unsigned)row * (FF * 2) + c * 16; }
    D.gs0 = (unsigned)ct * 224u; D.ld0 = lds0 + w * 2048; D.ld1 = lds0 + 16384 + w * 1024;
    if (w < 7) {
#pragma unroll
        for (int t = 0; t < 3; ++t) { K4_DMA(2, t, t * K4_SLOT); K4_DMA(3, t, t * K4_SLOT); }
    }
    if (SYNC) {
        if (w == 7) {
            int idx = -1;
            if (lane < 32) idx = FL_3 + rt * 32 + lane;
            else if (lane < 48) { const int k = lane - 32, gsel = k & 3, rq = k >> 2;
                const int c0 = (ct * 112 + ((gsel & 1) ? 111 : 0)) >> 7, bb = rq * 56 + (gsel >> 1) * 28 + c0;
                idx = FL_2 + (bb >> 5) * 32 + (bb & 7) * 4 + ((bb >> 3) & 3); }
            flags_wait(ws, idx, tag);
        }
        LBAR();
    }
    STAMP_AT(1);
#pragma unroll
    for (int t = 0; t < 3; ++t) { K4_DMA(0, t, t * K4_SLOT); K4_DMA(1, t, t * K4_SLOT); }
    {
        const srd_t srs = mk_srd(ws + WS_ST, SEQ * 128);
#pragma unroll
        for (int i = 0; i < 4; ++i) dma16c(lds0 + K4_STRAW + w * 4096 + i * 1024, lane * 16, srs, (unsigned)(rt * 256 + w * 32) * 128u + i * 1024);
        if (w == 7) {
            const srd_t src = mk_srd(ws + WS_CS, 4 * 7168 * 4);
            const unsigned cv = lane < 28 ? (unsigned)(ct * 112 + 4 * lane) * 4u : lane < 56 ? (unsigned)(3584 + ct * 112 + 4 * (lane - 28)) * 4u : 0x7ff00000u;
#pragma unroll
            for (int i = 0; i < 4; ++i) dma16c(lds0 + K4_CSRAW + i * 1024, cv, src, i * 7168 * 4);
        }
    }
    if ((ct & 1) == 0) {
        if (w < 4) k4_body<4, 0, true>(lds, D, ws, w, lane, rt, ct STAMP_PASS);
        else if (w < 7) k4_body<3, 4, true>(lds, D, ws, w, lane, rt, ct STAMP_PASS);
        else k4_body<3, 4, false>(lds, D, ws, w, lane, rt, ct STAMP_PASS);
    } else {
        if (w < 4) k4_body<3, 0, true>(lds, D, ws, w, lane, rt, ct STAMP_PASS);
        else if (w < 7) k4_body<4, 3, true>(lds, D, ws, w, lane, rt, ct STAMP_PASS);
        else k4_body<4, 3, false>(lds, D, ws, w, lane, rt, ct STAMP_PASS);
    }
    CODE_TOUCH(".Lcw_p4", ".Lcw_p5", 0, 512);
    STAMP_AT(4);
    WAITBAR(0);
    if (SYNC && tid == 0) flag_set(ws, FL_4 + rt * 32 + ct, tag);
}

constexpr int LDS_BYTES = 160 * 1024;
__global__ __launch_bounds__(512, 2) void mega(Params P) {
    extern __shared__ __attribute__((aligned(16))) char smem_g[];
    LAS char* lds = (LAS char*)smem_g;
    char* ws = P.ws;
    const unsigned tag = __hip_atomic_load((gu32*)(ws + WS_FLG) + FL_EPOCH, RLX_AGENT) + 1u;
    LITE_BEGIN()
    asm volatile(".Lcw_p0:");
    { STAMP_DECL STAMP_AT(0); phase1<true>(lds, P, tag STAMP_PASS); STAMP_OUT(1); }
    asm volatile(".Lcw_p1:");
    { STAMP_DECL STAMP_AT(0); phase2<true>(lds, P, tag STAMP_PASS); STAMP_OUT(2); }
    asm volatile(".Lcw_p2:");
    { STAMP_DECL STAMP_AT(0); phase35<0, true>(lds, P, tag STAMP_PASS); STAMP_OUT(3); }
    asm volatile(".Lcw_p3:");
    { STAMP_DECL STAMP_AT(0); phase4<true>(lds, P, tag STAMP_PASS); STAMP_OUT(4); }
    asm volatile(".Lcw_p4:");
    { STAMP_DECL STAMP_AT(0); phase5<true>(lds, P, tag STAMP_PASS); STAMP_OUT(5); }
    asm volatile(".Lcw_p5:");
    LITE_END();
    if (blockIdx.x == 0 && threadIdx.x == 0) __hip_atomic_store((gu32*)(ws + WS_FLG) + FL_EPOCH, tag, RLX_AGENT);
}

extern "C" void kernel_launch(void* const* d_in, const int* in_sizes, int n_in, void* d_out, int out_size, void* d_ws, size_t ws_size, hipStream_t stream) {
    Params P;
    P.x = (const u16*)d_in[0]; P.wqkv = (const u16*)d_in[1]; P.wo = (const u16*)d_in[2];
    P.wg = (const u16*)d_in[4]; P.wu = (const u16*)d_in[5]; P.wd = (const u16*)d_in[6];
    P.out = (u16*)d_out; P.ws = (char*)d_ws;
    (void)hipFuncSetAttribute((const void*)mega, hipFuncAttributeMaxDynamicSharedMemorySize, LDS_BYTES);
    mega<<<256, 512, LDS_BYTES, stream>>>(P);
}
```

```cpp
#include <hip/hip_runtime.h>
#include <hip/hip_bf16.h>
#include <stdint.h>

#define LAS __attribute__((address_space(3)))
typedef unsigned short u16;
typedef short bf16x8 __attribute__((ext_vector_type(8)));
typedef short s16x4 __attribute__((ext_vector_type(4)));
typedef float f32x2 __attribute__((ext_vector_type(2)));
typedef float f32x4 __attribute__((ext_vector_type(4)));
typedef float f32x16 __attribute__((ext_vector_type(16)));
typedef unsigned u32x2 __attribute__((ext_vector_type(2)));
typedef unsigned u32x4 __attribute__((ext_vector_type(4)));
typedef __bf16 bf16x2_t __attribute__((ext_vector_type(2)));
typedef __amdgpu_buffer_rsrc_t srd_t;

constexpr int SEQ = 2048, DM = 1024, NH = 32, FF = 3584;
constexpr float LN_EPS = 1e-5f;
constexpr float QSCALE = 0.17677669529663687f * 1.4426950408889634f;
constexpr float LOG2E = 1.4426950408889634f;

constexpr size_t WS_Q = 0;
constexpr size_t WS_K = WS_Q + (4u << 20);
constexpr size_t WS_V = WS_K + (4u << 20);
constexpr size_t WS_O = WS_V + (4u << 20);
constexpr size_t WS_H = WS_O + (4u << 20);
constexpr size_t WS_ACT = WS_H + (4u << 20);
constexpr size_t WS_ST = WS_ACT + (size_t)SEQ * FF * 2;
constexpr size_t WS_CS = WS_ST + (size_t)SEQ * 16 * 8;
constexpr size_t WS_S1 = WS_CS + 4 * 7168 * 4;
constexpr size_t WS_C1 = WS_S1 + SEQ * 8;
constexpr size_t WS_FLG = WS_C1 + 16 * 3072 * 4;
constexpr size_t WS_XCH = WS_FLG + 16384;
constexpr size_t WS_END = WS_XCH + (8u << 20);
constexpr int FL_EPOCH = 0, FL_ERR = 32, FL_0 = 64, FL_1 = FL_0 + 256, FL_2 = FL_1 + 256, FL_3 = FL_2 + 256, FL_4 = FL_3 + 256, FL_5 = FL_4 + 256;

#define FRAG(mt, kt, KT) (((unsigned)(kt) * 128u + (unsigned)(mt)) * 1024u)
#define ST_AUX 16

__device__ __forceinline__ unsigned pk_bf16(float lo, float hi) { f32x2 v = {lo, hi}; bf16x2_t b = __builtin_convertvector(v, bf16x2_t); return __builtin_bit_cast(unsigned, b); }
__device__ __forceinline__ float bf_lo(unsigned u) { return __uint_as_float(u << 16); }
__device__ __forceinline__ float bf_hi(unsigned u) { return __uint_as_float(u & 0xffff0000u); }
__device__ __forceinline__ s16x4 trd(LAS const char* p) { return __builtin_bit_cast(s16x4, __builtin_amdgcn_ds_read_tr16_b64_v4i16((LAS s16x4*)p)); }
__device__ __forceinline__ bf16x8 cat8(s16x4 a, s16x4 b) { return (bf16x8){a[0], a[1], a[2], a[3], b[0], b[1], b[2], b[3]}; }
__device__ __forceinline__ bf16x8 ldf(LAS const char* p) { return *(LAS const bf16x8*)p; }
__device__ __forceinline__ srd_t mk_srd(const void* p, unsigned bytes) { return __builtin_amdgcn_make_buffer_rsrc((void*)p, (short)0, (int)bytes, 0x00020000); }
__device__ __forceinline__ void dma16(unsigned ldsa, unsigned voff, srd_t srd, unsigned soff) {
    asm volatile("s_mov_b32 m0, %0\n\ts_nop 0\n\tbuffer_load_dwordx4 %1, %2, %3 offen lds" :: "s"(ldsa), "v"(voff), "s"(srd), "s"(soff));
}
__device__ __forceinline__ void dma16c(unsigned ldsa, unsigned voff, srd_t srd, unsigned soff) {
    asm volatile("s_mov_b32 m0, %0\n\ts_nop 0\n\tbuffer_load_dwordx4 %1, %2, %3 offen sc1 lds" :: "s"(ldsa), "v"(voff), "s"(srd), "s"(soff));
}
__device__ __forceinline__ void st8(srd_t srd, unsigned off, u32x2 v) { __builtin_amdgcn_raw_buffer_store_b64(v, srd, off, 0, ST_AUX); }
__device__ __forceinline__ void st16(srd_t srd, unsigned off, u32x4 v) { __builtin_amdgcn_raw_buffer_store_b128(v, srd, off, 0, ST_AUX); }
__device__ __forceinline__ u32x4 pk8(const f32x4 a, const f32x4 b) { u32x4 o; o.x = pk_bf16(a[0], a[1]); o.y = pk_bf16(a[2], a[3]); o.z = pk_bf16(b[0], b[1]); o.w = pk_bf16(b[2], b[3]); return o; }
#define MFMA16(a, b, c) __builtin_amdgcn_mfma_f32_16x16x32_bf16(a, b, c, 0, 0, 0)
#define MFMA32(a, b, c) __builtin_amdgcn_mfma_f32_32x32x16_bf16(a, b, c, 0, 0, 0)
#define WAITVL(n) asm volatile("s_waitcnt vmcnt(%0) lgkmcnt(0)" :: "n"(n) : "memory")
#define WAITBAR(n) asm volatile("s_waitcnt vmcnt(%0) lgkmcnt(0)\n\ts_barrier" :: "n"(n) : "memory")
#define LBAR() asm volatile("s_waitcnt lgkmcnt(0)\n\ts_barrier" ::: "memory")
#define SCHED0() __builtin_amdgcn_sched_barrier(0)
#define CODE_TOUCH(A, B, t0, nthr) do { unsigned long long pc_; int lo_, hi_; unsigned dm_ = 0u; \
        asm volatile("s_getpc_b64 %0\n.Lct%=:\n\ts_mov_b32 %1, " A "-.Lct%=\n\ts_mov_b32 %2, " B "-.Lct%=" : "=&s"(pc_), "=&s"(lo_), "=&s"(hi_)); \
        for (int o_ = lo_ + ((int)threadIdx.x - (t0)) * 128; o_ < hi_; o_ += (nthr) * 128) asm volatile("global_load_dword %0, %1, off" : "+v"(dm_) : "v"((const char*)pc_ + o_)); \
        asm volatile("s_waitcnt vmcnt(0)" : "+v"(dm_) :: "memory");        \
        } while (0)
#define CODE_TOUCH_ASYNC(A, B) do { unsigned long long pc_; int lo_, hi_; \
        asm volatile("s_getpc_b64 %0\n.Lct%=:\n\ts_mov_b32 %1, " A "-.Lct%=\n\ts_mov_b32 %2, " B "-.Lct%=" : "=&s"(pc_), "=&s"(lo_), "=&s"(hi_)); \
        const srd_t sc_ = mk_srd((const char*)pc_ + lo_, (unsigned)(hi_ - lo_)); \
        for (int o_ = 0; o_ < hi_ - lo_; o_ += 512 * 128) \
            asm volatile("s_mov_b32 m0, %0\n\ts_nop 0\n\tbuffer_load_dword %1, %2, %3 offen lds" :: "s"((unsigned)(uintptr_t)lds + 163584u), "v"(threadIdx.x * 128u), "s"(sc_), "s"(o_)); \
        } while (0)
__device__ __forceinline__ void line_touch(LAS char* lds, unsigned voff, srd_t srd) {
    asm volatile("s_mov_b32 m0, %0\n\ts_nop 0\n\tbuffer_load_dword %1, %2, 0 offen sc1 lds" :: "s"((unsigned)(uintptr_t)lds + 163584u), "v"(voff), "s"(srd));
}
#define WAITV(n) asm volatile("s_waitcnt vmcnt(%0)" :: "n"(n) : "memory")
#define RLX_AGENT __ATOMIC_RELAXED, __HIP_MEMORY_SCOPE_AGENT
typedef __attribute__((address_space(1))) unsigned gu32;
typedef __attribute__((address_space(1))) unsigned long long gu64;
struct Params { const u16 *x, *wqkv, *wo, *wg, *wu, *wd; u16* out; char* ws; };
__device__ __forceinline__ void flag_set(char* ws, int idx, unsigned tag) { __hip_atomic_store((gu32*)(ws + WS_FLG) + idx, tag, RLX_AGENT); }
__device__ __forceinline__ void flags_wait(char* ws, int idx, unsigned tag) {
    gu32* fl = (gu32*)(ws + WS_FLG);
    const unsigned long long t0 = __builtin_amdgcn_s_memrealtime();
    for (unsigned spins = 1;; ++spins) {
        const unsigned v = idx >= 0 ? __hip_atomic_load(fl + idx, RLX_AGENT) : tag;
        if (__all(v == tag)) break;
        if ((spins & 63u) == 0u) {
            if (__any(__hip_atomic_load(fl + FL_ERR, RLX_AGENT) == tag)) break;
            if (__builtin_amdgcn_s_memrealtime() - t0 > 2000000ull) { __hip_atomic_store(fl + FL_ERR, tag, RLX_AGENT); break; }
        }
        __builtin_amdgcn_s_sleep(1);
    }
    asm volatile("" ::: "memory");
}
template <int CTRL, int RM> __device__ __forceinline__ float dpp_add(float v) { return v + __int_as_float(__builtin_amdgcn_update_dpp(0, __float_as_int(v), CTRL, RM, 0xf, true)); }
__device__ __forceinline__ float sum32(float v) { v = dpp_add<0x111, 0xf>(v); v = dpp_add<0x112, 0xf>(v); v = dpp_add<0x114, 0xf>(v); v = dpp_add<0x118, 0xf>(v); return dpp_add<0x142, 0xa>(v); }
__device__ __forceinline__ void wait_mask(char* ws, int idx, unsigned tag, unsigned long long need, unsigned long long& rdy) {
    if ((rdy & need) == need) return;
    gu32* fl = (gu32*)(ws + WS_FLG);
    const unsigned long long t0 = __builtin_amdgcn_s_memrealtime();
    for (unsigned spins = 1;; ++spins) {
        const unsigned v = idx >= 0 ? __hip_atomic_load(fl + idx, RLX_AGENT) : ~tag;
        rdy = __ballot(v == tag);
        if ((rdy & need) == need) break;
        if ((spins & 63u) == 0u) {
            if (__any(__hip_atomic_load(fl + FL_ERR, RLX_AGENT) == tag)) break;
            if (__builtin_amdgcn_s_memrealtime() - t0 > 2000000ull) { __hip_atomic_store(fl + FL_ERR, tag, RLX_AGENT); break; }
        }
        __builtin_amdgcn_s_sleep(1);
    }
    asm volatile("" ::: "memory");
}
__device__ __forceinline__ u32x4 ldc16(srd_t srd, unsigned off) { return __builtin_amdgcn_raw_buffer_load_b128(srd, off, 0, 16); }
__device__ __forceinline__ float ldc4(srd_t srd, unsigned off) { return __uint_as_float(__builtin_amdgcn_raw_buffer_load_b32(srd, off, 0, 16)); }

constexpr size_t WS_LITE = (size_t)80 << 20;
#define LITE_BEGIN()
#define LITE_END()
#define STAMP_DECL
#define STAMP_AT(i)
#define STAMP_OUT(kid)
#define STAMP_ARGS
#define STAMP_PASS

constexpr int K1_SLOT = 40960, K1_NS = 4, K1_AUX = 0;
template <bool SYNC>
__device__ __forceinline__ void phase1(LAS char* lds, const Params& P, unsigned tag STAMP_ARGS) {
    const unsigned lds0 = (unsigned)(uintptr_t)lds;
    const int tid = threadIdx.x, lane = tid & 63, w = __builtin_amdgcn_readfirstlane(tid >> 6), g = lane >> 4, r = lane & 15;
    const int b = blockIdx.x, xcd = b & 7, loc = b >> 3;
        const int p = xcd * 2 + (loc & 1), rt = loc >> 1;
    char* ws = P.ws;
    constexpr int NT = DM / 64;
    LAS f32x2* stat = (LAS f32x2*)(lds + K1_AUX);
    LAS float* csl = (LAS float*)(lds + K1_AUX + 1024);
    if (__builtin_expect(w >= 4, 1)) {
        const srd_t srx = mk_srd(P.x, SEQ * DM * 2), srw = mk_srd(P.wqkv, DM * 3 * DM * 2);
        const int j = w - 4, rr = lane >> 3, c8 = lane & 7;
        unsigned xv[2], wv[2];
#pragma unroll
        for (int i = 0; i < 2; ++i) {
            xv[i] = rr * 2048 + ((c8 ^ (4 * i + (rr >> 1))) * 16);
            const int f = ((rr >> 1) & 1) | (i << 1);
            wv[i] = rr * 6144 + (((c8 >> 1) ^ f) * 32) + (c8 & 1) * 16;
        }
        const unsigned xs0 = (unsigned)(rt * 128 + 32 * j) * 2048u, ws0 = (unsigned)p * 128u + (unsigned)(16 * j) * 6144u;
        const unsigned xl0 = lds0 + j * 4096, wl0 = lds0 + 16384 + j * 2048;
#define K1_DMA(i, t, slot) do { if ((i) < 4) dma16(xl0 + (slot) + (i) * 1024, xv[(i) & 1], srx, xs0 + (i) * (8 * 2048) + (((t) + rt) & 15) * 128); \
        else dma16(wl0 + (slot) + (((i) - 4) >> 1) * 8192 + (((i) - 4) & 1) * 1024, wv[((i) - 4) & 1], srw, ws0 + (((i) - 4) >> 1) * 2048 + (((i) - 4) & 1) * (8 * 6144) + (((t) + rt) & 15) * (64 * 6144)); } while (0)
#define K1_STAGE(t, slot) do { _Pragma("unroll") for (int i_ = 0; i_ < 10; ++i_) K1_DMA(i_, t, slot); } while (0)
        const int srow = 8 * b + 2 * j + (lane >> 5);
        u32x4 xr[4];
        { const unsigned xo = (unsigned)srow * 2048u + (lane & 31) * 16;
#pragma unroll
          for (int k = 0; k < 4; ++k) asm volatile("buffer_load_dwordx4 %0, %1, %2, 0 offen offset:%3" : "=v"(xr[k]) : "v"(xo), "s"(srx), "n"(k * 512)); }
        K1_STAGE(0, 0); K1_STAGE(1, K1_SLOT); K1_STAGE(2, 2 * K1_SLOT);
        WAITBAR(20);
        asm volatile("" : "+v"(xr[0]), "+v"(xr[1]), "+v"(xr[2]), "+v"(xr[3]));
        K1_STAGE(3, 3 * K1_SLOT);
        {
            const unsigned x00 = xr[0][0];
            const float pv = bf_lo(lane < 32 ? __builtin_amdgcn_readlane(x00, 0) : __builtin_amdgcn_readlane(x00, 32));
            float s = 0.f, q = 0.f;
#pragma unroll
            for (int k = 0; k < 4; ++k)
#pragma unroll
                for (int e = 0; e < 4; ++e) { const unsigned d = xr[k][e]; const float u = bf_lo(d) - pv, v = bf_hi(d) - pv; s += u + v; q += u * u + v * v; }
            s = sum32(s); q = sum32(q);
            const float dm = s * (1.0f / DM), var = fmaxf(q * (1.0f / DM) - dm * dm, 0.f);
            const u32x2 sv = {__float_as_uint(pv + dm), __float_as_uint(1.0f / sqrtf(var + LN_EPS))};
            __builtin_amdgcn_raw_buffer_store_b64(sv, mk_srd(ws + WS_S1, SEQ * 8), (lane & 31) == 31 ? (unsigned)srow * 8u : 0x7ff00000u, 0, 16);
        }
        WAITBAR(21); K1_STAGE(4, 0);
        WAITBAR(21); K1_STAGE(5, K1_SLOT);
        WAITBAR(21); K1_STAGE(6, 2 * K1_SLOT);
        WAITBAR(20);
        if (SYNC && tid == 256) flag_set(ws, FL_0 + b, tag);
        K1_STAGE(7, 3 * K1_SLOT);
        WAITBAR(20); K1_STAGE(8, 0);
        WAITBAR(20); K1_STAGE(9, K1_SLOT);
        WAITBAR(20); K1_STAGE(10, 2 * K1_SLOT);
        WAITBAR(20);
        const int fidx = lane < 16 ? FL_0 + 16 * rt + lane : lane < 32 ? FL_0 + ((lane - 16) * 2 + (p & 1)) * 8 + (p >> 1) : -1;
        unsigned fseen;
        asm volatile("global_load_dword %0, %1, off sc1" : "=v"(fseen) : "v"((const char*)(ws + WS_FLG) + (fidx >= 0 ? fidx : FL_0 + b) * 4));
        K1_STAGE(11, 3 * K1_SLOT);
        WAITBAR(21); K1_STAGE(12, 0);
        WAITBAR(21); K1_STAGE(13, K1_SLOT);
        WAITBAR(20);
        asm volatile("" : "+v"(fseen));
        K1_STAGE(14, 2 * K1_SLOT);
        if (SYNC && !__all(fseen == tag)) flags_wait(ws, fidx, tag);
        u32x4 gs, gp[16];
        const int tc = 48 * j + 4 * lane;
        { const srd_t srs = mk_srd(ws + WS_S1, SEQ * 8), src1 = mk_srd(ws + WS_C1, 16 * 3072 * 4);
          const unsigned so = j == 0 ? (unsigned)(rt * 128) * 8u + lane * 16 : 0x7ff00000u, po = lane < 12 ? (unsigned)((tc >> 6) * 1024 + p * 64 + (tc & 63)) * 4u : 0x7ff00000u;
          asm volatile("buffer_load_dwordx4 %0, %1, %2, 0 offen sc1" : "=v"(gs) : "v"(so), "s"(srs));
#pragma unroll
          for (int i = 0; i < 16; ++i) asm volatile("buffer_load_dwordx4 %0, %1, %2, %3 offen sc1" : "=v"(gp[i]) : "v"(po), "s"(src1), "s"(i * 3072 * 4)); }
        WAITBAR(37); K1_STAGE(15, 3 * K1_SLOT);
        WAITBAR(37);
        WAITBAR(10);
        asm volatile("" : "+v"(gs), "+v"(gp[0]), "+v"(gp[1]), "+v"(gp[2]), "+v"(gp[3]), "+v"(gp[4]), "+v"(gp[5]), "+v"(gp[6]), "+v"(gp[7]));
        asm volatile("" : "+v"(gp[8]), "+v"(gp[9]), "+v"(gp[10]), "+v"(gp[11]), "+v"(gp[12]), "+v"(gp[13]), "+v"(gp[14]), "+v"(gp[15]));
        {
            f32x4 v = {0.f, 0.f, 0.f, 0.f};
#pragma unroll
            for (int i = 0; i < 16; ++i) v += __builtin_bit_cast(f32x4, gp[i]);
            if (lane < 12) *(LAS f32x4*)(csl + tc) = v;
            if (j == 0) *(LAS u32x4*)(lds + K1_AUX + lane * 16) = gs;
        }
        WAITBAR(0);
    } else {
        const int wr = w >> 1, wc = w & 1;
#define CWV 2
        CODE_TOUCH(".Lcw_p0", ".Lcw_p2", 0, 256); CODE_TOUCH(".Lcw_p5", ".Lcw_end", 0, 256);
        int xrd[2], wrd[2];
        { const int q4 = (lane >> 2) & 3, p4 = lane & 3, trow = 8 * g + q4, f = ((trow >> 1) & 1) | (((trow >> 3) & 1) << 1);
#pragma unroll
          for (int s = 0; s < 2; ++s) { xrd[s] = (64 * wr + r) * 128 + (((4 * s + g) ^ ((r >> 1) & 7)) * 16); wrd[s] = 16384 + trow * 128 + (((2 * wc + s) ^ f) * 32) + (p4 >> 1) * 16 + (p4 & 1) * 8; } }
        f32x4 acc[4][6];
#pragma unroll
        for (int m = 0; m < 4; ++m)
#pragma unroll
            for (int n = 0; n < 6; ++n) acc[m][n] = (f32x4){0.f, 0.f, 0.f, 0.f};
        bf16x8 xa[4], xb[4]; s16x4 wa[12], wb[12];
#define K1_RW(i, W, sl, s) W[i] = trd(lds + (sl) + wrd[((i) >> 1) & 1] + ((i) >> 2) * 8192 + (s) * 4096 + ((i) & 1) * 512)
#define K1_RX(i, X, sl, s) X[i] = ldf(lds + (sl) + xrd[s] + (i) * 2048)
#define K1_RD(i, X, W, sl, s) do { if ((i) == 2) K1_RX(0, X, sl, s); else if ((i) == 7) K1_RX(1, X, sl, s); else if ((i) == 12) K1_RX(2, X, sl, s); else if ((i) == 15) K1_RX(3, X, sl, s); \
        else K1_RW((i) < 2 ? (i) : (i) < 7 ? (i) - 1 : (i) < 12 ? (i) - 2 : (i) - 3, W, sl, s); } while (0)
#define K1_MM(i, X, W) do { const int m_ = (i) / 6, n_ = (i) % 6; const bf16x8 wf_ = cat8(W[2 * n_], W[2 * n_ + 1]); \
        acc[m_][n_] = (n_ < 4) ? MFMA16(wf_, X[m_], acc[m_][n_]) : MFMA16(X[m_], wf_, acc[m_][n_]); } while (0)
#define K1_HALF(XM, WM, XR, WR, sl, s, DOMMA) do { \
        _Pragma("unroll") for (int i_ = 0; i_ < 24; ++i_) { \
            if (DOMMA) K1_MM(i_, XM, WM); \
            if (i_ < 16) K1_RD(i_, XR, WR, sl, s); \
            SCHED0(); } } while (0)
        LBAR();
        STAMP_AT(2);
        K1_HALF(xb, wb, xa, wa, 0, 0, false);
        K1_HALF(xa, wa, xb, wb, 0, 1, true);
        if (wr == 0) {
            bf16x8 one8;
#pragma unroll
            for (int e = 0; e < 8; ++e) one8[e] = (short)0x3f80;
            const srd_t src1 = mk_srd(ws + WS_C1, 16 * 3072 * 4);
#pragma unroll
            for (int n = 0; n < 6; ++n) {
                f32x4 c = MFMA16(cat8(wa[2 * n], wa[2 * n + 1]), one8, ((f32x4){0.f, 0.f, 0.f, 0.f}));
                c = MFMA16(cat8(wb[2 * n], wb[2 * n + 1]), one8, c);
                __builtin_amdgcn_raw_buffer_store_b128(__builtin_bit_cast(u32x4, c), src1, r == 0 ? (unsigned)(rt * 3072 + (n >> 1) * 1024 + p * 64 + wc * 32 + (n & 1) * 16 + 4 * g) * 4u : 0x7ff00000u, 0, 16);
            }
        }
        int cur = K1_SLOT;
        LBAR();
        K1_HALF(xb, wb, xa, wa, cur, 0, true);
        K1_HALF(xa, wa, xb, wb, cur, 1, true);
        cur = 2 * K1_SLOT;
        LBAR();
        K1_HALF(xb, wb, xa, wa, cur, 0, true);
        K1_HALF(xa, wa, xb, wb, cur, 1, true);
        cur = 3 * K1_SLOT;
        WAITBAR(0);
        K1_HALF(xb, wb, xa, wa, cur, 0, true);
        K1_HALF(xa, wa, xb, wb, cur, 1, true);
        cur = 0;
#pragma unroll 1
        for (int t = 4; t < NT; ++t) {
            LBAR();
            K1_HALF(xb, wb, xa, wa, cur, 0, true);
            K1_HALF(xa, wa, xb, wb, cur, 1, true);
            cur = (cur == 3 * K1_SLOT) ? 0 : cur + K1_SLOT;
        }
#pragma unroll
        for (int i = 0; i < 24; ++i) K1_MM(i, xb, wb);
        STAMP_AT(3);
        const int head = 2 * p + wc;
        const srd_t srq = mk_srd(ws + WS_Q, 12u << 20);
        f32x4 cq[4]; float cv[2];
#pragma unroll
        for (int n = 0; n < 4; ++n) cq[n] = *(LAS const f32x4*)(csl + (n >> 1) * 64 + wc * 32 + (n & 1) * 16 + 4 * g);
#pragma unroll
        for (int n = 0; n < 2; ++n) cv[n] = csl[128 + wc * 32 + n * 16 + r];
        const int rho = 16 * (r >> 3) + 4 * ((r >> 2) & 1) + (r & 3);
        f32x4 vv[2][2];
#pragma unroll
        for (int m = 0; m < 4; ++m) {
            const int lrow = wr * 64 + m * 16, row0 = rt * 128 + lrow;
            const unsigned blk = ((unsigned)head * 64 + (row0 >> 5)) * 2048u + (g >> 1) * 1024 + (g & 1) * 512;
            const f32x2 st = stat[lrow + r];
            const float qs = st.y * QSCALE;
            st16(srq, (4u << 20) + blk + (rho + (m & 1) * 8) * 16, pk8((acc[m][2] - st.x * cq[2]) * st.y, (acc[m][3] - st.x * cq[3]) * st.y));
            const f32x4 s01 = *(LAS const f32x4*)(stat + lrow + 4 * g), s23 = *(LAS const f32x4*)(stat + lrow + 4 * g + 2);
            const f32x4 mean4 = {s01[0], s01[2], s23[0], s23[2]}, rstd4 = {s01[1], s01[3], s23[1], s23[3]};
#pragma unroll
            for (int n = 0; n < 2; ++n) vv[m & 1][n] = (acc[m][4 + n] - mean4 * cv[n]) * rstd4;
            if (m & 1) {
#pragma unroll
                for (int n = 0; n < 2; ++n) st16(srq, (8u << 20) + blk + (16 * n + r) * 16, pk8(vv[0][n], vv[1][n]));
            }
            st16(srq, blk + ((m & 1) * 16 + r) * 16, pk8((acc[m][0] - st.x * cq[0]) * qs, (acc[m][1] - st.x * cq[1]) * qs));
        }
        asm volatile(".Lcw_end:");
    }
    STAMP_AT(4);
    WAITBAR(0);
    if (SYNC && tid == 0) flag_set(ws, FL_1 + p * 16 + rt, tag);
    if (w < 6) line_touch(lds, (unsigned)(w >> 1) * (4u << 20) + ((unsigned)(2 * p + (w & 1)) * 64 + rt * 4) * 2048u + lane * 128, mk_srd(ws + WS_Q, 12u << 20));
    CODE_TOUCH_ASYNC(".Lcw_p1", ".Lcw_p2");
}

constexpr int K2_SLOT = 32768, K2_SIDE = 3 * K2_SLOT;
struct K2Side { srd_t src, srp, sro; unsigned cv, cs0, ps0; };
template <int KH>
__device__ __forceinline__ void attn_fast(LAS char* lds, srd_t srk, srd_t srv, unsigned hbase, const K2Side& S, int w, int lane, const bf16x8 (&qf)[2][2], f32x16 (&o)[2], f32x4 (&lacc)[2], f32x4& cs) {
    const int tl = w & 3;
    const unsigned lds0 = (unsigned)(uintptr_t)lds;
    const unsigned lv = lane * 16;
    const unsigned s0 = hbase + (unsigned)(KH * 32 + tl) * 2048u, d0 = lds0 + KH * 16384 + tl * 2048;
    const int rd0 = KH * 16384 + lane * 16;
    bf16x8 ones, sel;
    { const short v = ((lane & 15) == ((lane >> 4) & 1)) ? (short)0x3f80 : (short)0;
#pragma unroll
      for (int j = 0; j < 8; ++j) { ones[j] = v; sel[j] = ((lane & 15) == j) ? (short)0x3f80 : (short)0; } }
    const unsigned sd = lds0 + K2_SIDE + w * 1024;
#define K2_DMA(k, rd, fr) do { \
        if ((k) < 4) dma16c(d0 + (fr) + ((k) >> 1) * 8192 + ((k) & 1) * 1024, lv, (k) < 2 ? srk : srv, s0 + ((rd) + 2) * 8192 + ((k) & 1) * 1024); \
        else if ((k) == 4) dma16(sd + sc, S.cv, S.src, S.cs0 + (rd) * (4 * FF * 2)); \
        else dma16(sd + 24576, lv, (rd) < 4 ? S.srp : S.sro, (rd) < 4 ? S.ps0 + (rd) * 1024 : (rd) == 4 ? (S.ps0 >> 2) : 0x7ff00000u); } while (0)
#define K2_HOOK(gi, rd, fr) do { if (((gi) >> 1) < 6 && ((gi) & 1) == KH) K2_DMA((gi) >> 1, rd, fr); } while (0)
    f32x16 sa[2], sb[2];
    const f32x16 zero16 = {};
    bf16x8 k0 = ldf(lds + rd0), k1 = ldf(lds + rd0 + 1024), v0, v1;
#pragma unroll
    for (int qt = 0; qt < 2; ++qt) { sa[qt] = MFMA32(k0, qf[qt][0], zero16); sa[qt] = MFMA32(k1, qf[qt][1], sa[qt]); }
    k0 = ldf(lds + rd0 + 2048);
    u32x4 pa = {0u, 0u, 0u, 0u}, pb = {0u, 0u, 0u, 0u}; float e6 = 0.f, e7 = 0.f;
    v1 = k1;
    bf16x8 csd = {};
#define EX2(x) __builtin_amdgcn_exp2f(x)
#define K2_GROUP(S_, B, PC, PP, QK, VP, QP, RD, gi) do { \
        QK; PP[3] = pk_bf16(e6, e7); SCHED0(); \
        const float x0 = EX2(S_[B + 0]), x1 = EX2(S_[B + 1]), x2 = EX2(S_[B + 2]); RD; SCHED0(); \
        o[QP] = MFMA32(VP, __builtin_bit_cast(bf16x8, PP), o[QP]); SCHED0(); \
        const float x3 = EX2(S_[B + 3]), x4 = EX2(S_[B + 4]), x5 = EX2(S_[B + 5]); PC[0] = pk_bf16(x0, x1); SCHED0(); \
        lacc[QP] = MFMA16(ones, __builtin_bit_cast(bf16x8, PP), lacc[QP]); K2_HOOK(gi, rd, fr); SCHED0(); \
        e6 = EX2(S_[B + 6]); e7 = EX2(S_[B + 7]); PC[1] = pk_bf16(x2, x3); PC[2] = pk_bf16(x4, x5); SCHED0(); } while (0)
#define K2_TILE(SC, SN, kn1, kn2, va, ti) do { \
        K2_GROUP(SC[0], 0, pa, pb, SN[0] = MFMA32(k0, qf[0][0], zero16), v1, 1, v0 = ldf(va), 4 * (ti)); \
        K2_GROUP(SC[1], 0, pb, pa, SN[1] = MFMA32(k0, qf[1][0], zero16), v0, 0, k1 = ldf((kn1) + 1024), 4 * (ti) + 1); \
        K2_GROUP(SC[0], 8, pa, pb, SN[0] = MFMA32(k1, qf[0][1], SN[0]), v0, 1, v1 = ldf((va) + 1024), 4 * (ti) + 2); \
        K2_GROUP(SC[1], 8, pb, pa, SN[1] = MFMA32(k1, qf[1][1], SN[1]), v1, 0, k0 = ldf(kn2), 4 * (ti) + 3); } while (0)
    int cur = 0, fr = 2 * K2_SLOT, sc = 0;
#pragma unroll 1
    for (int rd = 0; rd < 8; ++rd) {
        const int nx = (cur == 2 * K2_SLOT) ? 0 : cur + K2_SLOT;
        const int sn = (sc == 16384) ? 0 : sc + 8192;
        if (rd > 0) WAITBAR(2);
        LAS const char* pc = lds + cur + rd0; LAS const char* pn = lds + nx + rd0;
        K2_TILE(sa, sb, pc + 2048, pc + 4096, pc + 8192, 0);
        csd = ldf(lds + K2_SIDE + sn + w * 1024 + lane * 16);
        if (rd < 2) csd = (bf16x8){};
        K2_TILE(sb, sa, pc + 4096, pc + 6144, pc + 8192 + 2048, 1);
        cs = MFMA16(sel, csd, cs);
        K2_TILE(sa, sb, pc + 6144, pn, pc + 8192 + 4096, 2);
        K2_TILE(sb, sa, pn, pn + 2048, pc + 8192 + 6144, 3);
        fr = cur; cur = nx; sc = sn;
    }
    pb[3] = pk_bf16(e6, e7);
    o[1] = MFMA32(v1, __builtin_bit_cast(bf16x8, pb), o[1]);
    lacc[1] = MFMA16(ones, __builtin_bit_cast(bf16x8, pb), lacc[1]);
    WAITBAR(0);
    cs = MFMA16(sel, ldf(lds + K2_SIDE + w * 1024 + lane * 16), cs);
    cs = MFMA16(sel, ldf(lds + K2_SIDE + 8192 + w * 1024 + lane * 16), cs);
#undef K2_GROUP
#undef K2_TILE
#undef K2_DMA
#undef K2_HOOK
}
#define K2_STAGE(rd, slot) do { dma16c(d0 + (slot), lv, srk, s0 + (rd) * 8192); dma16c(d0 + (slot) + 1024, lv, srk, s0 + (rd) * 8192 + 1024); \
        dma16c(d0 + (slot) + 8192, lv, srv, s0 + (rd) * 8192); dma16c(d0 + (slot) + 9216, lv, srv, s0 + (rd) * 8192 + 1024); } while (0)
__device__ __forceinline__ void attn_safe(LAS char* lds, srd_t srk, srd_t srv, unsigned hbase, const bf16x8 (&qf)[2][2], int w, int lane, f32x16 (&o)[2], float (&mrun)[2], float (&lrun)[2]) {
    const int kh = w >> 2, tl = w & 3;
    const unsigned lds0 = (unsigned)(uintptr_t)lds;
    const unsigned lv = lane * 16;
    const unsigned s0 = hbase + (unsigned)(kh * 32 + tl) * 2048u, d0 = lds0 + kh * 16384 + tl * 2048;
    const int rd0 = kh * 16384 + lane * 16;
    const f32x16 zero16 = {};
#pragma unroll 1
    for (int rd = 0; rd < 8; ++rd) {
        K2_STAGE(rd, 0);
        WAITBAR(0);
#pragma unroll 1
        for (int i = 0; i < 4; ++i) {
            LAS const char* pk = lds + rd0 + i * 2048;
            const bf16x8 k0 = ldf(pk), k1 = ldf(pk + 1024), v0 = ldf(pk + 8192), v1 = ldf(pk + 9216);
#pragma unroll
            for (int qt = 0; qt < 2; ++qt) {
                f32x16 sc = MFMA32(k0, qf[qt][0], zero16); sc = MFMA32(k1, qf[qt][1], sc);
                float mx = sc[0];
#pragma unroll
                for (int j = 1; j < 16; ++j) mx = fmaxf(mx, sc[j]);
                mx = fmaxf(mx, __shfl_xor(mx, 32));
                const float mn = fmaxf(mrun[qt], mx);
                const float ms = (mn == -INFINITY) ? 0.f : mn;
                const float al = __builtin_amdgcn_exp2f(mrun[qt] - ms);
                mrun[qt] = mn;
                u32x4 p0, p1;
#pragma unroll
                for (int j = 0; j < 4; ++j) { p0[j] = pk_bf16(__builtin_amdgcn_exp2f(sc[2 * j] - ms), __builtin_amdgcn_exp2f(sc[2 * j + 1] - ms));
                                              p1[j] = pk_bf16(__builtin_amdgcn_exp2f(sc[8 + 2 * j] - ms), __builtin_amdgcn_exp2f(sc[9 + 2 * j] - ms)); }
                float s_ = 0.f;
#pragma unroll
                for (int j = 0; j < 4; ++j) { const unsigned a = p0[j], c = p1[j]; s_ += bf_lo(a) + bf_hi(a); s_ += bf_lo(c) + bf_hi(c); }
                lrun[qt] = lrun[qt] * al + s_;
#pragma unroll
                for (int j = 0; j < 16; ++j) o[qt][j] *= al;
                o[qt] = MFMA32(v0, __builtin_bit_cast(bf16x8, p0), o[qt]); o[qt] = MFMA32(v1, __builtin_bit_cast(bf16x8, p1), o[qt]);
            }
        }
        WAITBAR(0);
    }
}

template <bool SYNC>
__device__ __forceinline__ void phase2(LAS char* lds, const Params& P, unsigned tag STAMP_ARGS) {
    const int tid = threadIdx.x, lane = tid & 63, w = __builtin_amdgcn_readfirstlane(tid >> 6);
    const int b = blockIdx.x, xcd = b & 7, loc = b >> 3;
    const int head = xcd * 4 + (loc & 3), qb = loc >> 2;
    char* ws = P.ws;
    K2Side S;
    const int cgrp = b % 56, rq = b / 56;
    const bool do_cs = b < 224;
    S.src = mk_srd(cgrp < 28 ? P.wg : P.wu, DM * FF * 2);
    S.cv = (unsigned)(lane >> 4) * (FF * 2) + (lane & 15) * 16;
    S.cs0 = do_cs ? (unsigned)(rq * 256 + w * 32) * (FF * 2) + (unsigned)(cgrp < 28 ? cgrp : cgrp - 28) * 256u : 0x7ff00000u;
    S.srp = mk_srd(P.wd, FF * DM * 2); S.sro = mk_srd(P.wo, DM * DM * 2);
    S.ps0 = (unsigned)(b * 8 + w) * 4096u;
    const srd_t srk = mk_srd(ws + WS_K, SEQ * DM * 2), srv = mk_srd(ws + WS_V, SEQ * DM * 2);
    const unsigned hbase = (unsigned)head * 64u * 2048u;
    const int kh = w >> 2;
    if (SYNC) { if (w == 0) flags_wait(ws, lane < 16 ? FL_1 + (head >> 1) * 16 + lane : -1, tag); LBAR(); }
    STAMP_AT(1);
    bf16x8 qf[2][2];
    {
        const unsigned lds0 = (unsigned)(uintptr_t)lds, lv = lane * 16;
        const unsigned s0 = hbase + (unsigned)(kh * 32 + (w & 3)) * 2048u, d0 = lds0 + kh * 16384 + (w & 3) * 2048;
        K2_STAGE(0, 0);
        const srd_t srq = mk_srd(ws + WS_Q, SEQ * DM * 2);
        const unsigned qo = ((unsigned)head * 64 + qb * 8 + (w & 3) * 2) * 2048u + lane * 16;
#pragma unroll
        for (int qt = 0; qt < 2; ++qt)
#pragma unroll
            for (int s = 0; s < 2; ++s) qf[qt][s] = __builtin_bit_cast(bf16x8, ldc16(srq, qo + qt * 2048 + s * 1024));
        K2_STAGE(1, K2_SLOT);
    }
    WAITBAR(0);
    STAMP_AT(2);
    f32x16 o[2] = {}; f32x4 lacc[2] = {}; f32x4 cs = {0.f, 0.f, 0.f, 0.f};
    if (kh == 0) attn_fast<0>(lds, srk, srv, hbase, S, w, lane, qf, o, lacc, cs);
    else attn_fast<1>(lds, srk, srv, hbase, S, w, lane, qf, o, lacc, cs);
    STAMP_AT(3);
    LAS float* ob = (LAS float*)lds;
    LAS float* lb = (LAS float*)(lds + 32768);
    LAS unsigned* flag = (LAS unsigned*)(lds + 32768 + 4096);
    LAS float* cb = (LAS float*)(lds + 40960);
    const int hi = lane >> 5, q32 = lane & 31;
    const srd_t sro = mk_srd(ws + WS_O, SEQ * DM * 2);
    const unsigned dst = FRAG(qb * 16 + (w & 3) * 4 + kh * 2 + (q32 >> 4), head, 32) + (hi * 16 + (q32 & 15)) * 16;
    bool bad;
    {
        if (tid == 0) flag[0] = 0u;
        if (lane < 16) { lb[w * 64 + lane] = lacc[0][0]; lb[w * 64 + 16 + lane] = lacc[0][1]; lb[w * 64 + 32 + lane] = lacc[1][0]; lb[w * 64 + 48 + lane] = lacc[1][1]; }
#pragma unroll
        for (int j = 0; j < 4; ++j) { const f32x4 v = kh ? (f32x4){o[0][4 * j], o[0][4 * j + 1], o[0][4 * j + 2], o[0][4 * j + 3]} : (f32x4){o[1][4 * j], o[1][4 * j + 1], o[1][4 * j + 2], o[1][4 * j + 3]};
            *(LAS f32x4*)(ob + ((w * 4 + j) * 64 + lane) * 4) = v; }
        if (lane < 32) *(LAS f32x4*)(cb + w * 128 + (lane & 15) * 8 + (lane >> 4) * 4) = cs;
        LBAR();
        const int pw = w ^ 4;
        f32x16 of;
#pragma unroll
        for (int j = 0; j < 4; ++j) { const f32x4 v = *(LAS const f32x4*)(ob + ((pw * 4 + j) * 64 + lane) * 4);
#pragma unroll
            for (int e = 0; e < 4; ++e) of[4 * j + e] = (kh ? o[1][4 * j + e] : o[0][4 * j + e]) + v[e]; }
        const float l = lb[w * 64 + kh * 32 + q32] + lb[pw * 64 + kh * 32 + q32];
        bool ok = (l >= 0x1p-80f) && (l <= 0x1p+110f);
#pragma unroll
        for (int j = 0; j < 16; ++j) ok = ok && (fabsf(of[j]) <= 0x1p+120f);
        if (!__all(ok)) { if (lane == 0) flag[0] = 1u; }
        if (do_cs && tid < 128) {
            float s = 0.f;
#pragma unroll
            for (int i = 0; i < 8; ++i) s += cb[i * 128 + tid];
            __hip_atomic_store((gu32*)(ws + WS_CS) + (rq * 7168 + cgrp * 128 + tid), __float_as_uint(s), RLX_AGENT);
        }
        LBAR();
        bad = flag[0] != 0u;
        if (!bad) {
            const float rl = 1.0f / l;
#pragma unroll
            for (int c = 0; c < 2; ++c) {
                u32x4 ov; ov.x = pk_bf16(of[8 * c + 0] * rl, of[8 * c + 1] * rl); ov.y = pk_bf16(of[8 * c + 2] * rl, of[8 * c + 3] * rl);
                ov.z = pk_bf16(of[8 * c + 4] * rl, of[8 * c + 5] * rl); ov.w = pk_bf16(of[8 * c + 6] * rl, of[8 * c + 7] * rl);
                st16(sro, dst + c * 512, ov);
            }
        }
    }
    if (bad) {
        WAITBAR(0);
        o[0] = (f32x16){}; o[1] = (f32x16){}; float mrun[2] = {-INFINITY, -INFINITY}, lrun[2] = {0.f, 0.f};
        attn_safe(lds, srk, srv, hbase, qf, w, lane, o, mrun, lrun);
        LAS float* mb = lb + 512;
#pragma unroll
        for (int qt = 0; qt < 2; ++qt) { const float lt = lrun[qt] + __shfl_xor(lrun[qt], 32); if (lane < 32) { lb[w * 64 + qt * 32 + lane] = lt; mb[w * 64 + qt * 32 + lane] = mrun[qt]; } }
#pragma unroll
        for (int j = 0; j < 16; ++j) ob[(w * 16 + j) * 64 + lane] = kh ? o[0][j] : o[1][j];
        WAITBAR(0);
        const int pw = w ^ 4;
        const float ma = mb[w * 64 + kh * 32 + q32], mp = mb[pw * 64 + kh * 32 + q32];
        const float mn = fmaxf(ma, mp), ms = (mn == -INFINITY) ? 0.f : mn;
        const float fa = __builtin_amdgcn_exp2f(ma - ms), fp = __builtin_amdgcn_exp2f(mp - ms);
        const float l = lb[w * 64 + kh * 32 + q32] * fa + lb[pw * 64 + kh * 32 + q32] * fp;
        const float rl = 1.0f / l;
        float of[16];
#pragma unroll
        for (int j = 0; j < 16; ++j) of[j] = ((kh ? o[1][j] : o[0][j]) * fa + ob[(pw * 16 + j) * 64 + lane] * fp) * rl;
#pragma unroll
        for (int c = 0; c < 2; ++c) {
            u32x4 ov; ov.x = pk_bf16(of[8 * c + 0], of[8 * c + 1]); ov.y = pk_bf16(of[8 * c + 2], of[8 * c + 3]);
            ov.z = pk_bf16(of[8 * c + 4], of[8 * c + 5]); ov.w = pk_bf16(of[8 * c + 6], of[8 * c + 7]);
            st16(sro, dst + c * 512, ov);
        }
    }
    STAMP_AT(4);
    WAITBAR(0);
    if (SYNC && tid == 0) flag_set(ws, FL_2 + qb * 32 + head, tag);
    CODE_TOUCH_ASYNC(".Lcw_p2", ".Lcw_p3");
}
#undef K2_STAGE

constexpr int K3_SLOT = 24576, K3_NS = 6, K3_AUX = K3_NS * K3_SLOT;
template <int MODE, bool SYNC>
__device__ __forceinline__ void phase35(LAS char* lds, const Params& P, unsigned tag STAMP_ARGS) {
    constexpr int KD = MODE == 0 ? DM : FF, KT = KD / 32, NT = KD / 64;
    const unsigned lds0 = (unsigned)(uintptr_t)lds;
    const int tid = threadIdx.x, lane = tid & 63, w = __builtin_amdgcn_readfirstlane(tid >> 6), g = lane >> 4, r = lane & 15;
    const int b = blockIdx.x, xcd = b & 7, loc = b >> 3;
    const int rt = (xcd >> 1) * 4 + (loc & 3), ct = (xcd & 1) * 8 + (loc >> 2);
    char* ws = P.ws;
    if (w >= 4) {
        const int j = w - 4;
        const srd_t srx = mk_srd(ws + (MODE == 0 ? WS_O : WS_ACT), SEQ * KD * 2), srw = mk_srd(MODE == 0 ? P.wo : P.wd, KD * DM * 2);
        const unsigned xv = lane * 16, xs0 = FRAG(rt * 8 + 2 * j, 0, KT);
        constexpr unsigned XM = FRAG(1, 0, KT), XK = FRAG(0, 1, KT);
        const int wrow = lane >> 3, wch = (lane & 7) ^ (((wrow >> 1) & 3) << 1);
        const unsigned wv = (unsigned)wrow * 2048u + wch * 16, ws0 = (unsigned)ct * 128u + (unsigned)(16 * j) * 2048u;
        const unsigned ld0 = lds0 + j * 4096, ld1 = lds0 + 16384 + j * 2048;
        const int rot = 2 * xcd, fidx = lane < 32 ? FL_2 + (rt >> 1) * 32 + lane : -1;
        unsigned long long rdy = SYNC ? 0ull : ~0ull;
#define K3_T(t) (((t) + rot) & 15)
#define K3_W(t, slot) do { dma16(ld1 + (slot), wv, srw, ws0 + K3_T(t) * (64 * 2048)); dma16(ld1 + (slot) + 1024, wv, srw, ws0 + 8 * 2048 + K3_T(t) * (64 * 2048)); } while (0)
#define K3_X(t, slot) do { wait_mask(ws, fidx, tag, 3ull << (2 * K3_T(t)), rdy); \
        dma16c(ld0 + (slot), xv, srx, xs0 + K3_T(t) * (2 * XK)); dma16c(ld0 + (slot) + 1024, xv, srx, xs0 + K3_T(t) * (2 * XK) + XK); \
        dma16c(ld0 + (slot) + 2048, xv, srx, xs0 + XM + K3_T(t) * (2 * XK)); dma16c(ld0 + (slot) + 3072, xv, srx, xs0 + XM + K3_T(t) * (2 * XK) + XK); } while (0)
        K3_W(0, 0); K3_W(1, K3_SLOT); K3_W(2, 2 * K3_SLOT); K3_W(3, 3 * K3_SLOT); K3_W(4, 4 * K3_SLOT);
        K3_X(0, 0); K3_X(1, K3_SLOT); K3_X(2, 2 * K3_SLOT);
        WAITBAR(8);  K3_X(3, 3 * K3_SLOT); K3_X(4, 4 * K3_SLOT); K3_X(5, 5 * K3_SLOT); K3_W(5, 5 * K3_SLOT);
        WAITBAR(18); K3_X(6, 0); K3_W(6, 0);
        WAITBAR(20); K3_X(7, K3_SLOT); K3_W(7, K3_SLOT);
        WAITBAR(22); K3_X(8, 2 * K3_SLOT); K3_W(8, 2 * K3_SLOT);
        int fr = 3 * K3_SLOT;
#pragma unroll 1
        for (int t = 4; t + K3_NS - 1 < NT; ++t) {
            WAITBAR(6 * (K3_NS - 2));
            K3_X(t + K3_NS - 1, fr); K3_W(t + K3_NS - 1, fr);
            fr = (fr == (K3_NS - 1) * K3_SLOT) ? 0 : fr + K3_SLOT;
        }
        WAITBAR(24); WAITBAR(18); WAITBAR(12); WAITBAR(6); WAITBAR(0);
        if (MODE == 0) LBAR();
    } else {
        const int wm = w >> 1, wn = w & 1;
        const int col0 = ct * 64 + wn * 32 + 4 * g;
        STAMP_AT(1);
        u32x4 res[4];
#pragma unroll
        for (int m = 0; m < 4; ++m) {
            const int mrow = rt * 128 + wm * 64 + m * 16;
            if (MODE == 0) { const u32x2 a = *(const u32x2*)(P.x + (size_t)(mrow + r) * DM + col0), c = *(const u32x2*)(P.x + (size_t)(mrow + r) * DM + col0 + 16); res[m] = (u32x4){a.x, a.y, c.x, c.y}; }
            else res[m] = ldc16(mk_srd(ws + WS_H, SEQ * DM * 2), FRAG(mrow >> 4, ct * 2 + wn, 32) + lane * 16);
        }
        const int xrd = wm * 8192 + lane * 16;
        const int q4 = (lane >> 2) & 3, p4 = lane & 3;
        const int trow = (MODE == 0) ? (4 * (g & 1) + 16 * (g >> 1) + q4) : (4 * g + q4);
        constexpr int TR2 = (MODE == 0) ? 8 * 128 : 16 * 128;
        int wrd[2];
#pragma unroll
        for (int n = 0; n < 2; ++n) wrd[n] = 16384 + trow * 128 + (((wn * 2 + n) ^ ((trow >> 1) & 3)) * 32) + (p4 >> 1) * 16 + (p4 & 1) * 8;
        f32x4 acc[4][2];
#pragma unroll
        for (int m = 0; m < 4; ++m) { acc[m][0] = (f32x4){0.f, 0.f, 0.f, 0.f}; acc[m][1] = (f32x4){0.f, 0.f, 0.f, 0.f}; }
        bf16x8 xa[4], xb[4]; s16x4 wa[4], wb[4];
#define K3_RD(i, X, W, sl, s) do { if ((i) == 2) X[0] = ldf(lds + (sl) + xrd + (s) * 1024); else if ((i) >= 5) X[(i) - 4] = ldf(lds + (sl) + xrd + ((i) - 4) * 2048 + (s) * 1024); \
        else { const int k_ = (i) < 2 ? (i) : (i) - 1; W[k_] = trd(lds + (sl) + wrd[k_ >> 1] + (k_ & 1) * TR2 + (s) * 4096); } } while (0)
#define K3_MM(i, X, W) do { const int m_ = (i) >> 1, n_ = (i) & 1; acc[m_][n_] = MFMA16(cat8(W[2 * n_], W[2 * n_ + 1]), X[m_], acc[m_][n_]); } while (0)
#define K3_HALF(XM, WM, XR, WR, sl, s, DOMMA) do { _Pragma("unroll") for (int i_ = 0; i_ < 8; ++i_) { if (DOMMA) K3_MM(i_, XM, WM); K3_RD(i_, XR, WR, sl, s); SCHED0(); } } while (0)
        LBAR();
        STAMP_AT(2);
        K3_HALF(xb, wb, xa, wa, 0, 0, false);
        K3_HALF(xa, wa, xb, wb, 0, 1, true);
        int cur = K3_SLOT;
#pragma unroll 1
        for (int t = 1; t < NT; ++t) {
            LBAR();
            K3_HALF(xb, wb, xa, wa, cur, 0, true);
            K3_HALF(xa, wa, xb, wb, cur, 1, true);
            cur = (cur == (K3_NS - 1) * K3_SLOT) ? 0 : cur + K3_SLOT;
        }
#pragma unroll
        for (int i = 0; i < 8; ++i) K3_MM(i, xb, wb);
        STAMP_AT(3);
        if (MODE == 0) {
            LAS f32x2* sb = (LAS f32x2*)(lds + K3_AUX);
            const srd_t srh = mk_srd(ws + WS_H, SEQ * DM * 2);
#pragma unroll
            for (int m = 0; m < 4; ++m) {
                const int mrow = rt * 128 + wm * 64 + m * 16;
                const f32x4 a = {bf_lo(res[m].x) + acc[m][0][0], bf_hi(res[m].x) + acc[m][0][1], bf_lo(res[m].y) + acc[m][0][2], bf_hi(res[m].y) + acc[m][0][3]};
                const f32x4 c = {bf_lo(res[m].z) + acc[m][1][0], bf_hi(res[m].z) + acc[m][1][1], bf_lo(res[m].w) + acc[m][1][2], bf_hi(res[m].w) + acc[m][1][3]};
                const u32x4 hv = pk8(a, c);
                st16(srh, FRAG(mrow >> 4, ct * 2 + wn, 32) + lane * 16, hv);
                float s = 0.f, q = 0.f;
#pragma unroll
                for (int j = 0; j < 4; ++j) { const unsigned d = hv[j]; const float u = bf_lo(d), v = bf_hi(d); s += u + v; q += u * u + v * v; }
                s += __shfl_xor(s, 16); s += __shfl_xor(s, 32); q += __shfl_xor(q, 16); q += __shfl_xor(q, 32);
                if (g == 0) sb[(wm * 64 + m * 16 + r) * 2 + wn] = (f32x2){s, q};
            }
            LBAR();
            if (tid < 128) { const f32x2 a = sb[tid * 2] + sb[tid * 2 + 1];
                __hip_atomic_store((gu64*)(ws + WS_ST) + ((size_t)(rt * 128 + tid) * 16 + ct), ((unsigned long long)__float_as_uint(a[1]) << 32) | __float_as_uint(a[0]), RLX_AGENT); }
        } else {
#pragma unroll
            for (int m = 0; m < 4; ++m) {
                const int mrow = rt * 128 + wm * 64 + m * 16;
                unsigned a0 = pk_bf16(bf_lo(res[m].x) + acc[m][0][0], bf_hi(res[m].x) + acc[m][0][1]), a1 = pk_bf16(bf_lo(res[m].y) + acc[m][0][2], bf_hi(res[m].y) + acc[m][0][3]);
                unsigned b0 = pk_bf16(bf_lo(res[m].z) + acc[m][1][0], bf_hi(res[m].z) + acc[m][1][1]), b1 = pk_bf16(bf_lo(res[m].w) + acc[m][1][2], bf_hi(res[m].w) + acc[m][1][3]);
                const auto s0 = __builtin_amdgcn_permlane16_swap(b0, a0, false, false), s1 = __builtin_amdgcn_permlane16_swap(b1, a1, false, false);
                const u32x4 ov = {s0[0], s1[0], s0[1], s1[1]};
                const int c8 = ((g & 1) ? 0 : 16) + (g >> 1) * 8;
                *(u32x4*)(P.out + (size_t)(mrow + r) * DM + ct * 64 + wn * 32 + c8) = ov;
            }
        }
    }
    STAMP_AT(4);
    if (MODE == 0) {
        WAITBAR(0);
        if (SYNC && tid == 0) flag_set(ws, FL_3 + rt * 16 + ct, tag);
        CODE_TOUCH_ASYNC(".Lcw_p3", ".Lcw_p4");
    }
}

constexpr int K5_SLOT = 32768, K5_NS = 4;
template <int KH>
__device__ __forceinline__ void p5_finish(const Params& P, const f32x4 (&acc)[4][4], unsigned tag, int b, int w, int lane, int rt, int ct STAMP_ARGS) {
    char* ws = P.ws;
    const int g = lane >> 4, r = lane & 15, wm = w >> 1, wn = w & 1;
    const srd_t sre = mk_srd(ws + WS_XCH, 8u << 20);
    const unsigned eo = (unsigned)(w * 8192) + lane * 16;
#pragma unroll
    for (int m = 0; m < 4; ++m)
#pragma unroll
        for (int n = 0; n < 2; ++n) st16(sre, (unsigned)b * 32768u + eo + (m * 2 + n) * 1024, __builtin_bit_cast(u32x4, acc[m][2 * (1 - KH) + n]));
    u32x4 res[4];
#pragma unroll
    for (int m = 0; m < 4; ++m) res[m] = ldc16(mk_srd(ws + WS_H, SEQ * DM * 2), FRAG(rt * 8 + wm * 4 + m, ct * 4 + wn * 2 + KH, 32) + lane * 16);
    WAITV(0);
    if (lane == 0) flag_set(ws, FL_5 + b * 4 + w, tag);
    flags_wait(ws, lane == 0 ? FL_5 + (b ^ 2) * 4 + w : -1, tag);
    STAMP_AT(4);
    u32x4 pv[4][2];
#pragma unroll
    for (int m = 0; m < 4; ++m)
#pragma unroll
        for (int n = 0; n < 2; ++n) pv[m][n] = ldc16(sre, (unsigned)(b ^ 2) * 32768u + eo + (m * 2 + n) * 1024);
#pragma unroll
    for (int m = 0; m < 4; ++m) {
        const int mrow = rt * 128 + wm * 64 + m * 16;
        const f32x4 t0 = acc[m][2 * KH] + __builtin_bit_cast(f32x4, pv[m][0]), t1 = acc[m][2 * KH + 1] + __builtin_bit_cast(f32x4, pv[m][1]);
        unsigned a0 = pk_bf16(bf_lo(res[m].x) + t0[0], bf_hi(res[m].x) + t0[1]), a1 = pk_bf16(bf_lo(res[m].y) + t0[2], bf_hi(res[m].y) + t0[3]);
        unsigned b0 = pk_bf16(bf_lo(res[m].z) + t1[0], bf_hi(res[m].z) + t1[1]), b1 = pk_bf16(bf_lo(res[m].w) + t1[2], bf_hi(res[m].w) + t1[3]);
        const auto s0 = __builtin_amdgcn_permlane16_swap(b0, a0, false, false), s1 = __builtin_amdgcn_permlane16_swap(b1, a1, false, false);
        const u32x4 ov = {s0[0], s1[0], s0[1], s1[1]};
        const int c8 = ((g & 1) ? 0 : 16) + (g >> 1) * 8;
        *(u32x4*)(P.out + (size_t)(mrow + r) * DM + ct * 128 + wn * 64 + KH * 32 + c8) = ov;
    }
}
template <bool SYNC>
__device__ __forceinline__ void phase5(LAS char* lds, const Params& P, unsigned tag STAMP_ARGS) {
    constexpr int KT = FF / 32, NT = FF / 128;
    const unsigned lds0 = (unsigned)(uintptr_t)lds;
    const int tid = threadIdx.x, lane = tid & 63, w = __builtin_amdgcn_readfirstlane(tid >> 6), g = lane >> 4, r = lane & 15;
    const int b = blockIdx.x, xcd = b & 7, loc = b >> 3;
    const int kh = (xcd >> 1) & 1, rt = (xcd >> 2) * 8 + (xcd & 1) * 4 + (loc & 3), ct = loc >> 2;
    char* ws = P.ws;
    if (w >= 4) {
        const int j = w - 4;
        const srd_t srx = mk_srd(ws + WS_ACT, SEQ * FF * 2), srw = mk_srd(P.wd, FF * DM * 2);
        constexpr unsigned XM = FRAG(1, 0, KT), XK = FRAG(0, 1, KT);
        const unsigned xv = lane * 16, xs0 = FRAG(rt * 8 + 2 * j, kh * (KT / 2), KT);
        const int wrow = lane >> 3, wch = (lane & 7) ^ (((wrow >> 1) & 3) << 1);
        const unsigned wv = (unsigned)wrow * 2048u + wch * 16, ws0 = (unsigned)ct * 256u + (unsigned)(kh * (FF / 2) + 16 * j) * 2048u;
        const unsigned ld0 = lds0 + j * 4096, ld1 = lds0 + 16384 + j * 2048;
        const int rot = 14 * (xcd & 1), fidx = lane < 16 ? FL_4 + (rt >> 1) * 32 + kh * 16 + lane : -1;
        unsigned long long rdy = SYNC ? 0ull : ~0ull;
#define K5_T(t) ((t) + rot >= NT ? (t) + rot - NT : (t) + rot)
#define K5_W(t, slot) do { const unsigned o_ = ws0 + K5_T(t) * (64 * 2048); dma16(ld1 + (slot), wv, srw, o_); dma16(ld1 + (slot) + 1024, wv, srw, o_ + 8 * 2048); \
        dma16(ld1 + (slot) + 8192, wv, srw, o_ + 128); dma16(ld1 + (slot) + 9216, wv, srw, o_ + 128 + 8 * 2048); } while (0)
#define K5_X(t, slot) do { const int tt_ = K5_T(t); wait_mask(ws, fidx, tag, (1ull << ((4 * tt_) / 7)) | (1ull << ((4 * tt_ + 3) / 7)), rdy); const unsigned o_ = xs0 + tt_ * (2 * XK); \
        dma16c(ld0 + (slot), xv, srx, o_); dma16c(ld0 + (slot) + 1024, xv, srx, o_ + XK); dma16c(ld0 + (slot) + 2048, xv, srx, o_ + XM); dma16c(ld0 + (slot) + 3072, xv, srx, o_ + XM + XK); } while (0)
        K5_W(0, 0); K5_W(1, K5_SLOT); K5_W(2, 2 * K5_SLOT); K5_W(3, 3 * K5_SLOT);
        K5_X(0, 0); K5_X(1, K5_SLOT); K5_X(2, 2 * K5_SLOT); K5_X(3, 3 * K5_SLOT);
        WAITBAR(12);
        WAITBAR(8);  K5_X(4, 0); K5_W(4, 0);
        WAITBAR(12); K5_X(5, K5_SLOT); K5_W(5, K5_SLOT);
        WAITBAR(16); K5_X(6, 2 * K5_SLOT); K5_W(6, 2 * K5_SLOT);
        int fr = 3 * K5_SLOT;
#pragma unroll 1
        for (int t = 4; t + 3 < NT; ++t) {
            WAITBAR(16);
            K5_X(t + 3, fr); K5_W(t + 3, fr);
            fr = (fr == 3 * K5_SLOT) ? 0 : fr + K5_SLOT;
        }
        WAITBAR(16); WAITBAR(8); WAITBAR(0);
    } else {
        const int wm = w >> 1, wn = w & 1;
        STAMP_AT(1);
        const int xrd = wm * 8192 + lane * 16;
        const int q4 = (lane >> 2) & 3, p4 = lane & 3, trow = 4 * g + q4;
        int wrd[4];
#pragma unroll
        for (int n = 0; n < 4; ++n) wrd[n] = 16384 + wn * 8192 + trow * 128 + ((n ^ ((trow >> 1) & 3)) * 32) + (p4 >> 1) * 16 + (p4 & 1) * 8;
        f32x4 acc[4][4];
#pragma unroll
        for (int m = 0; m < 4; ++m)
#pragma unroll
            for (int n = 0; n < 4; ++n) acc[m][n] = (f32x4){0.f, 0.f, 0.f, 0.f};
        bf16x8 xa[4], xb[4]; s16x4 wa[8], wb[8];
#define K5_RD(i, X, W, sl, s) do { if ((i) == 2) X[0] = ldf(lds + (sl) + xrd + (s) * 1024); else if ((i) >= 9) X[(i) - 8] = ldf(lds + (sl) + xrd + ((i) - 8) * 2048 + (s) * 1024); \
        else { const int k_ = (i) < 2 ? (i) : (i) - 1; W[k_] = trd(lds + (sl) + wrd[k_ >> 1] + (k_ & 1) * 2048 + (s) * 4096); } } while (0)
#define K5_MM(i, X, W) do { const int m_ = (i) >> 2, n_ = (i) & 3; acc[m_][n_] = MFMA16(cat8(W[2 * n_], W[2 * n_ + 1]), X[m_], acc[m_][n_]); } while (0)
#define K5_HALF(XM_, WM_, XR, WR, sl, s, DOMMA) do { _Pragma("unroll") for (int i_ = 0; i_ < 16; ++i_) { if (DOMMA) K5_MM(i_, XM_, WM_); if (i_ < 12) K5_RD(i_, XR, WR, sl, s); SCHED0(); } } while (0)
        LBAR();
        STAMP_AT(2);
        K5_HALF(xb, wb, xa, wa, 0, 0, false);
        K5_HALF(xa, wa, xb, wb, 0, 1, true);
        int cur = K5_SLOT;
#pragma unroll 1
        for (int t = 1; t < NT; ++t) {
            LBAR();
            K5_HALF(xb, wb, xa, wa, cur, 0, true);
            K5_HALF(xa, wa, xb, wb, cur, 1, true);
            cur = (cur == 3 * K5_SLOT) ? 0 : cur + K5_SLOT;
        }
#pragma unroll
        for (int i = 0; i < 16; ++i) K5_MM(i, xb, wb);
        STAMP_AT(3);
        if (kh) p5_finish<1>(P, acc, tag, b, w, lane, rt, ct STAMP_PASS); else p5_finish<0>(P, acc, tag, b, w, lane, rt, ct STAMP_PASS);
    }
}

constexpr int K4_SLOT = 30720, K4_NS = 4, K4_STRAW = K4_NS * K4_SLOT, K4_STAT = K4_STRAW + 32768, K4_CS = K4_STAT + 2048, K4_CSRAW = K4_CS + 1024;
struct K4Dma { srd_t srx, srg, sru; unsigned xv, gv, xs0, gs0, ld0, ld1; };
#define K4_DMA(i, t, slot) do { if ((i) < 2) dma16c(D.ld0 + (slot) + (i) * 1024, D.xv, D.srx, D.xs0 + (t) * FRAG(0, 1, 32) + (i) * FRAG(1, 0, 32)); \
        else dma16(D.ld1 + (slot) + ((i) - 2) * 7168, D.gv, (i) == 2 ? D.srg : D.sru, D.gs0 + (t) * (32 * FF * 2)); } while (0)
template <int NJ, int J0, bool HASW>
__device__ __forceinline__ void k4_body(LAS char* lds, const K4Dma& D, char* ws, int w, int lane, int rt, int ct STAMP_ARGS) {
    const int wm = w & 3, g = lane >> 4, r = lane & 15;
    constexpr int NP = HASW ? 4 : 2;
    const int xrd = wm * 4096 + lane * 16;
    const int q4 = (lane >> 2) & 3, p4 = lane & 3;
    const int wrd = 16384 + (4 * g + q4) * 224 + J0 * 32 + (p4 >> 1) * 16 + (p4 & 1) * 8;
    f32x4 ag[4][NJ], au[4][NJ];
#pragma unroll
    for (int m = 0; m < 4; ++m)
#pragma unroll
        for (int j = 0; j < NJ; ++j) { ag[m][j] = (f32x4){0.f, 0.f, 0.f, 0.f}; au[m][j] = (f32x4){0.f, 0.f, 0.f, 0.f}; }
    bf16x8 xa[4], xb[4]; s16x4 wgf[2 * NJ], wuf[2 * NJ];
#define K4_RW(i, W, sl, o) W[i] = trd(lds + (sl) + wrd + (o) + ((i) >> 1) * 32 + ((i) & 1) * (16 * 224))
#define K4_RX(i, X, sl) X[i] = ldf(lds + (sl) + xrd + (i) * 1024)
#define K4_MM(i, A, X, W) do { const int j_ = (i) >> 2, m_ = (i) & 3; A[m_][j_] = MFMA16(cat8(W[2 * j_], W[2 * j_ + 1]), X[m_], A[m_][j_]); } while (0)
#define K4_RD1(i, XC, sl) do { if ((i) == 0) K4_RX(0, XC, sl); else if ((i) < 3) K4_RW((i) - 1, wgf, sl, 0); else if ((i) < 6) K4_RX((i) - 2, XC, sl); else if ((i) < 4 + 2 * NJ) K4_RW((i) - 4, wgf, sl, 0); } while (0)
#define K4_H1(XC, XP, DOMMA, DODMA, t, sl, fsl) do { \
        _Pragma("unroll") for (int i_ = 0; i_ < 4 * NJ; ++i_) { \
            if (DOMMA) K4_MM(i_, au, XP, wuf); \
            K4_RD1(i_, XC, sl); \
            if (DODMA) { if (i_ == (NJ == 4 ? 13 : 11)) K4_DMA(0, t, fsl); } \
            SCHED0(); } } while (0)
#define K4_H2(XC, sl, DODMA, t, fsl) do { \
        _Pragma("unroll") for (int i_ = 0; i_ < 4 * NJ; ++i_) { \
            K4_MM(i_, ag, XC, wgf); \
            if (i_ < 2 * NJ) K4_RW(i_, wuf, sl, 7168); \
            if (DODMA) { if (NJ == 4) { if (i_ == 8) K4_DMA(1, t, fsl); if (i_ == 11 && NP == 4) K4_DMA(2, t, fsl); if (i_ == 14 && NP == 4) K4_DMA(3, t, fsl); } \
                         else { if (i_ == 6) K4_DMA(1, t, fsl); if (i_ == 8 && NP == 4) K4_DMA(2, t, fsl); if (i_ == 10 && NP == 4) K4_DMA(3, t, fsl); } } \
            SCHED0(); } } while (0)
    constexpr int NT = DM / 32;
    constexpr int NX = HASW ? 4 : 8;
    WAITBAR(4 + NX);
    STAMP_AT(2);
    K4_H1(xa, xb, false, true, 3, 0, 3 * K4_SLOT);
    K4_H2(xa, 0, true, 3, 3 * K4_SLOT);
    int cur = K4_SLOT, fr = 0;
#define K4_ADV() do { fr = (fr == (K4_NS - 1) * K4_SLOT) ? 0 : fr + K4_SLOT; cur = (cur == (K4_NS - 1) * K4_SLOT) ? 0 : cur + K4_SLOT; } while (0)
    WAITBAR(2 + NX + NP); K4_H1(xb, xa, true, true, 4, cur, fr); K4_H2(xb, cur, true, 4, fr); K4_ADV();
    WAITBAR(NX + 2 * NP); K4_H1(xa, xb, true, true, 5, cur, fr); K4_H2(xa, cur, true, 5, fr); K4_ADV();
    WAITBAR(2 * NP);
    {
        const int tid = w * 64 + lane, row = tid >> 1, hf = tid & 1;
        float s = 0.f, q = 0.f;
#pragma unroll
        for (int i = 0; i < 4; ++i) { const f32x4 v = *(const LAS f32x4*)(lds + K4_STRAW + row * 128 + hf * 64 + i * 16); s += v[0] + v[2]; q += v[1] + v[3]; }
        s += __shfl_xor(s, 1); q += __shfl_xor(q, 1);
        const float mean = s * (1.0f / DM);
        const float var = fmaxf(q * (1.0f / DM) - mean * mean, 0.f);
        if (hf == 0) ((LAS f32x2*)(lds + K4_STAT))[row] = (f32x2){mean, 1.0f / sqrtf(var + LN_EPS)};
        if (tid < 224) { const LAS float* cr = (const LAS float*)(lds + K4_CSRAW) + tid; ((LAS float*)(lds + K4_CS))[tid] = (cr[0] + cr[256]) + (cr[512] + cr[768]); }
        SCHED0();
    }
    K4_H1(xb, xa, true, true, 6, cur, fr); K4_H2(xb, cur, true, 6, fr); K4_ADV();
    int t = 4;
#pragma unroll 1
    for (; t + 4 < NT; t += 2) {
        WAITBAR(2 * NP); K4_H1(xa, xb, true, true, t + 3, cur, fr); K4_H2(xa, cur, true, t + 3, fr); K4_ADV();
        WAITBAR(2 * NP); K4_H1(xb, xa, true, true, t + 4, cur, fr); K4_H2(xb, cur, true, t + 4, fr); K4_ADV();
    }
    WAITBAR(2 * NP); K4_H1(xa, xb, true, true, t + 3, cur, fr); K4_H2(xa, cur, true, t + 3, fr); K4_ADV();
    WAITBAR(2 * NP); K4_H1(xb, xa, true, false, 0, cur, fr); K4_H2(xb, cur, false, 0, fr); K4_ADV();
    WAITBAR(NP);     K4_H1(xa, xb, true, false, 0, cur, fr); K4_H2(xa, cur, false, 0, fr); K4_ADV();
    WAITBAR(0);      K4_H1(xb, xa, true, false, 0, cur, fr); K4_H2(xb, cur, false, 0, fr);
#pragma unroll
    for (int i = 0; i < 4 * NJ; ++i) K4_MM(i, au, xb, wuf);
    STAMP_AT(3);
    const LAS f32x2* st = (const LAS f32x2*)(lds + K4_STAT);
    const LAS float* cs = (const LAS float*)(lds + K4_CS);
    f32x2 ms[4];
#pragma unroll
    for (int m = 0; m < 4; ++m) ms[m] = st[wm * 64 + m * 16 + r];
    const srd_t sra = mk_srd(ws + WS_ACT, SEQ * FF * 2);
    const unsigned ap0 = FRAG(rt * 16 + wm * 4, 0, 112) + lane * 16;
    const int ft0 = ct * 7 + J0;
    constexpr int LONE = (NJ == 3) ? (J0 == 4 ? 2 : 0) : -1;
    u32x2 keep[4];
#pragma unroll
    for (int j = 0; j < NJ; ++j) {
        const f32x4 cg = *(const LAS f32x4*)(cs + (J0 + j) * 16 + 4 * g), cu = *(const LAS f32x4*)(cs + 112 + (J0 + j) * 16 + 4 * g);
        const bool second = (LONE == 0) ? (j == 2) : ((j & 1) == 1);
#pragma unroll
        for (int m = 0; m < 4; ++m) {
            const float c1 = -LOG2E * ms[m].y;
            const f32x4 gz = (ag[m][j] - cg * ms[m].x) * c1;
            const f32x4 uu = (au[m][j] - cu * ms[m].x) * ms[m].y;
            float a[4];
#pragma unroll
            for (int e = 0; e < 4; ++e) a[e] = uu[e] * __builtin_amdgcn_rcpf(1.0f + __builtin_amdgcn_exp2f(gz[e]));
            u32x2 ov; ov.x = pk_bf16(a[0], a[1]); ov.y = pk_bf16(a[2], a[3]);
            const int ft = ft0 + j;
            if (j == LONE) st8(sra, ap0 + FRAG(m, ft >> 1, 112) + (ft & 1) * 8, ov);
            else if (!second) keep[m] = ov;
            else st16(sra, ap0 + FRAG(m, ft >> 1, 112), (u32x4){keep[m].x, keep[m].y, ov.x, ov.y});
        }
        SCHED0();
    }
}

template <bool SYNC>
__device__ __forceinline__ void phase4(LAS char* lds, const Params& P, unsigned tag STAMP_ARGS) {
    const unsigned lds0 = (unsigned)(uintptr_t)lds;
    const int tid = threadIdx.x, lane = tid & 63, w = __builtin_amdgcn_readfirstlane(tid >> 6);
    const int b = blockIdx.x, xcd = b & 7, loc = b >> 3;
    const int rt = (xcd >> 2) * 4 + (loc & 3), ct = (xcd & 3) * 8 + (loc >> 2);
    char* ws = P.ws;
    K4Dma D;
    D.srx = mk_srd(ws + WS_H, SEQ * DM * 2); D.srg = mk_srd(P.wg, DM * FF * 2); D.sru = mk_srd(P.wu, DM * FF * 2);
    D.xv = lane * 16; D.xs0 = FRAG(rt * 16 + w * 2, 0, 32);
    { const int ci = (w < 7 ? w : 6) * 64 + lane; const int row = ci / 14, c = ci % 14; D.gv = (unsigned)row * (FF * 2) + c * 16; }
    D.gs0 = (unsigned)ct * 224u; D.ld0 = lds0 + w * 2048; D.ld1 = lds0 + 16384 + w * 1024;
    if (w < 7) {
#pragma unroll
        for (int t = 0; t < 3; ++t) { K4_DMA(2, t, t * K4_SLOT); K4_DMA(3, t, t * K4_SLOT); }
    }
    if (SYNC) {
        if (w == 7) {
            int idx = -1;
            if (lane < 32) idx = FL_3 + rt * 32 + lane;
            else if (lane < 48) { const int k = lane - 32, gsel = k & 3, rq = k >> 2;
                const int c0 = (ct * 112 + ((gsel & 1) ? 111 : 0)) >> 7, bb = rq * 56 + (gsel >> 1) * 28 + c0;
                idx = FL_2 + (bb >> 5) * 32 + (bb & 7) * 4 + ((bb >> 3) & 3); }
            flags_wait(ws, idx, tag);
        }
        LBAR();
    }
    STAMP_AT(1);
#pragma unroll
    for (int t = 0; t < 3; ++t) { K4_DMA(0, t, t * K4_SLOT); K4_DMA(1, t, t * K4_SLOT); }
    {
        const srd_t srs = mk_srd(ws + WS_ST, SEQ * 128);
#pragma unroll
        for (int i = 0; i < 4; ++i) dma16c(lds0 + K4_STRAW + w * 4096 + i * 1024, lane * 16, srs, (unsigned)(rt * 256 + w * 32) * 128u + i * 1024);
        if (w == 7) {
            const srd_t src = mk_srd(ws + WS_CS, 4 * 7168 * 4);
            const unsigned cv = lane < 28 ? (unsigned)(ct * 112 + 4 * lane) * 4u : lane < 56 ? (unsigned)(3584 + ct * 112 + 4 * (lane - 28)) * 4u : 0x7ff00000u;
#pragma unroll
            for (int i = 0; i < 4; ++i) dma16c(lds0 + K4_CSRAW + i * 1024, cv, src, i * 7168 * 4);
        }
    }
    if ((ct & 1) == 0) {
        if (w < 4) k4_body<4, 0, true>(lds, D, ws, w, lane, rt, ct STAMP_PASS);
        else if (w < 7) k4_body<3, 4, true>(lds, D, ws, w, lane, rt, ct STAMP_PASS);
        else k4_body<3, 4, false>(lds, D, ws, w, lane, rt, ct STAMP_PASS);
    } else {
        if (w < 4) k4_body<3, 0, true>(lds, D, ws, w, lane, rt, ct STAMP_PASS);
        else if (w < 7) k4_body<4, 3, true>(lds, D, ws, w, lane, rt, ct STAMP_PASS);
        else k4_body<4, 3, false>(lds, D, ws, w, lane, rt, ct STAMP_PASS);
    }
    STAMP_AT(4);
    WAITBAR(0);
    if (SYNC && tid == 0) flag_set(ws, FL_4 + rt * 32 + ct, tag);
    if (w < 6) line_touch(lds, FRAG(rt * 16, (ct * 7 + 1) / 2 + (w >> 1), 112) + (w & 1) * 8192 + lane * 128, mk_srd(ws + WS_ACT, SEQ * FF * 2));
    CODE_TOUCH_ASYNC(".Lcw_p4", ".Lcw_p5");
}

constexpr int LDS_BYTES = 160 * 1024;
__global__ __launch_bounds__(512, 2) void mega(Params P) {
    extern __shared__ __attribute__((aligned(16))) char smem_g[];
    LAS char* lds = (LAS char*)smem_g;
    char* ws = P.ws;
    const unsigned tag = __hip_atomic_load((gu32*)(ws + WS_FLG) + FL_EPOCH, RLX_AGENT) + 1u;
    LITE_BEGIN()
    asm volatile(".Lcw_p0:");
    { STAMP_DECL STAMP_AT(0); phase1<true>(lds, P, tag STAMP_PASS); STAMP_OUT(1); }
    asm volatile(".Lcw_p1:");
    { STAMP_DECL STAMP_AT(0); phase2<true>(lds, P, tag STAMP_PASS); STAMP_OUT(2); }
    asm volatile(".Lcw_p2:");
    { STAMP_DECL STAMP_AT(0); phase35<0, true>(lds, P, tag STAMP_PASS); STAMP_OUT(3); }
    asm volatile(".Lcw_p3:");
    { STAMP_DECL STAMP_AT(0); phase4<true>(lds, P, tag STAMP_PASS); STAMP_OUT(4); }
    asm volatile(".Lcw_p4:");
    { STAMP_DECL STAMP_AT(0); phase5<true>(lds, P, tag STAMP_PASS); STAMP_OUT(5); }
    asm volatile(".Lcw_p5:");
    LITE_END();
    if (blockIdx.x == 0 && threadIdx.x == 0) __hip_atomic_store((gu32*)(ws + WS_FLG) + FL_EPOCH, tag, RLX_AGENT);
}

extern "C" void kernel_launch(void* const* d_in, const int* in_sizes, int n_in, void* d_out, int out_size, void* d_ws, size_t ws_size, hipStream_t stream) {
    Params P;
    P.x = (const u16*)d_in[0]; P.wqkv = (const u16*)d_in[1]; P.wo = (const u16*)d_in[2];
    P.wg = (const u16*)d_in[4]; P.wu = (const u16*)d_in[5]; P.wd = (const u16*)d_in[6];
    P.out = (u16*)d_out;
    P.ws = (char*)d_ws;
    (void)hipFuncSetAttribute((const void*)mega, hipFuncAttributeMaxDynamicSharedMemorySize, LDS_BYTES);
    mega<<<256, 512, LDS_BYTES, stream>>>(P);
}
```

```cpp
#include <hip/hip_runtime.h>
#include <hip/hip_bf16.h>
#include <stdint.h>

#define LAS __attribute__((address_space(3)))
typedef unsigned short u16;
typedef short bf16x8 __attribute__((ext_vector_type(8)));
typedef short s16x4 __attribute__((ext_vector_type(4)));
typedef float f32x2 __attribute__((ext_vector_type(2)));
typedef float f32x4 __attribute__((ext_vector_type(4)));
typedef float f32x16 __attribute__((ext_vector_type(16)));
typedef unsigned u32x2 __attribute__((ext_vector_type(2)));
typedef unsigned u32x4 __attribute__((ext_vector_type(4)));
typedef __bf16 bf16x2_t __attribute__((ext_vector_type(2)));
typedef __amdgpu_buffer_rsrc_t srd_t;

constexpr int SEQ = 2048, DM = 1024, NH = 32, FF = 3584;
constexpr float LN_EPS = 1e-5f;
constexpr float QSCALE = 0.17677669529663687f * 1.4426950408889634f;
constexpr float LOG2E = 1.4426950408889634f;

constexpr size_t WS_Q = 0;
constexpr size_t WS_K = WS_Q + (4u << 20);
constexpr size_t WS_V = WS_K + (4u << 20);
constexpr size_t WS_O = WS_V + (4u << 20);
constexpr size_t WS_H = WS_O + (4u << 20);
constexpr size_t WS_ACT = WS_H + (4u << 20);
constexpr size_t WS_ST = WS_ACT + (size_t)SEQ * FF * 2;
constexpr size_t WS_CS = WS_ST + (size_t)SEQ * 16 * 8;
constexpr size_t WS_S1 = WS_CS + 4 * 7168 * 4;
constexpr size_t WS_C1 = WS_S1 + SEQ * 8;
constexpr size_t WS_FLG = WS_C1 + 16 * 3072 * 4;
constexpr size_t WS_XCH = WS_FLG + 16384;
constexpr size_t WS_END = WS_XCH + (8u << 20);
constexpr int FL_EPOCH = 0, FL_ERR = 32, FL_0 = 64, FL_1 = FL_0 + 256, FL_2 = FL_1 + 256, FL_3 = FL_2 + 256, FL_4 = FL_3 + 256, FL_5 = FL_4 + 256;

#define FRAG(mt, kt, KT) (((unsigned)(kt) * 128u + (unsigned)(mt)) * 1024u)
#define ST_AUX 16

__device__ __forceinline__ unsigned pk_bf16(float lo, float hi) { f32x2 v = {lo, hi}; bf16x2_t b = __builtin_convertvector(v, bf16x2_t); return __builtin_bit_cast(unsigned, b); }
__device__ __forceinline__ float bf_lo(unsigned u) { return __uint_as_float(u << 16); }
__device__ __forceinline__ float bf_hi(unsigned u) { return __uint_as_float(u & 0xffff0000u); }
__device__ __forceinline__ s16x4 trd(LAS const char* p) { return __builtin_bit_cast(s16x4, __builtin_amdgcn_ds_read_tr16_b64_v4i16((LAS s16x4*)p)); }
__device__ __forceinline__ bf16x8 cat8(s16x4 a, s16x4 b) { return (bf16x8){a[0], a[1], a[2], a[3], b[0], b[1], b[2], b[3]}; }
__device__ __forceinline__ bf16x8 ldf(LAS const char* p) { return *(LAS const bf16x8*)p; }
__device__ __forceinline__ srd_t mk_srd(const void* p, unsigned bytes) { return __builtin_amdgcn_make_buffer_rsrc((void*)p, (short)0, (int)bytes, 0x00020000); }
__device__ __forceinline__ void dma16(unsigned ldsa, unsigned voff, srd_t srd, unsigned soff) {
    asm volatile("s_mov_b32 m0, %0\n\ts_nop 0\n\tbuffer_load_dwordx4 %1, %2, %3 offen lds" :: "s"(ldsa), "v"(voff), "s"(srd), "s"(soff));
}
__device__ __forceinline__ void dma16c(unsigned ldsa, unsigned voff, srd_t srd, unsigned soff) {
    asm volatile("s_mov_b32 m0, %0\n\ts_nop 0\n\tbuffer_load_dwordx4 %1, %2, %3 offen sc1 lds" :: "s"(ldsa), "v"(voff), "s"(srd), "s"(soff));
}
__device__ __forceinline__ void st8(srd_t srd, unsigned off, u32x2 v) { __builtin_amdgcn_raw_buffer_store_b64(v, srd, off, 0, ST_AUX); }
__device__ __forceinline__ void st16(srd_t srd, unsigned off, u32x4 v) { __builtin_amdgcn_raw_buffer_store_b128(v, srd, off, 0, ST_AUX); }
__device__ __forceinline__ u32x4 pk8(const f32x4 a, const f32x4 b) { u32x4 o; o.x = pk_bf16(a[0], a[1]); o.y = pk_bf16(a[2], a[3]); o.z = pk_bf16(b[0], b[1]); o.w = pk_bf16(b[2], b[3]); return o; }
#define MFMA16(a, b, c) __builtin_amdgcn_mfma_f32_16x16x32_bf16(a, b, c, 0, 0, 0)
#define MFMA32(a, b, c) __builtin_amdgcn_mfma_f32_32x32x16_bf16(a, b, c, 0, 0, 0)
#define WAITVL(n) asm volatile("s_waitcnt vmcnt(%0) lgkmcnt(0)" :: "n"(n) : "memory")
#define WAITBAR(n) asm volatile("s_waitcnt vmcnt(%0) lgkmcnt(0)\n\ts_barrier" :: "n"(n) : "memory")
#define LBAR() asm volatile("s_waitcnt lgkmcnt(0)\n\ts_barrier" ::: "memory")
#define SCHED0() __builtin_amdgcn_sched_barrier(0)
#define CODE_TOUCH(A, B, t0, nthr) do { unsigned long long pc_; int lo_, hi_; unsigned dm_ = 0u; \
        asm volatile("s_getpc_b64 %0\n.Lct%=:\n\ts_mov_b32 %1, " A "-.Lct%=\n\ts_mov_b32 %2, " B "-.Lct%=" : "=&s"(pc_), "=&s"(lo_), "=&s"(hi_)); \
        for (int o_ = lo_ + ((int)threadIdx.x - (t0)) * 128; o_ < hi_; o_ += (nthr) * 128) asm volatile("global_load_dword %0, %1, off" : "+v"(dm_) : "v"((const char*)pc_ + o_)); \
        asm volatile("s_waitcnt vmcnt(0)" : "+v"(dm_) :: "memory");        \
        } while (0)
#define CODE_TOUCH_ASYNC(A, B) do { unsigned long long pc_; int lo_, hi_; \
        asm volatile("s_getpc_b64 %0\n.Lct%=:\n\ts_mov_b32 %1, " A "-.Lct%=\n\ts_mov_b32 %2, " B "-.Lct%=" : "=&s"(pc_), "=&s"(lo_), "=&s"(hi_)); \
        const srd_t sc_ = mk_srd((const char*)pc_ + lo_, (unsigned)(hi_ - lo_)); \
        for (int o_ = 0; o_ < hi_ - lo_; o_ += 512 * 128) \
            asm volatile("s_mov_b32 m0, %0\n\ts_nop 0\n\tbuffer_load_dword %1, %2, %3 offen lds" :: "s"((unsigned)(uintptr_t)lds + 163584u), "v"(threadIdx.x * 128u), "s"(sc_), "s"(o_)); \
        } while (0)
__device__ __forceinline__ void line_touch(LAS char* lds, unsigned voff, srd_t srd) {
    asm volatile("s_mov_b32 m0, %0\n\ts_nop 0\n\tbuffer_load_dword %1, %2, 0 offen sc1 lds" :: "s"((unsigned)(uintptr_t)lds + 163584u), "v"(voff), "s"(srd));
}
#define WAITV(n) asm volatile("s_waitcnt vmcnt(%0)" :: "n"(n) : "memory")
#define RLX_AGENT __ATOMIC_RELAXED, __HIP_MEMORY_SCOPE_AGENT
typedef __attribute__((address_space(1))) unsigned gu32;
typedef __attribute__((address_space(1))) unsigned long long gu64;
struct Params { const u16 *x, *wqkv, *wo, *wg, *wu, *wd; u16* out; char* ws; };
__device__ __forceinline__ void flag_set(char* ws, int idx, unsigned tag) { __hip_atomic_store((gu32*)(ws + WS_FLG) + idx, tag, RLX_AGENT); }
__device__ __forceinline__ void flags_wait(char* ws, int idx, unsigned tag) {
    gu32* fl = (gu32*)(ws + WS_FLG);
    const unsigned long long t0 = __builtin_amdgcn_s_memrealtime();
    for (unsigned spins = 1;; ++spins) {
        const unsigned v = idx >= 0 ? __hip_atomic_load(fl + idx, RLX_AGENT) : tag;
        if (__all(v == tag)) break;
        if ((spins & 63u) == 0u) {
            if (__any(__hip_atomic_load(fl + FL_ERR, RLX_AGENT) == tag)) break;
            if (__builtin_amdgcn_s_memrealtime() - t0 > 2000000ull) { __hip_atomic_store(fl + FL_ERR, tag, RLX_AGENT); break; }
        }
        __builtin_amdgcn_s_sleep(1);
    }
    asm volatile("" ::: "memory");
}
template <int CTRL, int RM> __device__ __forceinline__ float dpp_add(float v) { return v + __int_as_float(__builtin_amdgcn_update_dpp(0, __float_as_int(v), CTRL, RM, 0xf, true)); }
__device__ __forceinline__ float sum32(float v) { v = dpp_add<0x111, 0xf>(v); v = dpp_add<0x112, 0xf>(v); v = dpp_add<0x114, 0xf>(v); v = dpp_add<0x118, 0xf>(v); return dpp_add<0x142, 0xa>(v); }
__device__ __forceinline__ void wait_mask(char* ws, int idx, unsigned tag, unsigned long long need, unsigned long long& rdy) {
    if (__builtin_expect((rdy & need) == need, 1)) return;
    gu32* fl = (gu32*)(ws + WS_FLG);
    const unsigned long long t0 = __builtin_amdgcn_s_memrealtime();
    for (unsigned spins = 1;; ++spins) {
        const unsigned v = idx >= 0 ? __hip_atomic_load(fl + idx, RLX_AGENT) : ~tag;
        rdy = __ballot(v == tag);
        if ((rdy & need) == need) break;
        if ((spins & 63u) == 0u) {
            if (__any(__hip_atomic_load(fl + FL_ERR, RLX_AGENT) == tag)) break;
            if (__builtin_amdgcn_s_memrealtime() - t0 > 2000000ull) { __hip_atomic_store(fl + FL_ERR, tag, RLX_AGENT); break; }
        }
        __builtin_amdgcn_s_sleep(1);
    }
    asm volatile("" ::: "memory");
}
__device__ __forceinline__ u32x4 ldc16(srd_t srd, unsigned off) { return __builtin_amdgcn_raw_buffer_load_b128(srd, off, 0, 16); }
__device__ __forceinline__ float ldc4(srd_t srd, unsigned off) { return __uint_as_float(__builtin_amdgcn_raw_buffer_load_b32(srd, off, 0, 16)); }


constexpr int K1_SLOT = 40960, K1_NS = 4, K1_AUX = 0;
template <bool SYNC>
__device__ __forceinline__ void phase1(LAS char* lds, const Params& P, unsigned tag) {
    const unsigned lds0 = (unsigned)(uintptr_t)lds;
    const int tid = threadIdx.x, lane = tid & 63, w = __builtin_amdgcn_readfirstlane(tid >> 6), g = lane >> 4, r = lane & 15;
    const int b = blockIdx.x, xcd = b & 7, loc = b >> 3;
        const int p = xcd * 2 + (loc & 1), rt = loc >> 1;
    char* ws = P.ws;
    constexpr int NT = DM / 64;
    LAS f32x2* stat = (LAS f32x2*)(lds + K1_AUX);
    LAS float* csl = (LAS float*)(lds + K1_AUX + 1024);
    if (__builtin_expect(w >= 4, 1)) {
        const srd_t srx = mk_srd(P.x, SEQ * DM * 2), srw = mk_srd(P.wqkv, DM * 3 * DM * 2);
        const int j = w - 4, rr = lane >> 3, c8 = lane & 7;
        unsigned xv[2], wv[2];
#pragma unroll
        for (int i = 0; i < 2; ++i) {
            xv[i] = rr * 2048 + ((c8 ^ (4 * i + (rr >> 1))) * 16);
            const int f = ((rr >> 1) & 1) | (i << 1);
            wv[i] = rr * 6144 + (((c8 >> 1) ^ f) * 32) + (c8 & 1) * 16;
        }
        const unsigned xs0 = (unsigned)(rt * 128 + 32 * j) * 2048u, ws0 = (unsigned)p * 128u + (unsigned)(16 * j) * 6144u;
        const unsigned xl0 = lds0 + j * 4096, wl0 = lds0 + 16384 + j * 2048;
#define K1_DMA(i, t, slot) do { if ((i) < 4) dma16(xl0 + (slot) + (i) * 1024, xv[(i) & 1], srx, xs0 + (i) * (8 * 2048) + (((t) + rt) & 15) * 128); \
        else dma16(wl0 + (slot) + (((i) - 4) >> 1) * 8192 + (((i) - 4) & 1) * 1024, wv[((i) - 4) & 1], srw, ws0 + (((i) - 4) >> 1) * 2048 + (((i) - 4) & 1) * (8 * 6144) + (((t) + rt) & 15) * (64 * 6144)); } while (0)
#define K1_STAGE(t, slot) do { _Pragma("unroll") for (int i_ = 0; i_ < 10; ++i_) K1_DMA(i_, t, slot); } while (0)
        const int srow = 8 * b + 2 * j + (lane >> 5);
        u32x4 xr[4];
        { const unsigned xo = (unsigned)srow * 2048u + (lane & 31) * 16;
#pragma unroll
          for (int k = 0; k < 4; ++k) asm volatile("buffer_load_dwordx4 %0, %1, %2, 0 offen offset:%3" : "=v"(xr[k]) : "v"(xo), "s"(srx), "n"(k * 512)); }
        K1_STAGE(0, 0); K1_STAGE(1, K1_SLOT); K1_STAGE(2, 2 * K1_SLOT);
        WAITBAR(20);
        asm volatile("" : "+v"(xr[0]), "+v"(xr[1]), "+v"(xr[2]), "+v"(xr[3]));
        K1_STAGE(3, 3 * K1_SLOT);
        {
            const unsigned x00 = xr[0][0];
            const float pv = bf_lo(lane < 32 ? __builtin_amdgcn_readlane(x00, 0) : __builtin_amdgcn_readlane(x00, 32));
            float s = 0.f, q = 0.f;
#pragma unroll
            for (int k = 0; k < 4; ++k)
#pragma unroll
                for (int e = 0; e < 4; ++e) { const unsigned d = xr[k][e]; const float u = bf_lo(d) - pv, v = bf_hi(d) - pv; s += u + v; q += u * u + v * v; }
            s = sum32(s); q = sum32(q);
            const float dm = s * (1.0f / DM), var = fmaxf(q * (1.0f / DM) - dm * dm, 0.f);
            const u32x2 sv = {__float_as_uint(pv + dm), __float_as_uint(1.0f / sqrtf(var + LN_EPS))};
            __builtin_amdgcn_raw_buffer_store_b64(sv, mk_srd(ws + WS_S1, SEQ * 8), (lane & 31) == 31 ? (unsigned)srow * 8u : 0x7ff00000u, 0, 16);
        }
        WAITBAR(21); K1_STAGE(4, 0);
        WAITBAR(21); K1_STAGE(5, K1_SLOT);
        WAITBAR(21); K1_STAGE(6, 2 * K1_SLOT);
        WAITBAR(20);
        if (SYNC && tid == 256) flag_set(ws, FL_0 + b, tag);
        K1_STAGE(7, 3 * K1_SLOT);
        WAITBAR(20); K1_STAGE(8, 0);
        WAITBAR(20); K1_STAGE(9, K1_SLOT);
        WAITBAR(20); K1_STAGE(10, 2 * K1_SLOT);
        WAITBAR(20);
        const int fidx = lane < 16 ? FL_0 + 16 * rt + lane : lane < 32 ? FL_0 + ((lane - 16) * 2 + (p & 1)) * 8 + (p >> 1) : -1;
        unsigned fseen;
        asm volatile("global_load_dword %0, %1, off sc1" : "=v"(fseen) : "v"((const char*)(ws + WS_FLG) + (fidx >= 0 ? fidx : FL_0 + b) * 4));
        K1_STAGE(11, 3 * K1_SLOT);
        WAITBAR(21); K1_STAGE(12, 0);
        WAITBAR(21); K1_STAGE(13, K1_SLOT);
        WAITBAR(20);
        asm volatile("" : "+v"(fseen));
        K1_STAGE(14, 2 * K1_SLOT);
        if (SYNC && !__all(fseen == tag)) flags_wait(ws, fidx, tag);
        u32x4 gs, gp[16];
        const int tc = 48 * j + 4 * lane;
        { const srd_t srs = mk_srd(ws + WS_S1, SEQ * 8), src1 = mk_srd(ws + WS_C1, 16 * 3072 * 4);
          const unsigned so = j == 0 ? (unsigned)(rt * 128) * 8u + lane * 16 : 0x7ff00000u, po = lane < 12 ? (unsigned)((tc >> 6) * 1024 + p * 64 + (tc & 63)) * 4u : 0x7ff00000u;
          asm volatile("buffer_load_dwordx4 %0, %1, %2, 0 offen sc1" : "=v"(gs) : "v"(so), "s"(srs));
#pragma unroll
          for (int i = 0; i < 16; ++i) asm volatile("buffer_load_dwordx4 %0, %1, %2, %3 offen sc1" : "=v"(gp[i]) : "v"(po), "s"(src1), "s"(i * 3072 * 4)); }
        WAITBAR(37); K1_STAGE(15, 3 * K1_SLOT);
        WAITBAR(37);
        WAITBAR(10);
        asm volatile("" : "+v"(gs), "+v"(gp[0]), "+v"(gp[1]), "+v"(gp[2]), "+v"(gp[3]), "+v"(gp[4]), "+v"(gp[5]), "+v"(gp[6]), "+v"(gp[7]));
        asm volatile("" : "+v"(gp[8]), "+v"(gp[9]), "+v"(gp[10]), "+v"(gp[11]), "+v"(gp[12]), "+v"(gp[13]), "+v"(gp[14]), "+v"(gp[15]));
        {
            f32x4 v = {0.f, 0.f, 0.f, 0.f};
#pragma unroll
            for (int i = 0; i < 16; ++i) v += __builtin_bit_cast(f32x4, gp[i]);
            if (lane < 12) *(LAS f32x4*)(csl + tc) = v;
            if (j == 0) *(LAS u32x4*)(lds + K1_AUX + lane * 16) = gs;
        }
        WAITBAR(0);
    } else {
        const int wr = w >> 1, wc = w & 1;
        CODE_TOUCH(".Lcw_p0", ".Lcw_p2", 0, 256); CODE_TOUCH(".Lcw_p5", ".Lcw_end", 0, 256);
        int xrd[2], wrd[2];
        { const int q4 = (lane >> 2) & 3, p4 = lane & 3, trow = 8 * g + q4, f = ((trow >> 1) & 1) | (((trow >> 3) & 1) << 1);
#pragma unroll
          for (int s = 0; s < 2; ++s) { xrd[s] = (64 * wr + r) * 128 + (((4 * s + g) ^ ((r >> 1) & 7)) * 16); wrd[s] = 16384 + trow * 128 + (((2 * wc + s) ^ f) * 32) + (p4 >> 1) * 16 + (p4 & 1) * 8; } }
        f32x4 acc[4][6];
#pragma unroll
        for (int m = 0; m < 4; ++m)
#pragma unroll
            for (int n = 0; n < 6; ++n) acc[m][n] = (f32x4){0.f, 0.f, 0.f, 0.f};
        bf16x8 xa[4], xb[4]; s16x4 wa[12], wb[12];
#define K1_RW(i, W, sl, s) W[i] = trd(lds + (sl) + wrd[((i) >> 1) & 1] + ((i) >> 2) * 8192 + (s) * 4096 + ((i) & 1) * 512)
#define K1_RX(i, X, sl, s) X[i] = ldf(lds + (sl) + xrd[s] + (i) * 2048)
#define K1_RD(i, X, W, sl, s) do { if ((i) == 2) K1_RX(0, X, sl, s); else if ((i) == 7) K1_RX(1, X, sl, s); else if ((i) == 12) K1_RX(2, X, sl, s); else if ((i) == 15) K1_RX(3, X, sl, s); \
        else K1_RW((i) < 2 ? (i) : (i) < 7 ? (i) - 1 : (i) < 12 ? (i) - 2 : (i) - 3, W, sl, s); } while (0)
#define K1_MM(i, X, W) do { const int m_ = (i) / 6, n_ = (i) % 6; const bf16x8 wf_ = cat8(W[2 * n_], W[2 * n_ + 1]); \
        acc[m_][n_] = (n_ < 4) ? MFMA16(wf_, X[m_], acc[m_][n_]) : MFMA16(X[m_], wf_, acc[m_][n_]); } while (0)
#define K1_HALF(XM, WM, XR, WR, sl, s, DOMMA) do { \
        _Pragma("unroll") for (int i_ = 0; i_ < 24; ++i_) { \
            if (DOMMA) K1_MM(i_, XM, WM); \
            if (i_ < 16) K1_RD(i_, XR, WR, sl, s); \
            SCHED0(); } } while (0)
        LBAR();
        K1_HALF(xb, wb, xa, wa, 0, 0, false);
        K1_HALF(xa, wa, xb, wb, 0, 1, true);
        if (wr == 0) {
            bf16x8 one8;
#pragma unroll
            for (int e = 0; e < 8; ++e) one8[e] = (short)0x3f80;
            const srd_t src1 = mk_srd(ws + WS_C1, 16 * 3072 * 4);
#pragma unroll
            for (int n = 0; n < 6; ++n) {
                f32x4 c = MFMA16(cat8(wa[2 * n], wa[2 * n + 1]), one8, ((f32x4){0.f, 0.f, 0.f, 0.f}));
                c = MFMA16(cat8(wb[2 * n], wb[2 * n + 1]), one8, c);
                __builtin_amdgcn_raw_buffer_store_b128(__builtin_bit_cast(u32x4, c), src1, r == 0 ? (unsigned)(rt * 3072 + (n >> 1) * 1024 + p * 64 + wc * 32 + (n & 1) * 16 + 4 * g) * 4u : 0x7ff00000u, 0, 16);
            }
        }
        int cur = K1_SLOT;
        LBAR();
        K1_HALF(xb, wb, xa, wa, cur, 0, true);
        K1_HALF(xa, wa, xb, wb, cur, 1, true);
        cur = 2 * K1_SLOT;
        LBAR();
        K1_HALF(xb, wb, xa, wa, cur, 0, true);
        K1_HALF(xa, wa, xb, wb, cur, 1, true);
        cur = 3 * K1_SLOT;
        WAITBAR(0);
        K1_HALF(xb, wb, xa, wa, cur, 0, true);
        K1_HALF(xa, wa, xb, wb, cur, 1, true);
        cur = 0;
#pragma unroll 1
        for (int t = 4; t < NT; ++t) {
            LBAR();
            K1_HALF(xb, wb, xa, wa, cur, 0, true);
            K1_HALF(xa, wa, xb, wb, cur, 1, true);
            cur = (cur == 3 * K1_SLOT) ? 0 : cur + K1_SLOT;
        }
#pragma unroll
        for (int i = 0; i < 24; ++i) K1_MM(i, xb, wb);
        const int head = 2 * p + wc;
        const srd_t srq = mk_srd(ws + WS_Q, 12u << 20);
        f32x4 cq[4]; float cv[2];
#pragma unroll
        for (int n = 0; n < 4; ++n) cq[n] = *(LAS const f32x4*)(csl + (n >> 1) * 64 + wc * 32 + (n & 1) * 16 + 4 * g);
#pragma unroll
        for (int n = 0; n < 2; ++n) cv[n] = csl[128 + wc * 32 + n * 16 + r];
        const int rho = 16 * (r >> 3) + 4 * ((r >> 2) & 1) + (r & 3);
        f32x4 vv[2][2];
#pragma unroll
        for (int m = 0; m < 4; ++m) {
            const int lrow = wr * 64 + m * 16, row0 = rt * 128 + lrow;
            const unsigned blk = ((unsigned)head * 64 + (row0 >> 5)) * 2048u + (g >> 1) * 1024 + (g & 1) * 512;
            const f32x2 st = stat[lrow + r];
            const float qs = st.y * QSCALE;
            st16(srq, (4u << 20) + blk + (rho + (m & 1) * 8) * 16, pk8((acc[m][2] - st.x * cq[2]) * st.y, (acc[m][3] - st.x * cq[3]) * st.y));
            const f32x4 s01 = *(LAS const f32x4*)(stat + lrow + 4 * g), s23 = *(LAS const f32x4*)(stat + lrow + 4 * g + 2);
            const f32x4 mean4 = {s01[0], s01[2], s23[0], s23[2]}, rstd4 = {s01[1], s01[3], s23[1], s23[3]};
#pragma unroll
            for (int n = 0; n < 2; ++n) vv[m & 1][n] = (acc[m][4 + n] - mean4 * cv[n]) * rstd4;
            if (m & 1) {
#pragma unroll
                for (int n = 0; n < 2; ++n) st16(srq, (8u << 20) + blk + (16 * n + r) * 16, pk8(vv[0][n], vv[1][n]));
            }
            st16(srq, blk + ((m & 1) * 16 + r) * 16, pk8((acc[m][0] - st.x * cq[0]) * qs, (acc[m][1] - st.x * cq[1]) * qs));
        }
        asm volatile(".Lcw_end:");
    }
    WAITBAR(0);
    if (SYNC && tid == 0) flag_set(ws, FL_1 + p * 16 + rt, tag);
    if (w < 6) line_touch(lds, (unsigned)(w >> 1) * (4u << 20) + ((unsigned)(2 * p + (w & 1)) * 64 + rt * 4) * 2048u + lane * 128, mk_srd(ws + WS_Q, 12u << 20));
    CODE_TOUCH_ASYNC(".Lcw_p1", ".Lcw_p2");
}

constexpr int K2_SLOT = 32768, K2_SIDE = 3 * K2_SLOT;
struct K2Side { srd_t src, srp, sro; unsigned cv, cs0, ps0; };
template <int KH>
__device__ __forceinline__ void attn_fast(LAS char* lds, srd_t srk, srd_t srv, unsigned hbase, const K2Side& S, int w, int lane, const bf16x8 (&qf)[2][2], f32x16 (&o)[2], f32x4 (&lacc)[2], f32x4& cs,
                                          char* ws, int fidx, unsigned tag, unsigned long long rdy, int rot) {
    const int tl = w & 3;
    const unsigned lds0 = (unsigned)(uintptr_t)lds;
    const unsigned lv = lane * 16;
    const unsigned s0 = hbase + (unsigned)(KH * 32 + tl) * 2048u, d0 = lds0 + KH * 16384 + tl * 2048;
    const int rd0 = KH * 16384 + lane * 16;
    bf16x8 ones, sel;
    { const short v = ((lane & 15) == ((lane >> 4) & 1)) ? (short)0x3f80 : (short)0;
#pragma unroll
      for (int j = 0; j < 8; ++j) { ones[j] = v; sel[j] = ((lane & 15) == j) ? (short)0x3f80 : (short)0; } }
    const unsigned sd = lds0 + K2_SIDE + w * 1024;
#define K2_DMA(k, rd, fr) do { \
        if ((k) < 4) { const int rr_ = ((rd) + 2 + rot) & 7; if ((k) == 0) wait_mask(ws, fidx, tag, 1ull << (KH * 8 + rr_), rdy); \
            dma16c(d0 + (fr) + ((k) >> 1) * 8192 + ((k) & 1) * 1024, lv, (k) < 2 ? srk : srv, s0 + rr_ * 8192 + ((k) & 1) * 1024); } \
        else if ((k) == 4) dma16(sd + sc, S.cv, S.src, S.cs0 + (rd) * (4 * FF * 2)); \
        else dma16(sd + 24576, lv, (rd) < 4 ? S.srp : S.sro, (rd) < 4 ? S.ps0 + (rd) * 1024 : (rd) == 4 ? (S.ps0 >> 2) : 0x7ff00000u); } while (0)
#define K2_HOOK(gi, rd, fr) do { if (((gi) >> 1) < 6 && ((gi) & 1) == KH) K2_DMA((gi) >> 1, rd, fr); } while (0)
    f32x16 sa[2], sb[2];
    const f32x16 zero16 = {};
    bf16x8 k0 = ldf(lds + rd0), k1 = ldf(lds + rd0 + 1024), v0, v1;
#pragma unroll
    for (int qt = 0; qt < 2; ++qt) { sa[qt] = MFMA32(k0, qf[qt][0], zero16); sa[qt] = MFMA32(k1, qf[qt][1], sa[qt]); }
    k0 = ldf(lds + rd0 + 2048);
    u32x4 pa = {0u, 0u, 0u, 0u}, pb = {0u, 0u, 0u, 0u}; float e6 = 0.f, e7 = 0.f;
    v1 = k1;
    bf16x8 csd = {};
#define EX2(x) __builtin_amdgcn_exp2f(x)
#define K2_GROUP(S_, B, PC, PP, QK, VP, QP, RD, gi) do { \
        QK; PP[3] = pk_bf16(e6, e7); SCHED0(); \
        const float x0 = EX2(S_[B + 0]), x1 = EX2(S_[B + 1]), x2 = EX2(S_[B + 2]); RD; SCHED0(); \
        o[QP] = MFMA32(VP, __builtin_bit_cast(bf16x8, PP), o[QP]); SCHED0(); \
        const float x3 = EX2(S_[B + 3]), x4 = EX2(S_[B + 4]), x5 = EX2(S_[B + 5]); PC[0] = pk_bf16(x0, x1); SCHED0(); \
        lacc[QP] = MFMA16(ones, __builtin_bit_cast(bf16x8, PP), lacc[QP]); K2_HOOK(gi, rd, fr); SCHED0(); \
        e6 = EX2(S_[B + 6]); e7 = EX2(S_[B + 7]); PC[1] = pk_bf16(x2, x3); PC[2] = pk_bf16(x4, x5); SCHED0(); } while (0)
#define K2_TILE(SC, SN, kn1, kn2, va, ti) do { \
        K2_GROUP(SC[0], 0, pa, pb, SN[0] = MFMA32(k0, qf[0][0], zero16), v1, 1, v0 = ldf(va), 4 * (ti)); \
        K2_GROUP(SC[1], 0, pb, pa, SN[1] = MFMA32(k0, qf[1][0], zero16), v0, 0, k1 = ldf((kn1) + 1024), 4 * (ti) + 1); \
        K2_GROUP(SC[0], 8, pa, pb, SN[0] = MFMA32(k1, qf[0][1], SN[0]), v0, 1, v1 = ldf((va) + 1024), 4 * (ti) + 2); \
        K2_GROUP(SC[1], 8, pb, pa, SN[1] = MFMA32(k1, qf[1][1], SN[1]), v1, 0, k0 = ldf(kn2), 4 * (ti) + 3); } while (0)
    int cur = 0, fr = 2 * K2_SLOT, sc = 0;
#pragma unroll 1
    for (int rd = 0; rd < 8; ++rd) {
        const int nx = (cur == 2 * K2_SLOT) ? 0 : cur + K2_SLOT;
        const int sn = (sc == 16384) ? 0 : sc + 8192;
        if (rd > 0) WAITBAR(2);
        LAS const char* pc = lds + cur + rd0; LAS const char* pn = lds + nx + rd0;
        K2_TILE(sa, sb, pc + 2048, pc + 4096, pc + 8192, 0);
        csd = ldf(lds + K2_SIDE + sn + w * 1024 + lane * 16);
        if (rd < 2) csd = (bf16x8){};
        K2_TILE(sb, sa, pc + 4096, pc + 6144, pc + 8192 + 2048, 1);
        cs = MFMA16(sel, csd, cs);
        K2_TILE(sa, sb, pc + 6144, pn, pc + 8192 + 4096, 2);
        K2_TILE(sb, sa, pn, pn + 2048, pc + 8192 + 6144, 3);
        fr = cur; cur = nx; sc = sn;
    }
    pb[3] = pk_bf16(e6, e7);
    o[1] = MFMA32(v1, __builtin_bit_cast(bf16x8, pb), o[1]);
    lacc[1] = MFMA16(ones, __builtin_bit_cast(bf16x8, pb), lacc[1]);
    WAITBAR(0);
    cs = MFMA16(sel, ldf(lds + K2_SIDE + w * 1024 + lane * 16), cs);
    cs = MFMA16(sel, ldf(lds + K2_SIDE + 8192 + w * 1024 + lane * 16), cs);
#undef K2_GROUP
#undef K2_TILE
#undef K2_DMA
#undef K2_HOOK
}
#define K2_STAGE(rd, slot) do { dma16c(d0 + (slot), lv, srk, s0 + (rd) * 8192); dma16c(d0 + (slot) + 1024, lv, srk, s0 + (rd) * 8192 + 1024); \
        dma16c(d0 + (slot) + 8192, lv, srv, s0 + (rd) * 8192); dma16c(d0 + (slot) + 9216, lv, srv, s0 + (rd) * 8192 + 1024); } while (0)
__device__ __forceinline__ void attn_safe(LAS char* lds, srd_t srk, srd_t srv, unsigned hbase, const bf16x8 (&qf)[2][2], int w, int lane, f32x16 (&o)[2], float (&mrun)[2], float (&lrun)[2]) {
    const int kh = w >> 2, tl = w & 3;
    const unsigned lds0 = (unsigned)(uintptr_t)lds;
    const unsigned lv = lane * 16;
    const unsigned s0 = hbase + (unsigned)(kh * 32 + tl) * 2048u, d0 = lds0 + kh * 16384 + tl * 2048;
    const int rd0 = kh * 16384 + lane * 16;
    const f32x16 zero16 = {};
#pragma unroll 1
    for (int rd = 0; rd < 8; ++rd) {
        K2_STAGE(rd, 0);
        WAITBAR(0);
#pragma unroll 1
        for (int i = 0; i < 4; ++i) {
            LAS const char* pk = lds + rd0 + i * 2048;
            const bf16x8 k0 = ldf(pk), k1 = ldf(pk + 1024), v0 = ldf(pk + 8192), v1 = ldf(pk + 9216);
#pragma unroll
            for (int qt = 0; qt < 2; ++qt) {
                f32x16 sc = MFMA32(k0, qf[qt][0], zero16); sc = MFMA32(k1, qf[qt][1], sc);
                float mx = sc[0];
#pragma unroll
                for (int j = 1; j < 16; ++j) mx = fmaxf(mx, sc[j]);
                mx = fmaxf(mx, __shfl_xor(mx, 32));
                const float mn = fmaxf(mrun[qt], mx);
                const float ms = (mn == -INFINITY) ? 0.f : mn;
                const float al = __builtin_amdgcn_exp2f(mrun[qt] - ms);
                mrun[qt] = mn;
                u32x4 p0, p1;
#pragma unroll
                for (int j = 0; j < 4; ++j) { p0[j] = pk_bf16(__builtin_amdgcn_exp2f(sc[2 * j] - ms), __builtin_amdgcn_exp2f(sc[2 * j + 1] - ms));
                                              p1[j] = pk_bf16(__builtin_amdgcn_exp2f(sc[8 + 2 * j] - ms), __builtin_amdgcn_exp2f(sc[9 + 2 * j] - ms)); }
                float s_ = 0.f;
#pragma unroll
                for (int j = 0; j < 4; ++j) { const unsigned a = p0[j], c = p1[j]; s_ += bf_lo(a) + bf_hi(a); s_ += bf_lo(c) + bf_hi(c); }
                lrun[qt] = lrun[qt] * al + s_;
#pragma unroll
                for (int j = 0; j < 16; ++j) o[qt][j] *= al;
                o[qt] = MFMA32(v0, __builtin_bit_cast(bf16x8, p0), o[qt]); o[qt] = MFMA32(v1, __builtin_bit_cast(bf16x8, p1), o[qt]);
            }
        }
        WAITBAR(0);
    }
}

template <bool SYNC>
__device__ __forceinline__ void phase2(LAS char* lds, const Params& P, unsigned tag) {
    const int tid = threadIdx.x, lane = tid & 63, w = __builtin_amdgcn_readfirstlane(tid >> 6);
    const int b = blockIdx.x, xcd = b & 7, loc = b >> 3;
    const int head = xcd * 4 + 2 * (loc & 1) + ((loc >> 1) & 1), qb = loc >> 2;
    char* ws = P.ws;
    K2Side S;
    const int cgrp = b % 56, rq = b / 56;
    const bool do_cs = b < 224;
    S.src = mk_srd(cgrp < 28 ? P.wg : P.wu, DM * FF * 2);
    S.cv = (unsigned)(lane >> 4) * (FF * 2) + (lane & 15) * 16;
    S.cs0 = do_cs ? (unsigned)(rq * 256 + w * 32) * (FF * 2) + (unsigned)(cgrp < 28 ? cgrp : cgrp - 28) * 256u : 0x7ff00000u;
    S.srp = mk_srd(P.wd, FF * DM * 2); S.sro = mk_srd(P.wo, DM * DM * 2);
    S.ps0 = (unsigned)(b * 8 + w) * 4096u;
    const srd_t srk = mk_srd(ws + WS_K, SEQ * DM * 2), srv = mk_srd(ws + WS_V, SEQ * DM * 2);
    const unsigned hbase = (unsigned)head * 64u * 2048u;
    const int kh = w >> 2;
    const int fidx = lane < 16 ? FL_1 + (head >> 1) * 16 + lane : -1, rot = (loc >> 1) & 7;
    unsigned long long rdy = SYNC ? 0ull : ~0ull;
    bf16x8 qf[2][2];
    {
        const unsigned lds0 = (unsigned)(uintptr_t)lds, lv = lane * 16;
        const unsigned s0 = hbase + (unsigned)(kh * 32 + (w & 3)) * 2048u, d0 = lds0 + kh * 16384 + (w & 3) * 2048;
        wait_mask(ws, fidx, tag, (1ull << (kh * 8 + rot)) | (1ull << (2 * qb + ((w & 3) >> 1))), rdy);
        K2_STAGE(rot, 0);
        const srd_t srq = mk_srd(ws + WS_Q, SEQ * DM * 2);
        const unsigned qo = ((unsigned)head * 64 + qb * 8 + (w & 3) * 2) * 2048u + lane * 16;
#pragma unroll
        for (int qt = 0; qt < 2; ++qt)
#pragma unroll
            for (int s = 0; s < 2; ++s) qf[qt][s] = __builtin_bit_cast(bf16x8, ldc16(srq, qo + qt * 2048 + s * 1024));
        wait_mask(ws, fidx, tag, 1ull << (kh * 8 + ((rot + 1) & 7)), rdy);
        K2_STAGE((rot + 1) & 7, K2_SLOT);
    }
    WAITBAR(0);
    f32x16 o[2] = {}; f32x4 lacc[2] = {}; f32x4 cs = {0.f, 0.f, 0.f, 0.f};
    if (kh == 0) attn_fast<0>(lds, srk, srv, hbase, S, w, lane, qf, o, lacc, cs, ws, fidx, tag, rdy, rot);
    else attn_fast<1>(lds, srk, srv, hbase, S, w, lane, qf, o, lacc, cs, ws, fidx, tag, rdy, rot);
    LAS float* ob = (LAS float*)lds;
    LAS float* lb = (LAS float*)(lds + 32768);
    LAS unsigned* flag = (LAS unsigned*)(lds + 32768 + 4096);
    LAS float* cb = (LAS float*)(lds + 40960);
    const int hi = lane >> 5, q32 = lane & 31;
    const srd_t sro = mk_srd(ws + WS_O, SEQ * DM * 2);
    const unsigned dst = FRAG(qb * 16 + (w & 3) * 4 + kh * 2 + (q32 >> 4), head, 32) + (hi * 16 + (q32 & 15)) * 16;
    bool bad;
    {
        if (tid == 0) flag[0] = 0u;
        if (lane < 16) { lb[w * 64 + lane] = lacc[0][0]; lb[w * 64 + 16 + lane] = lacc[0][1]; lb[w * 64 + 32 + lane] = lacc[1][0]; lb[w * 64 + 48 + lane] = lacc[1][1]; }
#pragma unroll
        for (int j = 0; j < 4; ++j) { const f32x4 v = kh ? (f32x4){o[0][4 * j], o[0][4 * j + 1], o[0][4 * j + 2], o[0][4 * j + 3]} : (f32x4){o[1][4 * j], o[1][4 * j + 1], o[1][4 * j + 2], o[1][4 * j + 3]};
            *(LAS f32x4*)(ob + ((w * 4 + j) * 64 + lane) * 4) = v; }
        if (lane < 32) *(LAS f32x4*)(cb + w * 128 + (lane & 15) * 8 + (lane >> 4) * 4) = cs;
        LBAR();
        const int pw = w ^ 4;
        f32x16 of;
#pragma unroll
        for (int j = 0; j < 4; ++j) { const f32x4 v = *(LAS const f32x4*)(ob + ((pw * 4 + j) * 64 + lane) * 4);
#pragma unroll
            for (int e = 0; e < 4; ++e) of[4 * j + e] = (kh ? o[1][4 * j + e] : o[0][4 * j + e]) + v[e]; }
        const float l = lb[w * 64 + kh * 32 + q32] + lb[pw * 64 + kh * 32 + q32];
        bool ok = (l >= 0x1p-80f) && (l <= 0x1p+110f);
#pragma unroll
        for (int j = 0; j < 16; ++j) ok = ok && (fabsf(of[j]) <= 0x1p+120f);
        if (!__all(ok)) { if (lane == 0) flag[0] = 1u; }
        if (do_cs && tid < 128) {
            float s = 0.f;
#pragma unroll
            for (int i = 0; i < 8; ++i) s += cb[i * 128 + tid];
            __hip_atomic_store((gu32*)(ws + WS_CS) + (rq * 7168 + cgrp * 128 + tid), __float_as_uint(s), RLX_AGENT);
        }
        LBAR();
        bad = flag[0] != 0u;
        if (!bad) {
            const float rl = 1.0f / l;
#pragma unroll
            for (int c = 0; c < 2; ++c) {
                u32x4 ov; ov.x = pk_bf16(of[8 * c + 0] * rl, of[8 * c + 1] * rl); ov.y = pk_bf16(of[8 * c + 2] * rl, of[8 * c + 3] * rl);
                ov.z = pk_bf16(of[8 * c + 4] * rl, of[8 * c + 5] * rl); ov.w = pk_bf16(of[8 * c + 6] * rl, of[8 * c + 7] * rl);
                st16(sro, dst + c * 512, ov);
            }
        }
    }
    if (bad) {
        WAITBAR(0);
        o[0] = (f32x16){}; o[1] = (f32x16){}; float mrun[2] = {-INFINITY, -INFINITY}, lrun[2] = {0.f, 0.f};
        attn_safe(lds, srk, srv, hbase, qf, w, lane, o, mrun, lrun);
        LAS float* mb = lb + 512;
#pragma unroll
        for (int qt = 0; qt < 2; ++qt) { const float lt = lrun[qt] + __shfl_xor(lrun[qt], 32); if (lane < 32) { lb[w * 64 + qt * 32 + lane] = lt; mb[w * 64 + qt * 32 + lane] = mrun[qt]; } }
#pragma unroll
        for (int j = 0; j < 16; ++j) ob[(w * 16 + j) * 64 + lane] = kh ? o[0][j] : o[1][j];
        WAITBAR(0);
        const int pw = w ^ 4;
        const float ma = mb[w * 64 + kh * 32 + q32], mp = mb[pw * 64 + kh * 32 + q32];
        const float mn = fmaxf(ma, mp), ms = (mn == -INFINITY) ? 0.f : mn;
        const float fa = __builtin_amdgcn_exp2f(ma - ms), fp = __builtin_amdgcn_exp2f(mp - ms);
        const float l = lb[w * 64 + kh * 32 + q32] * fa + lb[pw * 64 + kh * 32 + q32] * fp;
        const float rl = 1.0f / l;
        float of[16];
#pragma unroll
        for (int j = 0; j < 16; ++j) of[j] = ((kh ? o[1][j] : o[0][j]) * fa + ob[(pw * 16 + j) * 64 + lane] * fp) * rl;
#pragma unroll
        for (int c = 0; c < 2; ++c) {
            u32x4 ov; ov.x = pk_bf16(of[8 * c + 0], of[8 * c + 1]); ov.y = pk_bf16(of[8 * c + 2], of[8 * c + 3]);
            ov.z = pk_bf16(of[8 * c + 4], of[8 * c + 5]); ov.w = pk_bf16(of[8 * c + 6], of[8 * c + 7]);
            st16(sro, dst + c * 512, ov);
        }
    }
    WAITBAR(0);
    if (SYNC && tid == 0) flag_set(ws, FL_2 + qb * 32 + head, tag);
    CODE_TOUCH_ASYNC(".Lcw_p2", ".Lcw_p3");
}
#undef K2_STAGE

constexpr int K3_SLOT = 24576, K3_NS = 6, K3_AUX = K3_NS * K3_SLOT;
template <int MODE, bool SYNC>
__device__ __forceinline__ void phase35(LAS char* lds, const Params& P, unsigned tag) {
    constexpr int KD = MODE == 0 ? DM : FF, KT = KD / 32, NT = KD / 64;
    const unsigned lds0 = (unsigned)(uintptr_t)lds;
    const int tid = threadIdx.x, lane = tid & 63, w = __builtin_amdgcn_readfirstlane(tid >> 6), g = lane >> 4, r = lane & 15;
    const int b = blockIdx.x, xcd = b & 7, loc = b >> 3;
    const int rt = (xcd >> 1) * 4 + (loc & 3), ct = (xcd & 1) * 8 + (loc >> 2);
    char* ws = P.ws;
    if (w >= 4) {
        const int j = w - 4;
        const srd_t srx = mk_srd(ws + (MODE == 0 ? WS_O : WS_ACT), SEQ * KD * 2), srw = mk_srd(MODE == 0 ? P.wo : P.wd, KD * DM * 2);
        const unsigned xv = lane * 16, xs0 = FRAG(rt * 8 + 2 * j, 0, KT);
        constexpr unsigned XM = FRAG(1, 0, KT), XK = FRAG(0, 1, KT);
        const int wrow = lane >> 3, wch = (lane & 7) ^ (((wrow >> 1) & 3) << 1);
        const unsigned wv = (unsigned)wrow * 2048u + wch * 16, ws0 = (unsigned)ct * 128u + (unsigned)(16 * j) * 2048u;
        const unsigned ld0 = lds0 + j * 4096, ld1 = lds0 + 16384 + j * 2048;
        const int rot = 2 * xcd, fidx = lane < 32 ? FL_2 + (rt >> 1) * 32 + lane : -1;
        unsigned long long rdy = SYNC ? 0ull : ~0ull;
#define K3_T(t) (((t) + rot) & 15)
#define K3_W(t, slot) do { dma16(ld1 + (slot), wv, srw, ws0 + K3_T(t) * (64 * 2048)); dma16(ld1 + (slot) + 1024, wv, srw, ws0 + 8 * 2048 + K3_T(t) * (64 * 2048)); } while (0)
#define K3_X(t, slot) do { wait_mask(ws, fidx, tag, 3ull << (2 * K3_T(t)), rdy); \
        dma16c(ld0 + (slot), xv, srx, xs0 + K3_T(t) * (2 * XK)); dma16c(ld0 + (slot) + 1024, xv, srx, xs0 + K3_T(t) * (2 * XK) + XK); \
        dma16c(ld0 + (slot) + 2048, xv, srx, xs0 + XM + K3_T(t) * (2 * XK)); dma16c(ld0 + (slot) + 3072, xv, srx, xs0 + XM + K3_T(t) * (2 * XK) + XK); } while (0)
        K3_W(0, 0); K3_W(1, K3_SLOT); K3_W(2, 2 * K3_SLOT); K3_W(3, 3 * K3_SLOT); K3_W(4, 4 * K3_SLOT);
        K3_X(0, 0); K3_X(1, K3_SLOT); K3_X(2, 2 * K3_SLOT);
        WAITBAR(8);  K3_X(3, 3 * K3_SLOT); K3_X(4, 4 * K3_SLOT); K3_X(5, 5 * K3_SLOT); K3_W(5, 5 * K3_SLOT);
        WAITBAR(18); K3_X(6, 0); K3_W(6, 0);
        WAITBAR(20); K3_X(7, K3_SLOT); K3_W(7, K3_SLOT);
        WAITBAR(22); K3_X(8, 2 * K3_SLOT); K3_W(8, 2 * K3_SLOT);
        int fr = 3 * K3_SLOT;
#pragma unroll 1
        for (int t = 4; t + K3_NS - 1 < NT; ++t) {
            WAITBAR(6 * (K3_NS - 2));
            K3_X(t + K3_NS - 1, fr); K3_W(t + K3_NS - 1, fr);
            fr = (fr == (K3_NS - 1) * K3_SLOT) ? 0 : fr + K3_SLOT;
        }
        WAITBAR(24); WAITBAR(18); WAITBAR(12); WAITBAR(6); WAITBAR(0);
        if (MODE == 0) LBAR();
    } else {
        const int wm = w >> 1, wn = w & 1;
        const int col0 = ct * 64 + wn * 32 + 4 * g;
        u32x4 res[4];
#pragma unroll
        for (int m = 0; m < 4; ++m) {
            const int mrow = rt * 128 + wm * 64 + m * 16;
            if (MODE == 0) { const u32x2 a = *(const u32x2*)(P.x + (size_t)(mrow + r) * DM + col0), c = *(const u32x2*)(P.x + (size_t)(mrow + r) * DM + col0 + 16); res[m] = (u32x4){a.x, a.y, c.x, c.y}; }
            else res[m] = ldc16(mk_srd(ws + WS_H, SEQ * DM * 2), FRAG(mrow >> 4, ct * 2 + wn, 32) + lane * 16);
        }
        const int xrd = wm * 8192 + lane * 16;
        const int q4 = (lane >> 2) & 3, p4 = lane & 3;
        const int trow = (MODE == 0) ? (4 * (g & 1) + 16 * (g >> 1) + q4) : (4 * g + q4);
        constexpr int TR2 = (MODE == 0) ? 8 * 128 : 16 * 128;
        int wrd[2];
#pragma unroll
        for (int n = 0; n < 2; ++n) wrd[n] = 16384 + trow * 128 + (((wn * 2 + n) ^ ((trow >> 1) & 3)) * 32) + (p4 >> 1) * 16 + (p4 & 1) * 8;
        f32x4 acc[4][2];
#pragma unroll
        for (int m = 0; m < 4; ++m) { acc[m][0] = (f32x4){0.f, 0.f, 0.f, 0.f}; acc[m][1] = (f32x4){0.f, 0.f, 0.f, 0.f}; }
        bf16x8 xa[4], xb[4]; s16x4 wa[4], wb[4];
#define K3_RD(i, X, W, sl, s) do { if ((i) == 2) X[0] = ldf(lds + (sl) + xrd + (s) * 1024); else if ((i) >= 5) X[(i) - 4] = ldf(lds + (sl) + xrd + ((i) - 4) * 2048 + (s) * 1024); \
        else { const int k_ = (i) < 2 ? (i) : (i) - 1; W[k_] = trd(lds + (sl) + wrd[k_ >> 1] + (k_ & 1) * TR2 + (s) * 4096); } } while (0)
#define K3_MM(i, X, W) do { const int m_ = (i) >> 1, n_ = (i) & 1; acc[m_][n_] = MFMA16(cat8(W[2 * n_], W[2 * n_ + 1]), X[m_], acc[m_][n_]); } while (0)
#define K3_HALF(XM, WM, XR, WR, sl, s, DOMMA) do { _Pragma("unroll") for (int i_ = 0; i_ < 8; ++i_) { if (DOMMA) K3_MM(i_, XM, WM); K3_RD(i_, XR, WR, sl, s); SCHED0(); } } while (0)
        LBAR();
        K3_HALF(xb, wb, xa, wa, 0, 0, false);
        K3_HALF(xa, wa, xb, wb, 0, 1, true);
        int cur = K3_SLOT;
#pragma unroll 1
        for (int t = 1; t < NT; ++t) {
            LBAR();
            K3_HALF(xb, wb, xa, wa, cur, 0, true);
            K3_HALF(xa, wa, xb, wb, cur, 1, true);
            cur = (cur == (K3_NS - 1) * K3_SLOT) ? 0 : cur + K3_SLOT;
        }
#pragma unroll
        for (int i = 0; i < 8; ++i) K3_MM(i, xb, wb);
        if (MODE == 0) {
            LAS f32x2* sb = (LAS f32x2*)(lds + K3_AUX);
            const srd_t srh = mk_srd(ws + WS_H, SEQ * DM * 2);
#pragma unroll
            for (int m = 0; m < 4; ++m) {
                const int mrow = rt * 128 + wm * 64 + m * 16;
                const f32x4 a = {bf_lo(res[m].x) + acc[m][0][0], bf_hi(res[m].x) + acc[m][0][1], bf_lo(res[m].y) + acc[m][0][2], bf_hi(res[m].y) + acc[m][0][3]};
                const f32x4 c = {bf_lo(res[m].z) + acc[m][1][0], bf_hi(res[m].z) + acc[m][1][1], bf_lo(res[m].w) + acc[m][1][2], bf_hi(res[m].w) + acc[m][1][3]};
                const u32x4 hv = pk8(a, c);
                st16(srh, FRAG(mrow >> 4, ct * 2 + wn, 32) + lane * 16, hv);
                float s = 0.f, q = 0.f;
#pragma unroll
                for (int j = 0; j < 4; ++j) { const unsigned d = hv[j]; const float u = bf_lo(d), v = bf_hi(d); s += u + v; q += u * u + v * v; }
                s += __shfl_xor(s, 16); s += __shfl_xor(s, 32); q += __shfl_xor(q, 16); q += __shfl_xor(q, 32);
                if (g == 0) sb[(wm * 64 + m * 16 + r) * 2 + wn] = (f32x2){s, q};
            }
            LBAR();
            if (tid < 128) { const f32x2 a = sb[tid * 2] + sb[tid * 2 + 1];
                __hip_atomic_store((gu64*)(ws + WS_ST) + ((size_t)(rt * 128 + tid) * 16 + ct), ((unsigned long long)__float_as_uint(a[1]) << 32) | __float_as_uint(a[0]), RLX_AGENT); }
        } else {
#pragma unroll
            for (int m = 0; m < 4; ++m) {
                const int mrow = rt * 128 + wm * 64 + m * 16;
                unsigned a0 = pk_bf16(bf_lo(res[m].x) + acc[m][0][0], bf_hi(res[m].x) + acc[m][0][1]), a1 = pk_bf16(bf_lo(res[m].y) + acc[m][0][2], bf_hi(res[m].y) + acc[m][0][3]);
                unsigned b0 = pk_bf16(bf_lo(res[m].z) + acc[m][1][0], bf_hi(res[m].z) + acc[m][1][1]), b1 = pk_bf16(bf_lo(res[m].w) + acc[m][1][2], bf_hi(res[m].w) + acc[m][1][3]);
                const auto s0 = __builtin_amdgcn_permlane16_swap(b0, a0, false, false), s1 = __builtin_amdgcn_permlane16_swap(b1, a1, false, false);
                const u32x4 ov = {s0[0], s1[0], s0[1], s1[1]};
                const int c8 = ((g & 1) ? 0 : 16) + (g >> 1) * 8;
                *(u32x4*)(P.out + (size_t)(mrow + r) * DM + ct * 64 + wn * 32 + c8) = ov;
            }
        }
    }
    if (MODE == 0) {
        WAITBAR(0);
        if (SYNC && tid == 0) flag_set(ws, FL_3 + rt * 16 + ct, tag);
        CODE_TOUCH_ASYNC(".Lcw_p3", ".Lcw_p4");
    }
}

constexpr int K5_SLOT = 32768, K5_NS = 4;
template <int KH>
__device__ __forceinline__ void p5_finish(const Params& P, const f32x4 (&acc)[4][4], unsigned tag, int b, int w, int lane, int rt, int ct) {
    char* ws = P.ws;
    const int g = lane >> 4, r = lane & 15, wm = w >> 1, wn = w & 1;
    const srd_t sre = mk_srd(ws + WS_XCH, 8u << 20);
    const unsigned eo = (unsigned)(w * 8192) + lane * 16;
#pragma unroll
    for (int m = 0; m < 4; ++m)
#pragma unroll
        for (int n = 0; n < 2; ++n) st16(sre, (unsigned)b * 32768u + eo + (m * 2 + n) * 1024, __builtin_bit_cast(u32x4, acc[m][2 * (1 - KH) + n]));
    u32x4 res[4];
#pragma unroll
    for (int m = 0; m < 4; ++m) res[m] = ldc16(mk_srd(ws + WS_H, SEQ * DM * 2), FRAG(rt * 8 + wm * 4 + m, ct * 4 + wn * 2 + KH, 32) + lane * 16);
    WAITV(0);
    if (lane == 0) flag_set(ws, FL_5 + b * 4 + w, tag);
    flags_wait(ws, lane == 0 ? FL_5 + (b ^ 2) * 4 + w : -1, tag);
    u32x4 pv[4][2];
#pragma unroll
    for (int m = 0; m < 4; ++m)
#pragma unroll
        for (int n = 0; n < 2; ++n) pv[m][n] = ldc16(sre, (unsigned)(b ^ 2) * 32768u + eo + (m * 2 + n) * 1024);
#pragma unroll
    for (int m = 0; m < 4; ++m) {
        const int mrow = rt * 128 + wm * 64 + m * 16;
        const f32x4 t0 = acc[m][2 * KH] + __builtin_bit_cast(f32x4, pv[m][0]), t1 = acc[m][2 * KH + 1] + __builtin_bit_cast(f32x4, pv[m][1]);
        unsigned a0 = pk_bf16(bf_lo(res[m].x) + t0[0], bf_hi(res[m].x) + t0[1]), a1 = pk_bf16(bf_lo(res[m].y) + t0[2], bf_hi(res[m].y) + t0[3]);
        unsigned b0 = pk_bf16(bf_lo(res[m].z) + t1[0], bf_hi(res[m].z) + t1[1]), b1 = pk_bf16(bf_lo(res[m].w) + t1[2], bf_hi(res[m].w) + t1[3]);
        const auto s0 = __builtin_amdgcn_permlane16_swap(b0, a0, false, false), s1 = __builtin_amdgcn_permlane16_swap(b1, a1, false, false);
        const u32x4 ov = {s0[0], s1[0], s0[1], s1[1]};
        const int c8 = ((g & 1) ? 0 : 16) + (g >> 1) * 8;
        *(u32x4*)(P.out + (size_t)(mrow + r) * DM + ct * 128 + wn * 64 + KH * 32 + c8) = ov;
    }
}
template <bool SYNC>
__device__ __forceinline__ void phase5(LAS char* lds, const Params& P, unsigned tag) {
    constexpr int KT = FF / 32, NT = FF / 128;
    const unsigned lds0 = (unsigned)(uintptr_t)lds;
    const int tid = threadIdx.x, lane = tid & 63, w = __builtin_amdgcn_readfirstlane(tid >> 6), g = lane >> 4, r = lane & 15;
    const int b = blockIdx.x, xcd = b & 7, loc = b >> 3;
    const int kh = (xcd >> 1) & 1, rt = (xcd >> 2) * 8 + (xcd & 1) * 4 + (loc & 3), ct = loc >> 2;
    char* ws = P.ws;
    if (w >= 4) {
        const int j = w - 4;
        const srd_t srx = mk_srd(ws + WS_ACT, SEQ * FF * 2), srw = mk_srd(P.wd, FF * DM * 2);
        constexpr unsigned XM = FRAG(1, 0, KT), XK = FRAG(0, 1, KT);
        const unsigned xv = lane * 16, xs0 = FRAG(rt * 8 + 2 * j, kh * (KT / 2), KT);
        const int wrow = lane >> 3, wch = (lane & 7) ^ (((wrow >> 1) & 3) << 1);
        const unsigned wv = (unsigned)wrow * 2048u + wch * 16, ws0 = (unsigned)ct * 256u + (unsigned)(kh * (FF / 2) + 16 * j) * 2048u;
        const unsigned ld0 = lds0 + j * 4096, ld1 = lds0 + 16384 + j * 2048;
        const int rot = 14 * (xcd & 1), fidx = lane < 16 ? FL_4 + (rt >> 1) * 32 + kh * 16 + lane : -1;
        unsigned long long rdy = SYNC ? 0ull : ~0ull;
#define K5_T(t) ((t) + rot >= NT ? (t) + rot - NT : (t) + rot)
#define K5_W(t, slot) do { const unsigned o_ = ws0 + K5_T(t) * (64 * 2048); dma16(ld1 + (slot), wv, srw, o_); dma16(ld1 + (slot) + 1024, wv, srw, o_ + 8 * 2048); \
        dma16(ld1 + (slot) + 8192, wv, srw, o_ + 128); dma16(ld1 + (slot) + 9216, wv, srw, o_ + 128 + 8 * 2048); } while (0)
#define K5_X(t, slot) do { const int tt_ = K5_T(t); wait_mask(ws, fidx, tag, (1ull << ((4 * tt_) / 7)) | (1ull << ((4 * tt_ + 3) / 7)), rdy); const unsigned o_ = xs0 + tt_ * (2 * XK); \
        dma16c(ld0 + (slot), xv, srx, o_); dma16c(ld0 + (slot) + 1024, xv, srx, o_ + XK); dma16c(ld0 + (slot) + 2048, xv, srx, o_ + XM); dma16c(ld0 + (slot) + 3072, xv, srx, o_ + XM + XK); } while (0)
        K5_W(0, 0); K5_W(1, K5_SLOT); K5_W(2, 2 * K5_SLOT); K5_W(3, 3 * K5_SLOT);
        K5_X(0, 0); K5_X(1, K5_SLOT); K5_X(2, 2 * K5_SLOT); K5_X(3, 3 * K5_SLOT);
        WAITBAR(12);
        WAITBAR(8);  K5_X(4, 0); K5_W(4, 0);
        WAITBAR(12); K5_X(5, K5_SLOT); K5_W(5, K5_SLOT);
        WAITBAR(16); K5_X(6, 2 * K5_SLOT); K5_W(6, 2 * K5_SLOT);
        int fr = 3 * K5_SLOT;
#pragma unroll 1
        for (int t = 4; t + 3 < NT; ++t) {
            WAITBAR(16);
            K5_X(t + 3, fr); K5_W(t + 3, fr);
            fr = (fr == 3 * K5_SLOT) ? 0 : fr + K5_SLOT;
        }
        WAITBAR(16); WAITBAR(8); WAITBAR(0);
    } else {
        const int wm = w >> 1, wn = w & 1;
        const int xrd = wm * 8192 + lane * 16;
        const int q4 = (lane >> 2) & 3, p4 = lane & 3, trow = 4 * g + q4;
        int wrd[4];
#pragma unroll
        for (int n = 0; n < 4; ++n) wrd[n] = 16384 + wn * 8192 + trow * 128 + ((n ^ ((trow >> 1) & 3)) * 32) + (p4 >> 1) * 16 + (p4 & 1) * 8;
        f32x4 acc[4][4];
#pragma unroll
        for (int m = 0; m < 4; ++m)
#pragma unroll
            for (int n = 0; n < 4; ++n) acc[m][n] = (f32x4){0.f, 0.f, 0.f, 0.f};
        bf16x8 xa[4], xb[4]; s16x4 wa[8], wb[8];
#define K5_RD(i, X, W, sl, s) do { if ((i) == 2) X[0] = ldf(lds + (sl) + xrd + (s) * 1024); else if ((i) >= 9) X[(i) - 8] = ldf(lds + (sl) + xrd + ((i) - 8) * 2048 + (s) * 1024); \
        else { const int k_ = (i) < 2 ? (i) : (i) - 1; W[k_] = trd(lds + (sl) + wrd[k_ >> 1] + (k_ & 1) * 2048 + (s) * 4096); } } while (0)
#define K5_MM(i, X, W) do { const int m_ = (i) >> 2, n_ = (i) & 3; acc[m_][n_] = MFMA16(cat8(W[2 * n_], W[2 * n_ + 1]), X[m_], acc[m_][n_]); } while (0)
#define K5_HALF(XM_, WM_, XR, WR, sl, s, DOMMA) do { _Pragma("unroll") for (int i_ = 0; i_ < 16; ++i_) { if (DOMMA) K5_MM(i_, XM_, WM_); if (i_ < 12) K5_RD(i_, XR, WR, sl, s); SCHED0(); } } while (0)
        LBAR();
        K5_HALF(xb, wb, xa, wa, 0, 0, false);
        K5_HALF(xa, wa, xb, wb, 0, 1, true);
        int cur = K5_SLOT;
#pragma unroll 1
        for (int t = 1; t < NT; ++t) {
            LBAR();
            K5_HALF(xb, wb, xa, wa, cur, 0, true);
            K5_HALF(xa, wa, xb, wb, cur, 1, true);
            cur = (cur == 3 * K5_SLOT) ? 0 : cur + K5_SLOT;
        }
#pragma unroll
        for (int i = 0; i < 16; ++i) K5_MM(i, xb, wb);
        if (kh) p5_finish<1>(P, acc, tag, b, w, lane, rt, ct); else p5_finish<0>(P, acc, tag, b, w, lane, rt, ct);
    }
}

constexpr int K4_SLOT = 30720, K4_NS = 4, K4_STRAW = K4_NS * K4_SLOT, K4_STAT = K4_STRAW + 32768, K4_CS = K4_STAT + 2048, K4_CSRAW = K4_CS + 1024;
struct K4Dma { srd_t srx, srg, sru; unsigned xv, gv, xs0, gs0, ld0, ld1; };
#define K4_DMA(i, t, slot) do { if ((i) < 2) dma16c(D.ld0 + (slot) + (i) * 1024, D.xv, D.srx, D.xs0 + (t) * FRAG(0, 1, 32) + (i) * FRAG(1, 0, 32)); \
        else dma16(D.ld1 + (slot) + ((i) - 2) * 7168, D.gv, (i) == 2 ? D.srg : D.sru, D.gs0 + (t) * (32 * FF * 2)); } while (0)
template <int NJ, int J0, bool HASW>
__device__ __forceinline__ void k4_body(LAS char* lds, const K4Dma& D, char* ws, int w, int lane, int rt, int ct) {
    const int wm = w & 3, g = lane >> 4, r = lane & 15;
    constexpr int NP = HASW ? 4 : 2;
    const int xrd = wm * 4096 + lane * 16;
    const int q4 = (lane >> 2) & 3, p4 = lane & 3;
    const int wrd = 16384 + (4 * g + q4) * 224 + J0 * 32 + (p4 >> 1) * 16 + (p4 & 1) * 8;
    f32x4 ag[4][NJ], au[4][NJ];
#pragma unroll
    for (int m = 0; m < 4; ++m)
#pragma unroll
        for (int j = 0; j < NJ; ++j) { ag[m][j] = (f32x4){0.f, 0.f, 0.f, 0.f}; au[m][j] = (f32x4){0.f, 0.f, 0.f, 0.f}; }
    bf16x8 xa[4], xb[4]; s16x4 wgf[2 * NJ], wuf[2 * NJ];
#define K4_RW(i, W, sl, o) W[i] = trd(lds + (sl) + wrd + (o) + ((i) >> 1) * 32 + ((i) & 1) * (16 * 224))
#define K4_RX(i, X, sl) X[i] = ldf(lds + (sl) + xrd + (i) * 1024)
#define K4_MM(i, A, X, W) do { const int j_ = (i) >> 2, m_ = (i) & 3; A[m_][j_] = MFMA16(cat8(W[2 * j_], W[2 * j_ + 1]), X[m_], A[m_][j_]); } while (0)
#define K4_RD1(i, XC, sl) do { if ((i) == 0) K4_RX(0, XC, sl); else if ((i) < 3) K4_RW((i) - 1, wgf, sl, 0); else if ((i) < 6) K4_RX((i) - 2, XC, sl); else if ((i) < 4 + 2 * NJ) K4_RW((i) - 4, wgf, sl, 0); } while (0)
#define K4_H1(XC, XP, DOMMA, DODMA, t, sl, fsl) do { \
        _Pragma("unroll") for (int i_ = 0; i_ < 4 * NJ; ++i_) { \
            if (DOMMA) K4_MM(i_, au, XP, wuf); \
            K4_RD1(i_, XC, sl); \
            if (DODMA) { if (i_ == (NJ == 4 ? 13 : 11)) K4_DMA(0, t, fsl); } \
            SCHED0(); } } while (0)
#define K4_H2(XC, sl, DODMA, t, fsl) do { \
        _Pragma("unroll") for (int i_ = 0; i_ < 4 * NJ; ++i_) { \
            K4_MM(i_, ag, XC, wgf); \
            if (i_ < 2 * NJ) K4_RW(i_, wuf, sl, 7168); \
            if (DODMA) { if (NJ == 4) { if (i_ == 8) K4_DMA(1, t, fsl); if (i_ == 11 && NP == 4) K4_DMA(2, t, fsl); if (i_ == 14 && NP == 4) K4_DMA(3, t, fsl); } \
                         else { if (i_ == 6) K4_DMA(1, t, fsl); if (i_ == 8 && NP == 4) K4_DMA(2, t, fsl); if (i_ == 10 && NP == 4) K4_DMA(3, t, fsl); } } \
            SCHED0(); } } while (0)
    constexpr int NT = DM / 32;
    constexpr int NX = HASW ? 4 : 8;
    WAITBAR(4 + NX);
    K4_H1(xa, xb, false, true, 3, 0, 3 * K4_SLOT);
    K4_H2(xa, 0, true, 3, 3 * K4_SLOT);
    int cur = K4_SLOT, fr = 0;
#define K4_ADV() do { fr = (fr == (K4_NS - 1) * K4_SLOT) ? 0 : fr + K4_SLOT; cur = (cur == (K4_NS - 1) * K4_SLOT) ? 0 : cur + K4_SLOT; } while (0)
    WAITBAR(2 + NX + NP); K4_H1(xb, xa, true, true, 4, cur, fr); K4_H2(xb, cur, true, 4, fr); K4_ADV();
    WAITBAR(NX + 2 * NP); K4_H1(xa, xb, true, true, 5, cur, fr); K4_H2(xa, cur, true, 5, fr); K4_ADV();
    WAITBAR(2 * NP);
    {
        const int tid = w * 64 + lane, row = tid >> 1, hf = tid & 1;
        float s = 0.f, q = 0.f;
#pragma unroll
        for (int i = 0; i < 4; ++i) { const f32x4 v = *(const LAS f32x4*)(lds + K4_STRAW + row * 128 + hf * 64 + i * 16); s += v[0] + v[2]; q += v[1] + v[3]; }
        s += __shfl_xor(s, 1); q += __shfl_xor(q, 1);
        const float mean = s * (1.0f / DM);
        const float var = fmaxf(q * (1.0f / DM) - mean * mean, 0.f);
        if (hf == 0) ((LAS f32x2*)(lds + K4_STAT))[row] = (f32x2){mean, 1.0f / sqrtf(var + LN_EPS)};
        if (tid < 224) { const LAS float* cr = (const LAS float*)(lds + K4_CSRAW) + tid; ((LAS float*)(lds + K4_CS))[tid] = (cr[0] + cr[256]) + (cr[512] + cr[768]); }
        SCHED0();
    }
    K4_H1(xb, xa, true, true, 6, cur, fr); K4_H2(xb, cur, true, 6, fr); K4_ADV();
    int t = 4;
#pragma unroll 1
    for (; t + 4 < NT; t += 2) {
        WAITBAR(2 * NP); K4_H1(xa, xb, true, true, t + 3, cur, fr); K4_H2(xa, cur, true, t + 3, fr); K4_ADV();
        WAITBAR(2 * NP); K4_H1(xb, xa, true, true, t + 4, cur, fr); K4_H2(xb, cur, true, t + 4, fr); K4_ADV();
    }
    WAITBAR(2 * NP); K4_H1(xa, xb, true, true, t + 3, cur, fr); K4_H2(xa, cur, true, t + 3, fr); K4_ADV();
    WAITBAR(2 * NP); K4_H1(xb, xa, true, false, 0, cur, fr); K4_H2(xb, cur, false, 0, fr); K4_ADV();
    WAITBAR(NP);     K4_H1(xa, xb, true, false, 0, cur, fr); K4_H2(xa, cur, false, 0, fr); K4_ADV();
    WAITBAR(0);      K4_H1(xb, xa, true, false, 0, cur, fr); K4_H2(xb, cur, false, 0, fr);
#pragma unroll
    for (int i = 0; i < 4 * NJ; ++i) K4_MM(i, au, xb, wuf);
    const LAS f32x2* st = (const LAS f32x2*)(lds + K4_STAT);
    const LAS float* cs = (const LAS float*)(lds + K4_CS);
    f32x2 ms[4];
#pragma unroll
    for (int m = 0; m < 4; ++m) ms[m] = st[wm * 64 + m * 16 + r];
    const srd_t sra = mk_srd(ws + WS_ACT, SEQ * FF * 2);
    const unsigned ap0 = FRAG(rt * 16 + wm * 4, 0, 112) + lane * 16;
    const int ft0 = ct * 7 + J0;
    constexpr int LONE = (NJ == 3) ? (J0 == 4 ? 2 : 0) : -1;
    u32x2 keep[4];
#pragma unroll
    for (int j = 0; j < NJ; ++j) {
        const f32x4 cg = *(const LAS f32x4*)(cs + (J0 + j) * 16 + 4 * g), cu = *(const LAS f32x4*)(cs + 112 + (J0 + j) * 16 + 4 * g);
        const bool second = (LONE == 0) ? (j == 2) : ((j & 1) == 1);
#pragma unroll
        for (int m = 0; m < 4; ++m) {
            const float c1 = -LOG2E * ms[m].y;
            const f32x4 gz = (ag[m][j] - cg * ms[m].x) * c1;
            const f32x4 uu = (au[m][j] - cu * ms[m].x) * ms[m].y;
            float a[4];
#pragma unroll
            for (int e = 0; e < 4; ++e) a[e] = uu[e] * __builtin_amdgcn_rcpf(1.0f + __builtin_amdgcn_exp2f(gz[e]));
            u32x2 ov; ov.x = pk_bf16(a[0], a[1]); ov.y = pk_bf16(a[2], a[3]);
            const int ft = ft0 + j;
            if (j == LONE) st8(sra, ap0 + FRAG(m, ft >> 1, 112) + (ft & 1) * 8, ov);
            else if (!second) keep[m] = ov;
            else st16(sra, ap0 + FRAG(m, ft >> 1, 112), (u32x4){keep[m].x, keep[m].y, ov.x, ov.y});
        }
        SCHED0();
    }
}

template <bool SYNC>
__device__ __forceinline__ void phase4(LAS char* lds, const Params& P, unsigned tag) {
    const unsigned lds0 = (unsigned)(uintptr_t)lds;
    const int tid = threadIdx.x, lane = tid & 63, w = __builtin_amdgcn_readfirstlane(tid >> 6);
    const int b = blockIdx.x, xcd = b & 7, loc = b >> 3;
    const int rt = (xcd >> 2) * 4 + (loc & 3), ct = (xcd & 3) * 8 + (loc >> 2);
    char* ws = P.ws;
    K4Dma D;
    D.srx = mk_srd(ws + WS_H, SEQ * DM * 2); D.srg = mk_srd(P.wg, DM * FF * 2); D.sru = mk_srd(P.wu, DM * FF * 2);
    D.xv = lane * 16; D.xs0 = FRAG(rt * 16 + w * 2, 0, 32);
    { const int ci = (w < 7 ? w : 6) * 64 + lane; const int row = ci / 14, c = ci % 14; D.gv = (unsigned)row * (FF * 2) + c * 16; }
    D.gs0 = (unsigned)ct * 224u; D.ld0 = lds0 + w * 2048; D.ld1 = lds0 + 16384 + w * 1024;
    if (w < 7) {
#pragma unroll
        for (int t = 0; t < 3; ++t) { K4_DMA(2, t, t * K4_SLOT); K4_DMA(3, t, t * K4_SLOT); }
    }
    if (SYNC) {
        if (w == 7) {
            int idx = -1;
            if (lane < 32) idx = FL_3 + rt * 32 + lane;
            else if (lane < 48) { const int k = lane - 32, gsel = k & 3, rq = k >> 2;
                const int c0 = (ct * 112 + ((gsel & 1) ? 111 : 0)) >> 7, bb = rq * 56 + (gsel >> 1) * 28 + c0;
                idx = FL_2 + (bb >> 5) * 32 + (bb & 7) * 4 + 2 * ((bb >> 3) & 1) + ((bb >> 4) & 1); }
            flags_wait(ws, idx, tag);
        }
        LBAR();
    }
#pragma unroll
    for (int t = 0; t < 3; ++t) { K4_DMA(0, t, t * K4_SLOT); K4_DMA(1, t, t * K4_SLOT); }
    {
        const srd_t srs = mk_srd(ws + WS_ST, SEQ * 128);
#pragma unroll
        for (int i = 0; i < 4; ++i) dma16c(lds0 + K4_STRAW + w * 4096 + i * 1024, lane * 16, srs, (unsigned)(rt * 256 + w * 32) * 128u + i * 1024);
        if (w == 7) {
            const srd_t src = mk_srd(ws + WS_CS, 4 * 7168 * 4);
            const unsigned cv = lane < 28 ? (unsigned)(ct * 112 + 4 * lane) * 4u : lane < 56 ? (unsigned)(3584 + ct * 112 + 4 * (lane - 28)) * 4u : 0x7ff00000u;
#pragma unroll
            for (int i = 0; i < 4; ++i) dma16c(lds0 + K4_CSRAW + i * 1024, cv, src, i * 7168 * 4);
        }
    }
    if ((ct & 1) == 0) {
        if (w < 4) k4_body<4, 0, true>(lds, D, ws, w, lane, rt, ct);
        else if (w < 7) k4_body<3, 4, true>(lds, D, ws, w, lane, rt, ct);
        else k4_body<3, 4, false>(lds, D, ws, w, lane, rt, ct);
    } else {
        if (w < 4) k4_body<3, 0, true>(lds, D, ws, w, lane, rt, ct);
        else if (w < 7) k4_body<4, 3, true>(lds, D, ws, w, lane, rt, ct);
        else k4_body<4, 3, false>(lds, D, ws, w, lane, rt, ct);
    }
    WAITBAR(0);
    if (SYNC && tid == 0) flag_set(ws, FL_4 + rt * 32 + ct, tag);
    if (w < 6) line_touch(lds, FRAG(rt * 16, (ct * 7 + 1) / 2 + (w >> 1), 112) + (w & 1) * 8192 + lane * 128, mk_srd(ws + WS_ACT, SEQ * FF * 2));
    CODE_TOUCH_ASYNC(".Lcw_p4", ".Lcw_p5");
}

constexpr int LDS_BYTES = 160 * 1024;
__global__ __launch_bounds__(512, 2) void mega(Params P) {
    extern __shared__ __attribute__((aligned(16))) char smem_g[];
    LAS char* lds = (LAS char*)smem_g;
    char* ws = P.ws;
    const unsigned tag = __hip_atomic_load((gu32*)(ws + WS_FLG) + FL_EPOCH, RLX_AGENT) + 1u;
    asm volatile(".Lcw_p0:");
    phase1<true>(lds, P, tag);
    asm volatile(".Lcw_p1:");
    phase2<true>(lds, P, tag);
    asm volatile(".Lcw_p2:");
    phase35<0, true>(lds, P, tag);
    asm volatile(".Lcw_p3:");
    phase4<true>(lds, P, tag);
    asm volatile(".Lcw_p4:");
    phase5<true>(lds, P, tag);
    asm volatile(".Lcw_p5:");
    if (blockIdx.x == 0 && threadIdx.x == 0) __hip_atomic_store((gu32*)(ws + WS_FLG) + FL_EPOCH, tag, RLX_AGENT);
}

extern "C" void kernel_launch(void* const* d_in, const int* in_sizes, int n_in, void* d_out, int out_size, void* d_ws, size_t ws_size, hipStream_t stream) {
    Params P;
    P.x = (const u16*)d_in[0]; P.wqkv = (const u16*)d_in[1]; P.wo = (const u16*)d_in[2];
    P.wg = (const u16*)d_in[4]; P.wu = (const u16*)d_in[5]; P.wd = (const u16*)d_in[6];
    P.out = (u16*)d_out;
    P.ws = (char*)d_ws;
    (void)hipFuncSetAttribute((const void*)mega, hipFuncAttributeMaxDynamicSharedMemorySize, LDS_BYTES);
    mega<<<256, 512, LDS_BYTES, stream>>>(P);
}
```

```cpp
#include <hip/hip_runtime.h>
#include <hip/hip_bf16.h>
#include <stdint.h>

#define LAS __attribute__((address_space(3)))
typedef unsigned short u16;
typedef short bf16x8 __attribute__((ext_vector_type(8)));
typedef short s16x4 __attribute__((ext_vector_type(4)));
typedef float f32x2 __attribute__((ext_vector_type(2)));
typedef float f32x4 __attribute__((ext_vector_type(4)));
typedef float f32x16 __attribute__((ext_vector_type(16)));
typedef unsigned u32x2 __attribute__((ext_vector_type(2)));
typedef unsigned u32x4 __attribute__((ext_vector_type(4)));
typedef __bf16 bf16x2_t __attribute__((ext_vector_type(2)));
typedef __amdgpu_buffer_rsrc_t srd_t;

constexpr int SEQ = 2048, DM = 1024, NH = 32, FF = 3584;
constexpr float LN_EPS = 1e-5f;
constexpr float QSCALE = 0.17677669529663687f * 1.4426950408889634f;
constexpr float LOG2E = 1.4426950408889634f;

constexpr size_t WS_Q = 0;
constexpr size_t WS_K = WS_Q + (4u << 20);
constexpr size_t WS_V = WS_K + (4u << 20);
constexpr size_t WS_O = WS_V + (4u << 20);
constexpr size_t WS_H = WS_O + (4u << 20);
constexpr size_t WS_ACT = WS_H + (4u << 20);
constexpr size_t WS_ST = WS_ACT + (size_t)SEQ * FF * 2;
constexpr size_t WS_CS = WS_ST + (size_t)SEQ * 16 * 8;
constexpr size_t WS_S1 = WS_CS + 4 * 7168 * 4;
constexpr size_t WS_C1 = WS_S1 + SEQ * 8;
constexpr size_t WS_FLG = WS_C1 + 16 * 3072 * 4;
constexpr size_t WS_XCH = WS_FLG + 16384;
constexpr size_t WS_END = WS_XCH + (8u << 20);
constexpr int FL_EPOCH = 0, FL_ERR = 32, FL_0 = 64, FL_1 = FL_0 + 256, FL_2 = FL_1 + 256, FL_3 = FL_2 + 256, FL_4 = FL_3 + 256, FL_5 = FL_4 + 256;

#define FRAG(mt, kt, KT) (((unsigned)(kt) * 128u + (unsigned)(mt)) * 1024u)
#define ST_AUX 16

__device__ __forceinline__ unsigned pk_bf16(float lo, float hi) { f32x2 v = {lo, hi}; bf16x2_t b = __builtin_convertvector(v, bf16x2_t); return __builtin_bit_cast(unsigned, b); }
__device__ __forceinline__ float bf_lo(unsigned u) { return __uint_as_float(u << 16); }
__device__ __forceinline__ float bf_hi(unsigned u) { return __uint_as_float(u & 0xffff0000u); }
__device__ __forceinline__ s16x4 trd(LAS const char* p) { return __builtin_bit_cast(s16x4, __builtin_amdgcn_ds_read_tr16_b64_v4i16((LAS s16x4*)p)); }
__device__ __forceinline__ bf16x8 cat8(s16x4 a, s16x4 b) { return (bf16x8){a[0], a[1], a[2], a[3], b[0], b[1], b[2], b[3]}; }
__device__ __forceinline__ bf16x8 ldf(LAS const char* p) { return *(LAS const bf16x8*)p; }
__device__ __forceinline__ srd_t mk_srd(const void* p, unsigned bytes) { return __builtin_amdgcn_make_buffer_rsrc((void*)p, (short)0, (int)bytes, 0x00020000); }
__device__ __forceinline__ void dma16(unsigned ldsa, unsigned voff, srd_t srd, unsigned soff) {
    asm volatile("s_mov_b32 m0, %0\n\ts_nop 0\n\tbuffer_load_dwordx4 %1, %2, %3 offen lds" :: "s"(ldsa), "v"(voff), "s"(srd), "s"(soff));
}
__device__ __forceinline__ void dma16c(unsigned ldsa, unsigned voff, srd_t srd, unsigned soff) {
    asm volatile("s_mov_b32 m0, %0\n\ts_nop 0\n\tbuffer_load_dwordx4 %1, %2, %3 offen sc1 lds" :: "s"(ldsa), "v"(voff), "s"(srd), "s"(soff));
}
__device__ __forceinline__ void st8(srd_t srd, unsigned off, u32x2 v) { __builtin_amdgcn_raw_buffer_store_b64(v, srd, off, 0, ST_AUX); }
__device__ __forceinline__ void st16(srd_t srd, unsigned off, u32x4 v) { __builtin_amdgcn_raw_buffer_store_b128(v, srd, off, 0, ST_AUX); }
__device__ __forceinline__ u32x4 pk8(const f32x4 a, const f32x4 b) { u32x4 o; o.x = pk_bf16(a[0], a[1]); o.y = pk_bf16(a[2], a[3]); o.z = pk_bf16(b[0], b[1]); o.w = pk_bf16(b[2], b[3]); return o; }
#define MFMA16(a, b, c) __builtin_amdgcn_mfma_f32_16x16x32_bf16(a, b, c, 0, 0, 0)
#define MFMA32(a, b, c) __builtin_amdgcn_mfma_f32_32x32x16_bf16(a, b, c, 0, 0, 0)
#define WAITVL(n) asm volatile("s_waitcnt vmcnt(%0) lgkmcnt(0)" :: "n"(n) : "memory")
#define WAITBAR(n) asm volatile("s_waitcnt vmcnt(%0) lgkmcnt(0)\n\ts_barrier" :: "n"(n) : "memory")
#define LBAR() asm volatile("s_waitcnt lgkmcnt(0)\n\ts_barrier" ::: "memory")
#define SCHED0() __builtin_amdgcn_sched_barrier(0)
#define CODE_TOUCH(A, B, t0, nthr) do { unsigned long long pc_; int lo_, hi_; unsigned dm_ = 0u; \
        asm volatile("s_getpc_b64 %0\n.Lct%=:\n\ts_mov_b32 %1, " A "-.Lct%=\n\ts_mov_b32 %2, " B "-.Lct%=" : "=&s"(pc_), "=&s"(lo_), "=&s"(hi_)); \
        for (int o_ = lo_ + ((int)threadIdx.x - (t0)) * 128; o_ < hi_; o_ += (nthr) * 128) asm volatile("global_load_dword %0, %1, off" : "+v"(dm_) : "v"((const char*)pc_ + o_)); \
        asm volatile("s_waitcnt vmcnt(0)" : "+v"(dm_) :: "memory");        \
        } while (0)
#define CODE_TOUCH_ASYNC(A, B) do { unsigned long long pc_; int lo_, hi_; \
        asm volatile("s_getpc_b64 %0\n.Lct%=:\n\ts_mov_b32 %1, " A "-.Lct%=\n\ts_mov_b32 %2, " B "-.Lct%=" : "=&s"(pc_), "=&s"(lo_), "=&s"(hi_)); \
        const srd_t sc_ = mk_srd((const char*)pc_ + lo_, (unsigned)(hi_ - lo_)); \
        for (int o_ = 0; o_ < hi_ - lo_; o_ += 512 * 128) \
            asm volatile("s_mov_b32 m0, %0\n\ts_nop 0\n\tbuffer_load_dword %1, %2, %3 offen lds" :: "s"((unsigned)(uintptr_t)lds + 163584u), "v"(threadIdx.x * 128u), "s"(sc_), "s"(o_)); \
        } while (0)
__device__ __forceinline__ void line_touch(LAS char* lds, unsigned voff, srd_t srd) {
    asm volatile("s_mov_b32 m0, %0\n\ts_nop 0\n\tbuffer_load_dword %1, %2, 0 offen sc1 lds" :: "s"((unsigned)(uintptr_t)lds + 163584u), "v"(voff), "s"(srd));
}
#define WAITV(n) asm volatile("s_waitcnt vmcnt(%0)" :: "n"(n) : "memory")
#define RLX_AGENT __ATOMIC_RELAXED, __HIP_MEMORY_SCOPE_AGENT
typedef __attribute__((address_space(1))) unsigned gu32;
typedef __attribute__((address_space(1))) unsigned long long gu64;
struct Params { const u16 *x, *wqkv, *wo, *wg, *wu, *wd; u16* out; char* ws; };
__device__ __forceinline__ void flag_set(char* ws, int idx, unsigned tag) { __hip_atomic_store((gu32*)(ws + WS_FLG) + idx, tag, RLX_AGENT); }
__device__ __forceinline__ void flags_wait(char* ws, int idx, unsigned tag) {
    gu32* fl = (gu32*)(ws + WS_FLG);
    const unsigned long long t0 = __builtin_amdgcn_s_memrealtime();
    for (unsigned spins = 1;; ++spins) {
        const unsigned v = idx >= 0 ? __hip_atomic_load(fl + idx, RLX_AGENT) : tag;
        if (__all(v == tag)) break;
        if ((spins & 63u) == 0u) {
            if (__any(__hip_atomic_load(fl + FL_ERR, RLX_AGENT) == tag)) break;
            if (__builtin_amdgcn_s_memrealtime() - t0 > 2000000ull) { __hip_atomic_store(fl + FL_ERR, tag, RLX_AGENT); break; }
        }
        __builtin_amdgcn_s_sleep(1);
    }
    asm volatile("" ::: "memory");
}
template <int CTRL, int RM> __device__ __forceinline__ float dpp_add(float v) { return v + __int_as_float(__builtin_amdgcn_update_dpp(0, __float_as_int(v), CTRL, RM, 0xf, true)); }
__device__ __forceinline__ float sum32(float v) { v = dpp_add<0x111, 0xf>(v); v = dpp_add<0x112, 0xf>(v); v = dpp_add<0x114, 0xf>(v); v = dpp_add<0x118, 0xf>(v); return dpp_add<0x142, 0xa>(v); }
__device__ __forceinline__ float sum_rows(float v) {
    const auto a = __builtin_amdgcn_permlane16_swap(__float_as_uint(v), __float_as_uint(v), false, false);
    const float t = __uint_as_float(a[0]) + __uint_as_float(a[1]);
    const auto c = __builtin_amdgcn_permlane32_swap(__float_as_uint(t), __float_as_uint(t), false, false);
    return __uint_as_float(c[0]) + __uint_as_float(c[1]);
}
__device__ __forceinline__ void wait_mask(char* ws, int idx, unsigned tag, unsigned long long need, unsigned long long& rdy) {
    if (__builtin_expect((rdy & need) == need, 1)) return;
    gu32* fl = (gu32*)(ws + WS_FLG);
    const unsigned long long t0 = __builtin_amdgcn_s_memrealtime();
    for (unsigned spins = 1;; ++spins) {
        const unsigned v = idx >= 0 ? __hip_atomic_load(fl + idx, RLX_AGENT) : ~tag;
        rdy = __ballot(v == tag);
        if ((rdy & need) == need) break;
        if ((spins & 63u) == 0u) {
            if (__any(__hip_atomic_load(fl + FL_ERR, RLX_AGENT) == tag)) break;
            if (__builtin_amdgcn_s_memrealtime() - t0 > 2000000ull) { __hip_atomic_store(fl + FL_ERR, tag, RLX_AGENT); break; }
        }
        __builtin_amdgcn_s_sleep(1);
    }
    asm volatile("" ::: "memory");
}
__device__ __forceinline__ u32x4 ldc16(srd_t srd, unsigned off) { return __builtin_amdgcn_raw_buffer_load_b128(srd, off, 0, 16); }
__device__ __forceinline__ float ldc4(srd_t srd, unsigned off) { return __uint_as_float(__builtin_amdgcn_raw_buffer_load_b32(srd, off, 0, 16)); }


constexpr int K1_SLOT = 40960, K1_NS = 4, K1_AUX = 0;
template <bool SYNC>
__device__ __forceinline__ void phase1(LAS char* lds, const Params& P, unsigned tag) {
    const unsigned lds0 = (unsigned)(uintptr_t)lds;
    const int tid = threadIdx.x, lane = tid & 63, w = __builtin_amdgcn_readfirstlane(tid >> 6), g = lane >> 4, r = lane & 15;
    const int b = blockIdx.x, xcd = b & 7, loc = b >> 3;
    const int p = xcd * 2 + (loc & 1), rt = loc >> 1;
    char* ws = P.ws;
    constexpr int NT = DM / 64;
    LAS f32x2* stat = (LAS f32x2*)(lds + K1_AUX);
    LAS float* csl = (LAS float*)(lds + K1_AUX + 1024);
    if (__builtin_expect(w >= 4, 1)) {
        const srd_t srx = mk_srd(P.x, SEQ * DM * 2), srw = mk_srd(P.wqkv, DM * 3 * DM * 2);
        const int j = w - 4, rr = lane >> 3, c8 = lane & 7;
        unsigned xv[2], wv[2];
#pragma unroll
        for (int i = 0; i < 2; ++i) {
            xv[i] = rr * 2048 + ((c8 ^ (4 * i + (rr >> 1))) * 16);
            const int f = ((rr >> 1) & 1) | (i << 1);
            wv[i] = rr * 6144 + (((c8 >> 1) ^ f) * 32) + (c8 & 1) * 16;
        }
        const unsigned xs0 = (unsigned)(rt * 128 + 32 * j) * 2048u, ws0 = (unsigned)p * 128u + (unsigned)(16 * j) * 6144u;
        const unsigned xl0 = lds0 + j * 4096, wl0 = lds0 + 16384 + j * 2048;
#define K1_DMA(i, t, slot) do { if ((i) < 4) dma16(xl0 + (slot) + (i) * 1024, xv[(i) & 1], srx, xs0 + (i) * (8 * 2048) + (((t) + rt) & 15) * 128); \
        else dma16(wl0 + (slot) + (((i) - 4) >> 1) * 8192 + (((i) - 4) & 1) * 1024, wv[((i) - 4) & 1], srw, ws0 + (((i) - 4) >> 1) * 2048 + (((i) - 4) & 1) * (8 * 6144) + (((t) + rt) & 15) * (64 * 6144)); } while (0)
#define K1_STAGE(t, slot) do { _Pragma("unroll") for (int i_ = 0; i_ < 10; ++i_) K1_DMA(i_, t, slot); } while (0)
        const int srow = 8 * b + 2 * j + (lane >> 5);
        u32x4 xr[4];
        { const unsigned xo = (unsigned)srow * 2048u + (lane & 31) * 16;
#pragma unroll
          for (int k = 0; k < 4; ++k) asm volatile("buffer_load_dwordx4 %0, %1, %2, 0 offen offset:%3" : "=v"(xr[k]) : "v"(xo), "s"(srx), "n"(k * 512)); }
        K1_STAGE(0, 0); K1_STAGE(1, K1_SLOT); K1_STAGE(2, 2 * K1_SLOT);
        WAITBAR(20);
        asm volatile("" : "+v"(xr[0]), "+v"(xr[1]), "+v"(xr[2]), "+v"(xr[3]));
        K1_STAGE(3, 3 * K1_SLOT);
        {
            const unsigned x00 = xr[0][0];
            const float pv = bf_lo(lane < 32 ? __builtin_amdgcn_readlane(x00, 0) : __builtin_amdgcn_readlane(x00, 32));
            float s = 0.f, q = 0.f;
#pragma unroll
            for (int k = 0; k < 4; ++k)
#pragma unroll
                for (int e = 0; e < 4; ++e) { const unsigned d = xr[k][e]; const float u = bf_lo(d) - pv, v = bf_hi(d) - pv; s += u + v; q += u * u + v * v; }
            s = sum32(s); q = sum32(q);
            const float dm = s * (1.0f / DM), var = fmaxf(q * (1.0f / DM) - dm * dm, 0.f);
            const u32x2 sv = {__float_as_uint(pv + dm), __float_as_uint(1.0f / sqrtf(var + LN_EPS))};
            __builtin_amdgcn_raw_buffer_store_b64(sv, mk_srd(ws + WS_S1, SEQ * 8), (lane & 31) == 31 ? (unsigned)srow * 8u : 0x7ff00000u, 0, 16);
        }
        WAITBAR(21); K1_STAGE(4, 0);
        WAITBAR(21); K1_STAGE(5, K1_SLOT);
        WAITBAR(21); K1_STAGE(6, 2 * K1_SLOT);
        WAITBAR(20);
        if (SYNC && tid == 256) flag_set(ws, FL_0 + b, tag);
        K1_STAGE(7, 3 * K1_SLOT);
        WAITBAR(20); K1_STAGE(8, 0);
        WAITBAR(20); K1_STAGE(9, K1_SLOT);
        WAITBAR(20); K1_STAGE(10, 2 * K1_SLOT);
        WAITBAR(20);
        const int fidx = lane < 16 ? FL_0 + 16 * rt + lane : lane < 32 ? FL_0 + ((lane - 16) * 2 + (p & 1)) * 8 + (p >> 1) : -1;
        unsigned fseen;
        asm volatile("global_load_dword %0, %1, off sc1" : "=v"(fseen) : "v"((const char*)(ws + WS_FLG) + (fidx >= 0 ? fidx : FL_0 + b) * 4));
        K1_STAGE(11, 3 * K1_SLOT);
        WAITBAR(21); K1_STAGE(12, 0);
        WAITBAR(21); K1_STAGE(13, K1_SLOT);
        WAITBAR(20);
        asm volatile("" : "+v"(fseen));
        K1_STAGE(14, 2 * K1_SLOT);
        if (SYNC && !__all(fseen == tag)) flags_wait(ws, fidx, tag);
        u32x4 gs, gp[16];
        const int tc = 48 * j + 4 * lane;
        { const srd_t srs = mk_srd(ws + WS_S1, SEQ * 8), src1 = mk_srd(ws + WS_C1, 16 * 3072 * 4);
          const unsigned so = j == 0 ? (unsigned)(rt * 128) * 8u + lane * 16 : 0x7ff00000u, po = lane < 12 ? (unsigned)((tc >> 6) * 1024 + p * 64 + (tc & 63)) * 4u : 0x7ff00000u;
          asm volatile("buffer_load_dwordx4 %0, %1, %2, 0 offen sc1" : "=v"(gs) : "v"(so), "s"(srs));
#pragma unroll
          for (int i = 0; i < 16; ++i) asm volatile("buffer_load_dwordx4 %0, %1, %2, %3 offen sc1" : "=v"(gp[i]) : "v"(po), "s"(src1), "s"(i * 3072 * 4)); }
        WAITBAR(37); K1_STAGE(15, 3 * K1_SLOT);
        WAITBAR(37);
        WAITBAR(10);
        asm volatile("" : "+v"(gs), "+v"(gp[0]), "+v"(gp[1]), "+v"(gp[2]), "+v"(gp[3]), "+v"(gp[4]), "+v"(gp[5]), "+v"(gp[6]), "+v"(gp[7]));
        asm volatile("" : "+v"(gp[8]), "+v"(gp[9]), "+v"(gp[10]), "+v"(gp[11]), "+v"(gp[12]), "+v"(gp[13]), "+v"(gp[14]), "+v"(gp[15]));
        {
            f32x4 v = {0.f, 0.f, 0.f, 0.f};
#pragma unroll
            for (int i = 0; i < 16; ++i) v += __builtin_bit_cast(f32x4, gp[i]);
            if (lane < 12) *(LAS f32x4*)(csl + tc) = v;
            if (j == 0) *(LAS u32x4*)(lds + K1_AUX + lane * 16) = gs;
        }
        WAITBAR(0);
    } else {
        const int wr = w >> 1, wc = w & 1;
        CODE_TOUCH(".Lcw_p0", ".Lcw_p2", 0, 256); CODE_TOUCH(".Lcw_p5", ".Lcw_end", 0, 256);
        int xrd[2], wrd[2];
        { const int q4 = (lane >> 2) & 3, p4 = lane & 3, trow = 8 * g + q4, f = ((trow >> 1) & 1) | (((trow >> 3) & 1) << 1);
#pragma unroll
          for (int s = 0; s < 2; ++s) { xrd[s] = (64 * wr + r) * 128 + (((4 * s + g) ^ ((r >> 1) & 7)) * 16); wrd[s] = 16384 + trow * 128 + (((2 * wc + s) ^ f) * 32) + (p4 >> 1) * 16 + (p4 & 1) * 8; } }
        f32x4 acc[4][6];
#pragma unroll
        for (int m = 0; m < 4; ++m)
#pragma unroll
            for (int n = 0; n < 6; ++n) acc[m][n] = (f32x4){0.f, 0.f, 0.f, 0.f};
        bf16x8 xa[4], xb[4]; s16x4 wa[12], wb[12];
#define K1_RW(i, W, sl, s) W[i] = trd(lds + (sl) + wrd[((i) >> 1) & 1] + ((i) >> 2) * 8192 + (s) * 4096 + ((i) & 1) * 512)
#define K1_RX(i, X, sl, s) X[i] = ldf(lds + (sl) + xrd[s] + (i) * 2048)
#define K1_RD(i, X, W, sl, s) do { if ((i) == 2) K1_RX(0, X, sl, s); else if ((i) == 7) K1_RX(1, X, sl, s); else if ((i) == 12) K1_RX(2, X, sl, s); else if ((i) == 15) K1_RX(3, X, sl, s); \
        else K1_RW((i) < 2 ? (i) : (i) < 7 ? (i) - 1 : (i) < 12 ? (i) - 2 : (i) - 3, W, sl, s); } while (0)
#define K1_MM(i, X, W) do { const int m_ = (i) / 6, n_ = (i) % 6; const bf16x8 wf_ = cat8(W[2 * n_], W[2 * n_ + 1]); \
        acc[m_][n_] = (n_ < 4) ? MFMA16(wf_, X[m_], acc[m_][n_]) : MFMA16(X[m_], wf_, acc[m_][n_]); } while (0)
#define K1_HALF(XM, WM, XR, WR, sl, s, DOMMA) do { \
        _Pragma("unroll") for (int i_ = 0; i_ < 24; ++i_) { \
            if (DOMMA) K1_MM(i_, XM, WM); \
            if (i_ < 16) K1_RD(i_, XR, WR, sl, s); \
            SCHED0(); } } while (0)
        LBAR();
        K1_HALF(xb, wb, xa, wa, 0, 0, false);
        K1_HALF(xa, wa, xb, wb, 0, 1, true);
        if (wr == 0) {
            bf16x8 one8;
#pragma unroll
            for (int e = 0; e < 8; ++e) one8[e] = (short)0x3f80;
            const srd_t src1 = mk_srd(ws + WS_C1, 16 * 3072 * 4);
#pragma unroll
            for (int n = 0; n < 6; ++n) {
                f32x4 c = MFMA16(cat8(wa[2 * n], wa[2 * n + 1]), one8, ((f32x4){0.f, 0.f, 0.f, 0.f}));
                c = MFMA16(cat8(wb[2 * n], wb[2 * n + 1]), one8, c);
                __builtin_amdgcn_raw_buffer_store_b128(__builtin_bit_cast(u32x4, c), src1, r == 0 ? (unsigned)(rt * 3072 + (n >> 1) * 1024 + p * 64 + wc * 32 + (n & 1) * 16 + 4 * g) * 4u : 0x7ff00000u, 0, 16);
            }
        }
        int cur = K1_SLOT;
        LBAR();
        K1_HALF(xb, wb, xa, wa, cur, 0, true);
        K1_HALF(xa, wa, xb, wb, cur, 1, true);
        cur = 2 * K1_SLOT;
        LBAR();
        K1_HALF(xb, wb, xa, wa, cur, 0, true);
        K1_HALF(xa, wa, xb, wb, cur, 1, true);
        cur = 3 * K1_SLOT;
        WAITBAR(0);
        K1_HALF(xb, wb, xa, wa, cur, 0, true);
        K1_HALF(xa, wa, xb, wb, cur, 1, true);
        cur = 0;
#pragma unroll 1
        for (int t = 4; t < NT; ++t) {
            LBAR();
            K1_HALF(xb, wb, xa, wa, cur, 0, true);
            K1_HALF(xa, wa, xb, wb, cur, 1, true);
            cur = (cur == 3 * K1_SLOT) ? 0 : cur + K1_SLOT;
        }
#pragma unroll
        for (int i = 0; i < 24; ++i) K1_MM(i, xb, wb);
        const int head = 2 * p + wc;
        const srd_t srq = mk_srd(ws + WS_Q, 12u << 20);
        f32x4 cq[4]; float cv[2];
#pragma unroll
        for (int n = 0; n < 4; ++n) cq[n] = *(LAS const f32x4*)(csl + (n >> 1) * 64 + wc * 32 + (n & 1) * 16 + 4 * g);
#pragma unroll
        for (int n = 0; n < 2; ++n) cv[n] = csl[128 + wc * 32 + n * 16 + r];
        const int rho = 16 * (r >> 3) + 4 * ((r >> 2) & 1) + (r & 3);
        f32x4 vv[2][2];
#pragma unroll
        for (int m = 0; m < 4; ++m) {
            const int lrow = wr * 64 + m * 16, row0 = rt * 128 + lrow;
            const unsigned blk = ((unsigned)head * 64 + (row0 >> 5)) * 2048u + (g >> 1) * 1024 + (g & 1) * 512;
            const f32x2 st = stat[lrow + r];
            const float qs = st.y * QSCALE;
            st16(srq, (4u << 20) + blk + (rho + (m & 1) * 8) * 16, pk8((acc[m][2] - st.x * cq[2]) * st.y, (acc[m][3] - st.x * cq[3]) * st.y));
            const f32x4 s01 = *(LAS const f32x4*)(stat + lrow + 4 * g), s23 = *(LAS const f32x4*)(stat + lrow + 4 * g + 2);
            const f32x4 mean4 = {s01[0], s01[2], s23[0], s23[2]}, rstd4 = {s01[1], s01[3], s23[1], s23[3]};
#pragma unroll
            for (int n = 0; n < 2; ++n) vv[m & 1][n] = (acc[m][4 + n] - mean4 * cv[n]) * rstd4;
            if (m & 1) {
#pragma unroll
                for (int n = 0; n < 2; ++n) st16(srq, (8u << 20) + blk + (16 * n + r) * 16, pk8(vv[0][n], vv[1][n]));
            }
            st16(srq, blk + ((m & 1) * 16 + r) * 16, pk8((acc[m][0] - st.x * cq[0]) * qs, (acc[m][1] - st.x * cq[1]) * qs));
        }
        asm volatile(".Lcw_end:");
    }
    WAITBAR(0);
    if (SYNC && tid == 0) flag_set(ws, FL_1 + p * 16 + rt, tag);
    if (w < 6) line_touch(lds, (unsigned)(w >> 1) * (4u << 20) + ((unsigned)(2 * p + (w & 1)) * 64 + rt * 4) * 2048u + lane * 128, mk_srd(ws + WS_Q, 12u << 20));
    CODE_TOUCH_ASYNC(".Lcw_p1", ".Lcw_p2");
}

constexpr int K2_SLOT = 32768, K2_SIDE = 3 * K2_SLOT;
struct K2Side { srd_t src, srp, sro; unsigned cv, cs0, ps0; };
template <int KH>
__device__ __forceinline__ void attn_fast(LAS char* lds, srd_t srk, srd_t srv, unsigned hbase, const K2Side& S, int w, int lane, const bf16x8 (&qf)[2][2], f32x16 (&o)[2], f32x4 (&lacc)[2], f32x4& cs,
                                          char* ws, int fidx, unsigned tag, unsigned long long rdy, int rot) {
    const int tl = w & 3;
    const unsigned lds0 = (unsigned)(uintptr_t)lds;
    const unsigned lv = lane * 16;
    const unsigned s0 = hbase + (unsigned)(KH * 32 + tl) * 2048u, d0 = lds0 + KH * 16384 + tl * 2048;
    const int rd0 = KH * 16384 + lane * 16;
    bf16x8 ones, sel;
    { const short v = ((lane & 15) == ((lane >> 4) & 1)) ? (short)0x3f80 : (short)0;
#pragma unroll
      for (int j = 0; j < 8; ++j) { ones[j] = v; sel[j] = ((lane & 15) == j) ? (short)0x3f80 : (short)0; } }
    const unsigned sd = lds0 + K2_SIDE + w * 1024;
#define K2_DMA(k, rd, fr) do { \
        if ((k) < 4) { const int rr_ = ((rd) + 2 + rot) & 7; if ((k) == 0) wait_mask(ws, fidx, tag, 1ull << (KH * 8 + rr_), rdy); \
            dma16c(d0 + (fr) + ((k) >> 1) * 8192 + ((k) & 1) * 1024, lv, (k) < 2 ? srk : srv, s0 + rr_ * 8192 + ((k) & 1) * 1024); } \
        else if ((k) == 4) dma16(sd + sc, S.cv, S.src, S.cs0 + (rd) * (4 * FF * 2)); \
        else dma16(sd + 24576, lv, (rd) < 4 ? S.srp : S.sro, (rd) < 4 ? S.ps0 + (rd) * 1024 : (rd) == 4 ? (S.ps0 >> 2) : 0x7ff00000u); } while (0)
#define K2_HOOK(gi, rd, fr) do { if (((gi) >> 1) < 6 && ((gi) & 1) == KH) K2_DMA((gi) >> 1, rd, fr); } while (0)
    f32x16 sa[2], sb[2];
    const f32x16 zero16 = {};
    bf16x8 k0 = ldf(lds + rd0), k1 = ldf(lds + rd0 + 1024), v0, v1;
#pragma unroll
    for (int qt = 0; qt < 2; ++qt) { sa[qt] = MFMA32(k0, qf[qt][0], zero16); sa[qt] = MFMA32(k1, qf[qt][1], sa[qt]); }
    k0 = ldf(lds + rd0 + 2048);
    u32x4 pa = {0u, 0u, 0u, 0u}, pb = {0u, 0u, 0u, 0u}; float e6 = 0.f, e7 = 0.f;
    v1 = k1;
    bf16x8 csd = {};
#define EX2(x) __builtin_amdgcn_exp2f(x)
#define K2_GROUP(S_, B, PC, PP, QK, VP, QP, RD, gi) do { \
        QK; PP[3] = pk_bf16(e6, e7); SCHED0(); \
        const float x0 = EX2(S_[B + 0]), x1 = EX2(S_[B + 1]), x2 = EX2(S_[B + 2]); RD; SCHED0(); \
        o[QP] = MFMA32(VP, __builtin_bit_cast(bf16x8, PP), o[QP]); SCHED0(); \
        const float x3 = EX2(S_[B + 3]), x4 = EX2(S_[B + 4]), x5 = EX2(S_[B + 5]); PC[0] = pk_bf16(x0, x1); SCHED0(); \
        lacc[QP] = MFMA16(ones, __builtin_bit_cast(bf16x8, PP), lacc[QP]); K2_HOOK(gi, rd, fr); SCHED0(); \
        e6 = EX2(S_[B + 6]); e7 = EX2(S_[B + 7]); PC[1] = pk_bf16(x2, x3); PC[2] = pk_bf16(x4, x5); SCHED0(); } while (0)
#define K2_TILE(SC, SN, kn1, kn2, va, ti) do { \
        K2_GROUP(SC[0], 0, pa, pb, SN[0] = MFMA32(k0, qf[0][0], zero16), v1, 1, v0 = ldf(va), 4 * (ti)); \
        K2_GROUP(SC[1], 0, pb, pa, SN[1] = MFMA32(k0, qf[1][0], zero16), v0, 0, k1 = ldf((kn1) + 1024), 4 * (ti) + 1); \
        K2_GROUP(SC[0], 8, pa, pb, SN[0] = MFMA32(k1, qf[0][1], SN[0]), v0, 1, v1 = ldf((va) + 1024), 4 * (ti) + 2); \
        K2_GROUP(SC[1], 8, pb, pa, SN[1] = MFMA32(k1, qf[1][1], SN[1]), v1, 0, k0 = ldf(kn2), 4 * (ti) + 3); } while (0)
    int cur = 0, fr = 2 * K2_SLOT, sc = 0;
#pragma unroll 1
    for (int rd = 0; rd < 8; ++rd) {
        const int nx = (cur == 2 * K2_SLOT) ? 0 : cur + K2_SLOT;
        const int sn = (sc == 16384) ? 0 : sc + 8192;
        if (rd > 0) WAITBAR(2);
        LAS const char* pc = lds + cur + rd0; LAS const char* pn = lds + nx + rd0;
        K2_TILE(sa, sb, pc + 2048, pc + 4096, pc + 8192, 0);
        csd = ldf(lds + K2_SIDE + sn + w * 1024 + lane * 16);
        if (rd < 2) csd = (bf16x8){};
        K2_TILE(sb, sa, pc + 4096, pc + 6144, pc + 8192 + 2048, 1);
        cs = MFMA16(sel, csd, cs);
        K2_TILE(sa, sb, pc + 6144, pn, pc + 8192 + 4096, 2);
        K2_TILE(sb, sa, pn, pn + 2048, pc + 8192 + 6144, 3);
        fr = cur; cur = nx; sc = sn;
    }
    pb[3] = pk_bf16(e6, e7);
    o[1] = MFMA32(v1, __builtin_bit_cast(bf16x8, pb), o[1]);
    lacc[1] = MFMA16(ones, __builtin_bit_cast(bf16x8, pb), lacc[1]);
    WAITBAR(0);
    cs = MFMA16(sel, ldf(lds + K2_SIDE + w * 1024 + lane * 16), cs);
    cs = MFMA16(sel, ldf(lds + K2_SIDE + 8192 + w * 1024 + lane * 16), cs);
#undef K2_GROUP
#undef K2_TILE
#undef K2_DMA
#undef K2_HOOK
}
#define K2_STAGE(rd, slot) do { dma16c(d0 + (slot), lv, srk, s0 + (rd) * 8192); dma16c(d0 + (slot) + 1024, lv, srk, s0 + (rd) * 8192 + 1024); \
        dma16c(d0 + (slot) + 8192, lv, srv, s0 + (rd) * 8192); dma16c(d0 + (slot) + 9216, lv, srv, s0 + (rd) * 8192 + 1024); } while (0)
__device__ __forceinline__ void attn_safe(LAS char* lds, srd_t srk, srd_t srv, unsigned hbase, const bf16x8 (&qf)[2][2], int w, int lane, f32x16 (&o)[2], float (&mrun)[2], float (&lrun)[2]) {
    const int kh = w >> 2, tl = w & 3;
    const unsigned lds0 = (unsigned)(uintptr_t)lds;
    const unsigned lv = lane * 16;
    const unsigned s0 = hbase + (unsigned)(kh * 32 + tl) * 2048u, d0 = lds0 + kh * 16384 + tl * 2048;
    const int rd0 = kh * 16384 + lane * 16;
    const f32x16 zero16 = {};
#pragma unroll 1
    for (int rd = 0; rd < 8; ++rd) {
        K2_STAGE(rd, 0);
        WAITBAR(0);
#pragma unroll 1
        for (int i = 0; i < 4; ++i) {
            LAS const char* pk = lds + rd0 + i * 2048;
            const bf16x8 k0 = ldf(pk), k1 = ldf(pk + 1024), v0 = ldf(pk + 8192), v1 = ldf(pk + 9216);
#pragma unroll
            for (int qt = 0; qt < 2; ++qt) {
                f32x16 sc = MFMA32(k0, qf[qt][0], zero16); sc = MFMA32(k1, qf[qt][1], sc);
                float mx = sc[0];
#pragma unroll
                for (int j = 1; j < 16; ++j) mx = fmaxf(mx, sc[j]);
                mx = fmaxf(mx, __shfl_xor(mx, 32));
                const float mn = fmaxf(mrun[qt], mx);
                const float ms = (mn == -INFINITY) ? 0.f : mn;
                const float al = __builtin_amdgcn_exp2f(mrun[qt] - ms);
                mrun[qt] = mn;
                u32x4 p0, p1;
#pragma unroll
                for (int j = 0; j < 4; ++j) { p0[j] = pk_bf16(__builtin_amdgcn_exp2f(sc[2 * j] - ms), __builtin_amdgcn_exp2f(sc[2 * j + 1] - ms));
                                              p1[j] = pk_bf16(__builtin_amdgcn_exp2f(sc[8 + 2 * j] - ms), __builtin_amdgcn_exp2f(sc[9 + 2 * j] - ms)); }
                float s_ = 0.f;
#pragma unroll
                for (int j = 0; j < 4; ++j) { const unsigned a = p0[j], c = p1[j]; s_ += bf_lo(a) + bf_hi(a); s_ += bf_lo(c) + bf_hi(c); }
                lrun[qt] = lrun[qt] * al + s_;
#pragma unroll
                for (int j = 0; j < 16; ++j) o[qt][j] *= al;
                o[qt] = MFMA32(v0, __builtin_bit_cast(bf16x8, p0), o[qt]); o[qt] = MFMA32(v1, __builtin_bit_cast(bf16x8, p1), o[qt]);
            }
        }
        WAITBAR(0);
    }
}

template <bool SYNC>
__device__ __forceinline__ void phase2(LAS char* lds, const Params& P, unsigned tag) {
    const int tid = threadIdx.x, lane = tid & 63, w = __builtin_amdgcn_readfirstlane(tid >> 6);
    const int b = blockIdx.x, xcd = b & 7, loc = b >> 3;
    const int head = xcd * 4 + 2 * (loc & 1) + ((loc >> 1) & 1), qb = loc >> 2;
    char* ws = P.ws;
    K2Side S;
    const int cgrp = b % 56, rq = b / 56;
    const bool do_cs = b < 224;
    S.src = mk_srd(cgrp < 28 ? P.wg : P.wu, DM * FF * 2);
    S.cv = (unsigned)(lane >> 4) * (FF * 2) + (lane & 15) * 16;
    S.cs0 = do_cs ? (unsigned)(rq * 256 + w * 32) * (FF * 2) + (unsigned)(cgrp < 28 ? cgrp : cgrp - 28) * 256u : 0x7ff00000u;
    S.srp = mk_srd(P.wd, FF * DM * 2); S.sro = mk_srd(P.wo, DM * DM * 2);
    S.ps0 = (unsigned)(b * 8 + w) * 4096u;
    const srd_t srk = mk_srd(ws + WS_K, SEQ * DM * 2), srv = mk_srd(ws + WS_V, SEQ * DM * 2);
    const unsigned hbase = (unsigned)head * 64u * 2048u;
    const int kh = w >> 2;
    const int fidx = lane < 16 ? FL_1 + (head >> 1) * 16 + lane : -1, rot = (loc >> 1) & 7;
    unsigned long long rdy = SYNC ? 0ull : ~0ull;
    bf16x8 qf[2][2];
    {
        const unsigned lds0 = (unsigned)(uintptr_t)lds, lv = lane * 16;
        const unsigned s0 = hbase + (unsigned)(kh * 32 + (w & 3)) * 2048u, d0 = lds0 + kh * 16384 + (w & 3) * 2048;
        wait_mask(ws, fidx, tag, (1ull << (kh * 8 + rot)) | (1ull << (2 * qb + ((w & 3) >> 1))), rdy);
        K2_STAGE(rot, 0);
        const srd_t srq = mk_srd(ws + WS_Q, SEQ * DM * 2);
        const unsigned qo = ((unsigned)head * 64 + qb * 8 + (w & 3) * 2) * 2048u + lane * 16;
#pragma unroll
        for (int qt = 0; qt < 2; ++qt)
#pragma unroll
            for (int s = 0; s < 2; ++s) qf[qt][s] = __builtin_bit_cast(bf16x8, ldc16(srq, qo + qt * 2048 + s * 1024));
        wait_mask(ws, fidx, tag, 1ull << (kh * 8 + ((rot + 1) & 7)), rdy);
        K2_STAGE((rot + 1) & 7, K2_SLOT);
    }
    WAITBAR(0);
    f32x16 o[2] = {}; f32x4 lacc[2] = {}; f32x4 cs = {0.f, 0.f, 0.f, 0.f};
    if (kh == 0) attn_fast<0>(lds, srk, srv, hbase, S, w, lane, qf, o, lacc, cs, ws, fidx, tag, rdy, rot);
    else attn_fast<1>(lds, srk, srv, hbase, S, w, lane, qf, o, lacc, cs, ws, fidx, tag, rdy, rot);
    LAS float* ob = (LAS float*)lds;
    LAS float* lb = (LAS float*)(lds + 32768);
    LAS unsigned* flag = (LAS unsigned*)(lds + 160000);
    LAS float* cb = (LAS float*)(lds + 40960);
    const int hi = lane >> 5, q32 = lane & 31;
    const srd_t sro = mk_srd(ws + WS_O, SEQ * DM * 2);
    const unsigned dst = FRAG(qb * 16 + (w & 3) * 4 + kh * 2 + (q32 >> 4), head, 32) + (hi * 16 + (q32 & 15)) * 16;
    bool bad;
    {
        if (tid == 0) flag[0] = 0u;
        if (lane < 16) { lb[w * 64 + lane] = lacc[0][0]; lb[w * 64 + 16 + lane] = lacc[0][1]; lb[w * 64 + 32 + lane] = lacc[1][0]; lb[w * 64 + 48 + lane] = lacc[1][1]; }
#pragma unroll
        for (int j = 0; j < 4; ++j) { const f32x4 v = kh ? (f32x4){o[0][4 * j], o[0][4 * j + 1], o[0][4 * j + 2], o[0][4 * j + 3]} : (f32x4){o[1][4 * j], o[1][4 * j + 1], o[1][4 * j + 2], o[1][4 * j + 3]};
            *(LAS f32x4*)(ob + ((w * 4 + j) * 64 + lane) * 4) = v; }
        if (lane < 32) *(LAS f32x4*)(cb + w * 128 + (lane & 15) * 8 + (lane >> 4) * 4) = cs;
        LBAR();
        const int pw = w ^ 4;
        f32x16 of;
#pragma unroll
        for (int j = 0; j < 4; ++j) { const f32x4 v = *(LAS const f32x4*)(ob + ((pw * 4 + j) * 64 + lane) * 4);
#pragma unroll
            for (int e = 0; e < 4; ++e) of[4 * j + e] = (kh ? o[1][4 * j + e] : o[0][4 * j + e]) + v[e]; }
        const float l = lb[w * 64 + kh * 32 + q32] + lb[pw * 64 + kh * 32 + q32];
        bool ok = (l >= 0x1p-80f) && (l <= 0x1p+110f);
#pragma unroll
        for (int j = 0; j < 16; ++j) ok = ok && (fabsf(of[j]) <= 0x1p+120f);
        if (!__all(ok)) { if (lane == 0) flag[0] = 1u; }
        else {
            const float rl = 1.0f / l;
#pragma unroll
            for (int c = 0; c < 2; ++c) {
                u32x4 ov; ov.x = pk_bf16(of[8 * c + 0] * rl, of[8 * c + 1] * rl); ov.y = pk_bf16(of[8 * c + 2] * rl, of[8 * c + 3] * rl);
                ov.z = pk_bf16(of[8 * c + 4] * rl, of[8 * c + 5] * rl); ov.w = pk_bf16(of[8 * c + 6] * rl, of[8 * c + 7] * rl);
                st16(sro, dst + c * 512, ov);
            }
        }
        if (do_cs && tid < 128) {
            float s = 0.f;
#pragma unroll
            for (int i = 0; i < 8; ++i) s += cb[i * 128 + tid];
            __hip_atomic_store((gu32*)(ws + WS_CS) + (rq * 7168 + cgrp * 128 + tid), __float_as_uint(s), RLX_AGENT);
        }
        WAITBAR(0);
        bad = flag[0] != 0u;
    }
    if (bad) {
        WAITBAR(0);
        o[0] = (f32x16){}; o[1] = (f32x16){}; float mrun[2] = {-INFINITY, -INFINITY}, lrun[2] = {0.f, 0.f};
        attn_safe(lds, srk, srv, hbase, qf, w, lane, o, mrun, lrun);
        LAS float* mb = lb + 512;
#pragma unroll
        for (int qt = 0; qt < 2; ++qt) { const float lt = lrun[qt] + __shfl_xor(lrun[qt], 32); if (lane < 32) { lb[w * 64 + qt * 32 + lane] = lt; mb[w * 64 + qt * 32 + lane] = mrun[qt]; } }
#pragma unroll
        for (int j = 0; j < 16; ++j) ob[(w * 16 + j) * 64 + lane] = kh ? o[0][j] : o[1][j];
        WAITBAR(0);
        const int pw = w ^ 4;
        const float ma = mb[w * 64 + kh * 32 + q32], mp = mb[pw * 64 + kh * 32 + q32];
        const float mn = fmaxf(ma, mp), ms = (mn == -INFINITY) ? 0.f : mn;
        const float fa = __builtin_amdgcn_exp2f(ma - ms), fp = __builtin_amdgcn_exp2f(mp - ms);
        const float l = lb[w * 64 + kh * 32 + q32] * fa + lb[pw * 64 + kh * 32 + q32] * fp;
        const float rl = 1.0f / l;
        float of[16];
#pragma unroll
        for (int j = 0; j < 16; ++j) of[j] = ((kh ? o[1][j] : o[0][j]) * fa + ob[(pw * 16 + j) * 64 + lane] * fp) * rl;
#pragma unroll
        for (int c = 0; c < 2; ++c) {
            u32x4 ov; ov.x = pk_bf16(of[8 * c + 0], of[8 * c + 1]); ov.y = pk_bf16(of[8 * c + 2], of[8 * c + 3]);
            ov.z = pk_bf16(of[8 * c + 4], of[8 * c + 5]); ov.w = pk_bf16(of[8 * c + 6], of[8 * c + 7]);
            st16(sro, dst + c * 512, ov);
        }
        WAITBAR(0);
    }
    if (SYNC && tid == 0) flag_set(ws, FL_2 + qb * 32 + head, tag);
    CODE_TOUCH_ASYNC(".Lcw_p2", ".Lcw_p3");
}
#undef K2_STAGE

constexpr int K3_SLOT = 24576, K3_NS = 6, K3_AUX = K3_NS * K3_SLOT;
template <int MODE, bool SYNC>
__device__ __forceinline__ void phase35(LAS char* lds, const Params& P, unsigned tag) {
    constexpr int KD = MODE == 0 ? DM : FF, KT = KD / 32, NT = KD / 64;
    const unsigned lds0 = (unsigned)(uintptr_t)lds;
    const int tid = threadIdx.x, lane = tid & 63, w = __builtin_amdgcn_readfirstlane(tid >> 6), g = lane >> 4, r = lane & 15;
    const int b = blockIdx.x, xcd = b & 7, loc = b >> 3;
    const int rt = (xcd >> 1) * 4 + (loc & 3), ct = (xcd & 1) * 8 + (loc >> 2);
    char* ws = P.ws;
    if (w >= 4) {
        const int j = w - 4;
        const srd_t srx = mk_srd(ws + (MODE == 0 ? WS_O : WS_ACT), SEQ * KD * 2), srw = mk_srd(MODE == 0 ? P.wo : P.wd, KD * DM * 2);
        const unsigned xv = lane * 16, xs0 = FRAG(rt * 8 + 2 * j, 0, KT);
        constexpr unsigned XM = FRAG(1, 0, KT), XK = FRAG(0, 1, KT);
        const int wrow = lane >> 3, wch = (lane & 7) ^ (((wrow >> 1) & 3) << 1);
        const unsigned wv = (unsigned)wrow * 2048u + wch * 16, ws0 = (unsigned)ct * 128u + (unsigned)(16 * j) * 2048u;
        const unsigned ld0 = lds0 + j * 4096, ld1 = lds0 + 16384 + j * 2048;
        const int rot = 2 * xcd, fidx = lane < 32 ? FL_2 + (rt >> 1) * 32 + lane : -1;
        unsigned long long rdy = SYNC ? 0ull : ~0ull;
#define K3_T(t) (((t) + rot) & 15)
#define K3_W(t, slot) do { dma16(ld1 + (slot), wv, srw, ws0 + K3_T(t) * (64 * 2048)); dma16(ld1 + (slot) + 1024, wv, srw, ws0 + 8 * 2048 + K3_T(t) * (64 * 2048)); } while (0)
#define K3_X(t, slot) do { wait_mask(ws, fidx, tag, 3ull << (2 * K3_T(t)), rdy); \
        dma16c(ld0 + (slot), xv, srx, xs0 + K3_T(t) * (2 * XK)); dma16c(ld0 + (slot) + 1024, xv, srx, xs0 + K3_T(t) * (2 * XK) + XK); \
        dma16c(ld0 + (slot) + 2048, xv, srx, xs0 + XM + K3_T(t) * (2 * XK)); dma16c(ld0 + (slot) + 3072, xv, srx, xs0 + XM + K3_T(t) * (2 * XK) + XK); } while (0)
        K3_W(0, 0); K3_W(1, K3_SLOT); K3_W(2, 2 * K3_SLOT); K3_W(3, 3 * K3_SLOT); K3_W(4, 4 * K3_SLOT);
        K3_X(0, 0); K3_X(1, K3_SLOT); K3_X(2, 2 * K3_SLOT);
        WAITBAR(8);  K3_X(3, 3 * K3_SLOT); K3_X(4, 4 * K3_SLOT); K3_X(5, 5 * K3_SLOT); K3_W(5, 5 * K3_SLOT);
        WAITBAR(18); K3_X(6, 0); K3_W(6, 0);
        WAITBAR(20); K3_X(7, K3_SLOT); K3_W(7, K3_SLOT);
        WAITBAR(22); K3_X(8, 2 * K3_SLOT); K3_W(8, 2 * K3_SLOT);
        int fr = 3 * K3_SLOT;
#pragma unroll 1
        for (int t = 4; t + K3_NS - 1 < NT; ++t) {
            WAITBAR(6 * (K3_NS - 2));
            K3_X(t + K3_NS - 1, fr); K3_W(t + K3_NS - 1, fr);
            fr = (fr == (K3_NS - 1) * K3_SLOT) ? 0 : fr + K3_SLOT;
        }
        WAITBAR(24); WAITBAR(18); WAITBAR(12); WAITBAR(6); WAITBAR(0);
        if (MODE == 0) LBAR();
    } else {
        const int wm = w >> 1, wn = w & 1;
        const int col0 = ct * 64 + wn * 32 + 4 * g;
        u32x4 res[4];
#pragma unroll
        for (int m = 0; m < 4; ++m) {
            const int mrow = rt * 128 + wm * 64 + m * 16;
            if (MODE == 0) { const u32x2 a = *(const u32x2*)(P.x + (size_t)(mrow + r) * DM + col0), c = *(const u32x2*)(P.x + (size_t)(mrow + r) * DM + col0 + 16); res[m] = (u32x4){a.x, a.y, c.x, c.y}; }
            else res[m] = ldc16(mk_srd(ws + WS_H, SEQ * DM * 2), FRAG(mrow >> 4, ct * 2 + wn, 32) + lane * 16);
        }
        const int xrd = wm * 8192 + lane * 16;
        const int q4 = (lane >> 2) & 3, p4 = lane & 3;
        const int trow = (MODE == 0) ? (4 * (g & 1) + 16 * (g >> 1) + q4) : (4 * g + q4);
        constexpr int TR2 = (MODE == 0) ? 8 * 128 : 16 * 128;
        int wrd[2];
#pragma unroll
        for (int n = 0; n < 2; ++n) wrd[n] = 16384 + trow * 128 + (((wn * 2 + n) ^ ((trow >> 1) & 3)) * 32) + (p4 >> 1) * 16 + (p4 & 1) * 8;
        f32x4 acc[4][2];
#pragma unroll
        for (int m = 0; m < 4; ++m) { acc[m][0] = (f32x4){0.f, 0.f, 0.f, 0.f}; acc[m][1] = (f32x4){0.f, 0.f, 0.f, 0.f}; }
        bf16x8 xa[4], xb[4]; s16x4 wa[4], wb[4];
#define K3_RD(i, X, W, sl, s) do { if ((i) == 2) X[0] = ldf(lds + (sl) + xrd + (s) * 1024); else if ((i) >= 5) X[(i) - 4] = ldf(lds + (sl) + xrd + ((i) - 4) * 2048 + (s) * 1024); \
        else { const int k_ = (i) < 2 ? (i) : (i) - 1; W[k_] = trd(lds + (sl) + wrd[k_ >> 1] + (k_ & 1) * TR2 + (s) * 4096); } } while (0)
#define K3_MM(i, X, W) do { const int m_ = (i) >> 1, n_ = (i) & 1; acc[m_][n_] = MFMA16(cat8(W[2 * n_], W[2 * n_ + 1]), X[m_], acc[m_][n_]); } while (0)
#define K3_HALF(XM, WM, XR, WR, sl, s, DOMMA) do { _Pragma("unroll") for (int i_ = 0; i_ < 8; ++i_) { if (DOMMA) K3_MM(i_, XM, WM); K3_RD(i_, XR, WR, sl, s); SCHED0(); } } while (0)
        LBAR();
        K3_HALF(xb, wb, xa, wa, 0, 0, false);
        K3_HALF(xa, wa, xb, wb, 0, 1, true);
        int cur = K3_SLOT;
#pragma unroll 1
        for (int t = 1; t < NT; ++t) {
            LBAR();
            K3_HALF(xb, wb, xa, wa, cur, 0, true);
            K3_HALF(xa, wa, xb, wb, cur, 1, true);
            cur = (cur == (K3_NS - 1) * K3_SLOT) ? 0 : cur + K3_SLOT;
        }
#pragma unroll
        for (int i = 0; i < 8; ++i) K3_MM(i, xb, wb);
        if (MODE == 0) {
            LAS f32x2* sb = (LAS f32x2*)(lds + K3_AUX);
            const srd_t srh = mk_srd(ws + WS_H, SEQ * DM * 2);
#pragma unroll
            for (int m = 0; m < 4; ++m) {
                const int mrow = rt * 128 + wm * 64 + m * 16;
                const f32x4 a = {bf_lo(res[m].x) + acc[m][0][0], bf_hi(res[m].x) + acc[m][0][1], bf_lo(res[m].y) + acc[m][0][2], bf_hi(res[m].y) + acc[m][0][3]};
                const f32x4 c = {bf_lo(res[m].z) + acc[m][1][0], bf_hi(res[m].z) + acc[m][1][1], bf_lo(res[m].w) + acc[m][1][2], bf_hi(res[m].w) + acc[m][1][3]};
                const u32x4 hv = pk8(a, c);
                st16(srh, FRAG(mrow >> 4, ct * 2 + wn, 32) + lane * 16, hv);
                float s = 0.f, q = 0.f;
#pragma unroll
                for (int j = 0; j < 4; ++j) { const unsigned d = hv[j]; const float u = bf_lo(d), v = bf_hi(d); s += u + v; q += u * u + v * v; }
                s = sum_rows(s); q = sum_rows(q);
                if (g == 0) sb[(wm * 64 + m * 16 + r) * 2 + wn] = (f32x2){s, q};
            }
            LBAR();
            if (tid < 128) { const f32x2 a = sb[tid * 2] + sb[tid * 2 + 1];
                __hip_atomic_store((gu64*)(ws + WS_ST) + ((size_t)(rt * 128 + tid) * 16 + ct), ((unsigned long long)__float_as_uint(a[1]) << 32) | __float_as_uint(a[0]), RLX_AGENT); }
        } else {
#pragma unroll
            for (int m = 0; m < 4; ++m) {
                const int mrow = rt * 128 + wm * 64 + m * 16;
                unsigned a0 = pk_bf16(bf_lo(res[m].x) + acc[m][0][0], bf_hi(res[m].x) + acc[m][0][1]), a1 = pk_bf16(bf_lo(res[m].y) + acc[m][0][2], bf_hi(res[m].y) + acc[m][0][3]);
                unsigned b0 = pk_bf16(bf_lo(res[m].z) + acc[m][1][0], bf_hi(res[m].z) + acc[m][1][1]), b1 = pk_bf16(bf_lo(res[m].w) + acc[m][1][2], bf_hi(res[m].w) + acc[m][1][3]);
                const auto s0 = __builtin_amdgcn_permlane16_swap(b0, a0, false, false), s1 = __builtin_amdgcn_permlane16_swap(b1, a1, false, false);
                const u32x4 ov = {s0[0], s1[0], s0[1], s1[1]};
                const int c8 = ((g & 1) ? 0 : 16) + (g >> 1) * 8;
                *(u32x4*)(P.out + (size_t)(mrow + r) * DM + ct * 64 + wn * 32 + c8) = ov;
            }
        }
    }
    if (MODE == 0) {
        WAITBAR(0);
        if (SYNC && tid == 0) flag_set(ws, FL_3 + rt * 16 + ct, tag);
        CODE_TOUCH_ASYNC(".Lcw_p3", ".Lcw_p4");
    }
}

constexpr int K5_SLOT = 32768, K5_NS = 4;
template <int KH>
__device__ __forceinline__ void p5_finish(const Params& P, const f32x4 (&acc)[4][4], unsigned tag, int b, int w, int lane, int rt, int ct) {
    char* ws = P.ws;
    const int g = lane >> 4, r = lane & 15, wm = w >> 1, wn = w & 1;
    const srd_t sre = mk_srd(ws + WS_XCH, 8u << 20);
    const unsigned eo = (unsigned)(w * 8192) + lane * 16;
#pragma unroll
    for (int m = 0; m < 4; ++m)
#pragma unroll
        for (int n = 0; n < 2; ++n) st16(sre, (unsigned)b * 32768u + eo + (m * 2 + n) * 1024, __builtin_bit_cast(u32x4, acc[m][2 * (1 - KH) + n]));
    u32x4 res[4];
#pragma unroll
    for (int m = 0; m < 4; ++m) res[m] = ldc16(mk_srd(ws + WS_H, SEQ * DM * 2), FRAG(rt * 8 + wm * 4 + m, ct * 4 + wn * 2 + KH, 32) + lane * 16);
    WAITV(0);
    if (lane == 0) flag_set(ws, FL_5 + b * 4 + w, tag);
    flags_wait(ws, lane == 0 ? FL_5 + (b ^ 2) * 4 + w : -1, tag);
    u32x4 pv[4][2];
#pragma unroll
    for (int m = 0; m < 4; ++m)
#pragma unroll
        for (int n = 0; n < 2; ++n) pv[m][n] = ldc16(sre, (unsigned)(b ^ 2) * 32768u + eo + (m * 2 + n) * 1024);
#pragma unroll
    for (int m = 0; m < 4; ++m) {
        const int mrow = rt * 128 + wm * 64 + m * 16;
        const f32x4 t0 = acc[m][2 * KH] + __builtin_bit_cast(f32x4, pv[m][0]), t1 = acc[m][2 * KH + 1] + __builtin_bit_cast(f32x4, pv[m][1]);
        unsigned a0 = pk_bf16(bf_lo(res[m].x) + t0[0], bf_hi(res[m].x) + t0[1]), a1 = pk_bf16(bf_lo(res[m].y) + t0[2], bf_hi(res[m].y) + t0[3]);
        unsigned b0 = pk_bf16(bf_lo(res[m].z) + t1[0], bf_hi(res[m].z) + t1[1]), b1 = pk_bf16(bf_lo(res[m].w) + t1[2], bf_hi(res[m].w) + t1[3]);
        const auto s0 = __builtin_amdgcn_permlane16_swap(b0, a0, false, false), s1 = __builtin_amdgcn_permlane16_swap(b1, a1, false, false);
        const u32x4 ov = {s0[0], s1[0], s0[1], s1[1]};
        const int c8 = ((g & 1) ? 0 : 16) + (g >> 1) * 8;
        *(u32x4*)(P.out + (size_t)(mrow + r) * DM + ct * 128 + wn * 64 + KH * 32 + c8) = ov;
    }
}
template <bool SYNC>
__device__ __forceinline__ void phase5(LAS char* lds, const Params& P, unsigned tag) {
    constexpr int KT = FF / 32, NT = FF / 128;
    const unsigned lds0 = (unsigned)(uintptr_t)lds;
    const int tid = threadIdx.x, lane = tid & 63, w = __builtin_amdgcn_readfirstlane(tid >> 6), g = lane >> 4, r = lane & 15;
    const int b = blockIdx.x, xcd = b & 7, loc = b >> 3;
    const int kh = (xcd >> 1) & 1, rt = (xcd >> 2) * 8 + (xcd & 1) * 4 + (loc & 3), ct = loc >> 2;
    char* ws = P.ws;
    if (w >= 4) {
        const int j = w - 4;
        const srd_t srx = mk_srd(ws + WS_ACT, SEQ * FF * 2), srw = mk_srd(P.wd, FF * DM * 2);
        constexpr unsigned XM = FRAG(1, 0, KT), XK = FRAG(0, 1, KT);
        const unsigned xv = lane * 16, xs0 = FRAG(rt * 8 + 2 * j, kh * (KT / 2), KT);
        const int wrow = lane >> 3, wch = (lane & 7) ^ (((wrow >> 1) & 3) << 1);
        const unsigned wv = (unsigned)wrow * 2048u + wch * 16, ws0 = (unsigned)ct * 256u + (unsigned)(kh * (FF / 2) + 16 * j) * 2048u;
        const unsigned ld0 = lds0 + j * 4096, ld1 = lds0 + 16384 + j * 2048;
        const int rot = 14 * (xcd & 1), fidx = lane < 16 ? FL_4 + (rt >> 1) * 32 + kh * 16 + lane : -1;
        unsigned long long rdy = SYNC ? 0ull : ~0ull;
#define K5_T(t) ((t) + rot >= NT ? (t) + rot - NT : (t) + rot)
#define K5_W(t, slot) do { const unsigned o_ = ws0 + K5_T(t) * (64 * 2048); dma16(ld1 + (slot), wv, srw, o_); dma16(ld1 + (slot) + 1024, wv, srw, o_ + 8 * 2048); \
        dma16(ld1 + (slot) + 8192, wv, srw, o_ + 128); dma16(ld1 + (slot) + 9216, wv, srw, o_ + 128 + 8 * 2048); } while (0)
#define K5_X(t, slot) do { const int tt_ = K5_T(t); wait_mask(ws, fidx, tag, (1ull << ((4 * tt_) / 7)) | (1ull << ((4 * tt_ + 3) / 7)), rdy); const unsigned o_ = xs0 + tt_ * (2 * XK); \
        dma16c(ld0 + (slot), xv, srx, o_); dma16c(ld0 + (slot) + 1024, xv, srx, o_ + XK); dma16c(ld0 + (slot) + 2048, xv, srx, o_ + XM); dma16c(ld0 + (slot) + 3072, xv, srx, o_ + XM + XK); } while (0)
        K5_W(0, 0); K5_W(1, K5_SLOT); K5_W(2, 2 * K5_SLOT); K5_W(3, 3 * K5_SLOT);
        K5_X(0, 0); K5_X(1, K5_SLOT); K5_X(2, 2 * K5_SLOT); K5_X(3, 3 * K5_SLOT);
        WAITBAR(12);
        WAITBAR(8);  K5_X(4, 0); K5_W(4, 0);
        WAITBAR(12); K5_X(5, K5_SLOT); K5_W(5, K5_SLOT);
        WAITBAR(16); K5_X(6, 2 * K5_SLOT); K5_W(6, 2 * K5_SLOT);
        int fr = 3 * K5_SLOT;
#pragma unroll 1
        for (int t = 4; t + 3 < NT; ++t) {
            WAITBAR(16);
            K5_X(t + 3, fr); K5_W(t + 3, fr);
            fr = (fr == 3 * K5_SLOT) ? 0 : fr + K5_SLOT;
        }
        WAITBAR(16); WAITBAR(8); WAITBAR(0);
    } else {
        const int wm = w >> 1, wn = w & 1;
        const int xrd = wm * 8192 + lane * 16;
        const int q4 = (lane >> 2) & 3, p4 = lane & 3, trow = 4 * g + q4;
        int wrd[4];
#pragma unroll
        for (int n = 0; n < 4; ++n) wrd[n] = 16384 + wn * 8192 + trow * 128 + ((n ^ ((trow >> 1) & 3)) * 32) + (p4 >> 1) * 16 + (p4 & 1) * 8;
        f32x4 acc[4][4];
#pragma unroll
        for (int m = 0; m < 4; ++m)
#pragma unroll
            for (int n = 0; n < 4; ++n) acc[m][n] = (f32x4){0.f, 0.f, 0.f, 0.f};
        bf16x8 xa[4], xb[4]; s16x4 wa[8], wb[8];
#define K5_RD(i, X, W, sl, s) do { if ((i) == 2) X[0] = ldf(lds + (sl) + xrd + (s) * 1024); else if ((i) >= 9) X[(i) - 8] = ldf(lds + (sl) + xrd + ((i) - 8) * 2048 + (s) * 1024); \
        else { const int k_ = (i) < 2 ? (i) : (i) - 1; W[k_] = trd(lds + (sl) + wrd[k_ >> 1] + (k_ & 1) * 2048 + (s) * 4096); } } while (0)
#define K5_MM(i, X, W) do { const int m_ = (i) >> 2, n_ = (i) & 3; acc[m_][n_] = MFMA16(cat8(W[2 * n_], W[2 * n_ + 1]), X[m_], acc[m_][n_]); } while (0)
#define K5_HALF(XM_, WM_, XR, WR, sl, s, DOMMA) do { _Pragma("unroll") for (int i_ = 0; i_ < 16; ++i_) { if (DOMMA) K5_MM(i_, XM_, WM_); if (i_ < 12) K5_RD(i_, XR, WR, sl, s); SCHED0(); } } while (0)
        LBAR();
        K5_HALF(xb, wb, xa, wa, 0, 0, false);
        K5_HALF(xa, wa, xb, wb, 0, 1, true);
        int cur = K5_SLOT;
#pragma unroll 1
        for (int t = 1; t < NT; ++t) {
            LBAR();
            K5_HALF(xb, wb, xa, wa, cur, 0, true);
            K5_HALF(xa, wa, xb, wb, cur, 1, true);
            cur = (cur == 3 * K5_SLOT) ? 0 : cur + K5_SLOT;
        }
#pragma unroll
        for (int i = 0; i < 16; ++i) K5_MM(i, xb, wb);
        if (kh) p5_finish<1>(P, acc, tag, b, w, lane, rt, ct); else p5_finish<0>(P, acc, tag, b, w, lane, rt, ct);
    }
}

constexpr int K4_SLOT = 30720, K4_NS = 4, K4_STRAW = K4_NS * K4_SLOT, K4_STAT = K4_STRAW + 32768, K4_CS = K4_STAT + 2048, K4_CSRAW = K4_CS + 1024;
struct K4Dma { srd_t srx, srg, sru; unsigned xv, gv, xs0, gs0, ld0, ld1; };
#define K4_DMA(i, t, slot) do { if ((i) < 2) dma16c(D.ld0 + (slot) + (i) * 1024, D.xv, D.srx, D.xs0 + (t) * FRAG(0, 1, 32) + (i) * FRAG(1, 0, 32)); \
        else dma16(D.ld1 + (slot) + ((i) - 2) * 7168, D.gv, (i) == 2 ? D.srg : D.sru, D.gs0 + (t) * (32 * FF * 2)); } while (0)
template <int NJ, int J0, bool HASW>
__device__ __forceinline__ void k4_body(LAS char* lds, const K4Dma& D, char* ws, int w, int lane, int rt, int ct) {
    const int wm = w & 3, g = lane >> 4, r = lane & 15;
    constexpr int NP = HASW ? 4 : 2;
    const int xrd = wm * 4096 + lane * 16;
    const int q4 = (lane >> 2) & 3, p4 = lane & 3;
    const int wrd = 16384 + (4 * g + q4) * 224 + J0 * 32 + (p4 >> 1) * 16 + (p4 & 1) * 8;
    f32x4 ag[4][NJ], au[4][NJ];
#pragma unroll
    for (int m = 0; m < 4; ++m)
#pragma unroll
        for (int j = 0; j < NJ; ++j) { ag[m][j] = (f32x4){0.f, 0.f, 0.f, 0.f}; au[m][j] = (f32x4){0.f, 0.f, 0.f, 0.f}; }
    bf16x8 xa[4], xb[4]; s16x4 wgf[2 * NJ], wuf[2 * NJ];
#define K4_RW(i, W, sl, o) W[i] = trd(lds + (sl) + wrd + (o) + ((i) >> 1) * 32 + ((i) & 1) * (16 * 224))
#define K4_RX(i, X, sl) X[i] = ldf(lds + (sl) + xrd + (i) * 1024)
#define K4_MM(i, A, X, W) do { const int j_ = (i) >> 2, m_ = (i) & 3; A[m_][j_] = MFMA16(cat8(W[2 * j_], W[2 * j_ + 1]), X[m_], A[m_][j_]); } while (0)
#define K4_RD1(i, XC, sl) do { if ((i) == 0) K4_RX(0, XC, sl); else if ((i) < 3) K4_RW((i) - 1, wgf, sl, 0); else if ((i) < 6) K4_RX((i) - 2, XC, sl); else if ((i) < 4 + 2 * NJ) K4_RW((i) - 4, wgf, sl, 0); } while (0)
#define K4_H1(XC, XP, DOMMA, DODMA, t, sl, fsl) do { \
        _Pragma("unroll") for (int i_ = 0; i_ < 4 * NJ; ++i_) { \
            if (DOMMA) K4_MM(i_, au, XP, wuf); \
            K4_RD1(i_, XC, sl); \
            if (DODMA) { if (i_ == (NJ == 4 ? 13 : 11)) K4_DMA(0, t, fsl); } \
            SCHED0(); } } while (0)
#define K4_H2(XC, sl, DODMA, t, fsl) do { \
        _Pragma("unroll") for (int i_ = 0; i_ < 4 * NJ; ++i_) { \
            K4_MM(i_, ag, XC, wgf); \
            if (i_ < 2 * NJ) K4_RW(i_, wuf, sl, 7168); \
            if (DODMA) { if (NJ == 4) { if (i_ == 8) K4_DMA(1, t, fsl); if (i_ == 11 && NP == 4) K4_DMA(2, t, fsl); if (i_ == 14 && NP == 4) K4_DMA(3, t, fsl); } \
                         else { if (i_ == 6) K4_DMA(1, t, fsl); if (i_ == 8 && NP == 4) K4_DMA(2, t, fsl); if (i_ == 10 && NP == 4) K4_DMA(3, t, fsl); } } \
            SCHED0(); } } while (0)
    constexpr int NT = DM / 32;
    constexpr int NX = HASW ? 4 : 8;
    WAITBAR(4 + NX);
    K4_H1(xa, xb, false, true, 3, 0, 3 * K4_SLOT);
    K4_H2(xa, 0, true, 3, 3 * K4_SLOT);
    int cur = K4_SLOT, fr = 0;
#define K4_ADV() do { fr = (fr == (K4_NS - 1) * K4_SLOT) ? 0 : fr + K4_SLOT; cur = (cur == (K4_NS - 1) * K4_SLOT) ? 0 : cur + K4_SLOT; } while (0)
    WAITBAR(2 + NX + NP); K4_H1(xb, xa, true, true, 4, cur, fr); K4_H2(xb, cur, true, 4, fr); K4_ADV();
    WAITBAR(NX + 2 * NP); K4_H1(xa, xb, true, true, 5, cur, fr); K4_H2(xa, cur, true, 5, fr); K4_ADV();
    WAITBAR(2 * NP);
    {
        const int tid = w * 64 + lane, row = tid >> 1, hf = tid & 1;
        float s = 0.f, q = 0.f;
#pragma unroll
        for (int i = 0; i < 4; ++i) { const f32x4 v = *(const LAS f32x4*)(lds + K4_STRAW + row * 128 + hf * 64 + i * 16); s += v[0] + v[2]; q += v[1] + v[3]; }
        s += __shfl_xor(s, 1); q += __shfl_xor(q, 1);
        const float mean = s * (1.0f / DM);
        const float var = fmaxf(q * (1.0f / DM) - mean * mean, 0.f);
        if (hf == 0) ((LAS f32x2*)(lds + K4_STAT))[row] = (f32x2){mean, 1.0f / sqrtf(var + LN_EPS)};
        if (tid < 224) { const LAS float* cr = (const LAS float*)(lds + K4_CSRAW) + tid; ((LAS float*)(lds + K4_CS))[tid] = (cr[0] + cr[256]) + (cr[512] + cr[768]); }
        SCHED0();
    }
    K4_H1(xb, xa, true, true, 6, cur, fr); K4_H2(xb, cur, true, 6, fr); K4_ADV();
    int t = 4;
#pragma unroll 1
    for (; t + 4 < NT; t += 2) {
        WAITBAR(2 * NP); K4_H1(xa, xb, true, true, t + 3, cur, fr); K4_H2(xa, cur, true, t + 3, fr); K4_ADV();
        WAITBAR(2 * NP); K4_H1(xb, xa, true, true, t + 4, cur, fr); K4_H2(xb, cur, true, t + 4, fr); K4_ADV();
    }
    WAITBAR(2 * NP); K4_H1(xa, xb, true, true, t + 3, cur, fr); K4_H2(xa, cur, true, t + 3, fr); K4_ADV();
    WAITBAR(2 * NP); K4_H1(xb, xa, true, false, 0, cur, fr); K4_H2(xb, cur, false, 0, fr); K4_ADV();
    WAITBAR(NP);     K4_H1(xa, xb, true, false, 0, cur, fr); K4_H2(xa, cur, false, 0, fr); K4_ADV();
    WAITBAR(0);      K4_H1(xb, xa, true, false, 0, cur, fr); K4_H2(xb, cur, false, 0, fr);
#pragma unroll
    for (int i = 0; i < 4 * NJ; ++i) K4_MM(i, au, xb, wuf);
    const LAS f32x2* st = (const LAS f32x2*)(lds + K4_STAT);
    const LAS float* cs = (const LAS float*)(lds + K4_CS);
    f32x2 ms[4];
#pragma unroll
    for (int m = 0; m < 4; ++m) ms[m] = st[wm * 64 + m * 16 + r];
    const srd_t sra = mk_srd(ws + WS_ACT, SEQ * FF * 2);
    const unsigned ap0 = FRAG(rt * 16 + wm * 4, 0, 112) + lane * 16;
    const int ft0 = ct * 7 + J0;
    constexpr int LONE = (NJ == 3) ? (J0 == 4 ? 2 : 0) : -1;
    u32x2 keep[4];
#pragma unroll
    for (int j = 0; j < NJ; ++j) {
        const f32x4 cg = *(const LAS f32x4*)(cs + (J0 + j) * 16 + 4 * g), cu = *(const LAS f32x4*)(cs + 112 + (J0 + j) * 16 + 4 * g);
        const bool second = (LONE == 0) ? (j == 2) : ((j & 1) == 1);
#pragma unroll
        for (int m = 0; m < 4; ++m) {
            const float c1 = -LOG2E * ms[m].y;
            const f32x4 gz = (ag[m][j] - cg * ms[m].x) * c1;
            const f32x4 uu = (au[m][j] - cu * ms[m].x) * ms[m].y;
            float a[4];
#pragma unroll
            for (int e = 0; e < 4; ++e) a[e] = uu[e] * __builtin_amdgcn_rcpf(1.0f + __builtin_amdgcn_exp2f(gz[e]));
            u32x2 ov; ov.x = pk_bf16(a[0], a[1]); ov.y = pk_bf16(a[2], a[3]);
            const int ft = ft0 + j;
            if (j == LONE) st8(sra, ap0 + FRAG(m, ft >> 1, 112) + (ft & 1) * 8, ov);
            else if (!second) keep[m] = ov;
            else st16(sra, ap0 + FRAG(m, ft >> 1, 112), (u32x4){keep[m].x, keep[m].y, ov.x, ov.y});
        }
        SCHED0();
    }
}

template <bool SYNC>
__device__ __forceinline__ void phase4(LAS char* lds, const Params& P, unsigned tag) {
    const unsigned lds0 = (unsigned)(uintptr_t)lds;
    const int tid = threadIdx.x, lane = tid & 63, w = __builtin_amdgcn_readfirstlane(tid >> 6);
    const int b = blockIdx.x, xcd = b & 7, loc = b >> 3;
    const int rt = (xcd >> 2) * 4 + (loc & 3), ct = (xcd & 3) * 8 + (loc >> 2);
    char* ws = P.ws;
    K4Dma D;
    D.srx = mk_srd(ws + WS_H, SEQ * DM * 2); D.srg = mk_srd(P.wg, DM * FF * 2); D.sru = mk_srd(P.wu, DM * FF * 2);
    D.xv = lane * 16; D.xs0 = FRAG(rt * 16 + w * 2, 0, 32);
    { const int ci = (w < 7 ? w : 6) * 64 + lane; const int row = ci / 14, c = ci % 14; D.gv = (unsigned)row * (FF * 2) + c * 16; }
    D.gs0 = (unsigned)ct * 224u; D.ld0 = lds0 + w * 2048; D.ld1 = lds0 + 16384 + w * 1024;
    if (w < 7) {
#pragma unroll
        for (int t = 0; t < 3; ++t) { K4_DMA(2, t, t * K4_SLOT); K4_DMA(3, t, t * K4_SLOT); }
    }
    if (SYNC) {
        if (w == 7) {
            int idx = -1;
            if (lane < 32) idx = FL_3 + rt * 32 + lane;
            else if (lane < 48) { const int k = lane - 32, gsel = k & 3, rq = k >> 2;
                const int c0 = (ct * 112 + ((gsel & 1) ? 111 : 0)) >> 7, bb = rq * 56 + (gsel >> 1) * 28 + c0;
                idx = FL_2 + (bb >> 5) * 32 + (bb & 7) * 4 + 2 * ((bb >> 3) & 1) + ((bb >> 4) & 1); }
            flags_wait(ws, idx, tag);
        }
        LBAR();
    }
#pragma unroll
    for (int t = 0; t < 3; ++t) { K4_DMA(0, t, t * K4_SLOT); K4_DMA(1, t, t * K4_SLOT); }
    {
        const srd_t srs = mk_srd(ws + WS_ST, SEQ * 128);
#pragma unroll
        for (int i = 0; i < 4; ++i) dma16c(lds0 + K4_STRAW + w * 4096 + i * 1024, lane * 16, srs, (unsigned)(rt * 256 + w * 32) * 128u + i * 1024);
        if (w == 7) {
            const srd_t src = mk_srd(ws + WS_CS, 4 * 7168 * 4);
            const unsigned cv = lane < 28 ? (unsigned)(ct * 112 + 4 * lane) * 4u : lane < 56 ? (unsigned)(3584 + ct * 112 + 4 * (lane - 28)) * 4u : 0x7ff00000u;
#pragma unroll
            for (int i = 0; i < 4; ++i) dma16c(lds0 + K4_CSRAW + i * 1024, cv, src, i * 7168 * 4);
        }
    }
    if ((ct & 1) == 0) {
        if (w < 4) k4_body<4, 0, true>(lds, D, ws, w, lane, rt, ct);
        else if (w < 7) k4_body<3, 4, true>(lds, D, ws, w, lane, rt, ct);
        else k4_body<3, 4, false>(lds, D, ws, w, lane, rt, ct);
    } else {
        if (w < 4) k4_body<3, 0, true>(lds, D, ws, w, lane, rt, ct);
        else if (w < 7) k4_body<4, 3, true>(lds, D, ws, w, lane, rt, ct);
        else k4_body<4, 3, false>(lds, D, ws, w, lane, rt, ct);
    }
    WAITBAR(0);
    if (SYNC && tid == 0) flag_set(ws, FL_4 + rt * 32 + ct, tag);
    if (w < 6) line_touch(lds, FRAG(rt * 16, (ct * 7 + 1) / 2 + (w >> 1), 112) + (w & 1) * 8192 + lane * 128, mk_srd(ws + WS_ACT, SEQ * FF * 2));
    CODE_TOUCH_ASYNC(".Lcw_p4", ".Lcw_p5");
}

constexpr int LDS_BYTES = 160 * 1024;
__global__ __launch_bounds__(512, 2) void mega(Params P) {
    extern __shared__ __attribute__((aligned(16))) char smem_g[];
    LAS char* lds = (LAS char*)smem_g;
    char* ws = P.ws;
    const unsigned tag = __hip_atomic_load((gu32*)(ws + WS_FLG) + FL_EPOCH, RLX_AGENT) + 1u;
    asm volatile(".Lcw_p0:");
    phase1<true>(lds, P, tag);
    asm volatile(".Lcw_p1:");
    phase2<true>(lds, P, tag);
    asm volatile(".Lcw_p2:");
    phase35<0, true>(lds, P, tag);
    asm volatile(".Lcw_p3:");
    phase4<true>(lds, P, tag);
    asm volatile(".Lcw_p4:");
    phase5<true>(lds, P, tag);
    asm volatile(".Lcw_p5:");
    if (blockIdx.x == 0 && threadIdx.x == 0) __hip_atomic_store((gu32*)(ws + WS_FLG) + FL_EPOCH, tag, RLX_AGENT);
}

extern "C" void kernel_launch(void* const* d_in, const int* in_sizes, int n_in, void* d_out, int out_size, void* d_ws, size_t ws_size, hipStream_t stream) {
    Params P;
    P.x = (const u16*)d_in[0]; P.wqkv = (const u16*)d_in[1]; P.wo = (const u16*)d_in[2];
    P.wg = (const u16*)d_in[4]; P.wu = (const u16*)d_in[5]; P.wd = (const u16*)d_in[6];
    P.out = (u16*)d_out;
    P.ws = (char*)d_ws;
    (void)hipFuncSetAttribute((const void*)mega, hipFuncAttributeMaxDynamicSharedMemorySize, LDS_BYTES);
    mega<<<256, 512, LDS_BYTES, stream>>>(P);
}
```
